```python
import torch

def _softmax(x, axis=-1):
    return torch.softmax(x, dim=axis)
def _gelu(x):
    return torch.nn.functional.gelu(x, approximate="tanh")
def _silu(x):
    return torch.nn.functional.silu(x)
def _sigmoid(x):
    return torch.sigmoid(x)
def _layernorm(x, axis=-1, eps=1e-5):
    m = x.mean(dim=axis, keepdim=True)
    v = x.var(dim=axis, keepdim=True, unbiased=False)
    return (x - m) / torch.sqrt(v + eps)
def _scatter_add(tgt, idx, src, axis=0):
    return tgt.clone().index_add_(axis, idx, src)
def _segment_sum(src, seg, n):
    out = torch.zeros((n,) + src.shape[1:], dtype=src.dtype)
    return out.index_add_(0, seg, src)
def _pad(x, pw):
    flat = tuple(v for pair in reversed(pw) for v in pair)
    return torch.nn.functional.pad(x, flat)

kernel_name = 'recurrent_scan__softmax_full_mha__mixer_channel_mlp__prompt'

def setup_inputs():

    _w = (lambda a: (lambda t: t.to(torch.bfloat16) if t.is_floating_point() else t)(torch.as_tensor(a)))
    def _A_setup():
        B, S, D = 1, 2048, 640
        return {
            "x": _w((lambda *s: torch.randn(*s, dtype=torch.bfloat16) * 0.05)(B, S, D)),
            "wqkv": _w((lambda *s: torch.randn(*s, dtype=torch.bfloat16) * 0.05)(D, 3 * D)),
            "wo": _w((lambda *s: torch.randn(*s, dtype=torch.bfloat16) * 0.05)(D, D)),
        }
    _A_in = _A_setup()

    def _B_setup():
        B, S, D, F = 1, 2048, 640, 1536
        return {
            "x": _w((lambda *s: torch.randn(*s, dtype=torch.bfloat16) * 0.05)(B, S, D)),
            "w1": _w((lambda *s: torch.randn(*s, dtype=torch.bfloat16) * 1.6)(D, F) * (D ** -0.5)),
            "b1": _w((lambda *s: torch.randn(*s, dtype=torch.bfloat16) * 0.8)(F)),
            "w2": _w((lambda *s: torch.randn(*s, dtype=torch.bfloat16) * 0.05)(F, D) * (F ** -0.5)),
            "b2": _w((lambda *s: torch.randn(*s, dtype=torch.bfloat16) * 0.05)(D)),
        }
    _B_in = _B_setup()

    return {
        "A_x": _A_in["x"],
        "A_wqkv": _A_in["wqkv"],
        "A_wo": _A_in["wo"],
        "B_x": _B_in["x"],
        "B_w1": _B_in["w1"],
        "B_b1": _B_in["b1"],
        "B_w2": _B_in["w2"],
        "B_b2": _B_in["b2"],
    }

def reference(A_x, A_wqkv, A_wo, B_x, B_w1, B_b1, B_w2, B_b2):
    def _A_ref(x, wqkv, wo):
        B, S, D = x.shape
        H = 32; Dh = D // H
        qkv = (lambda x, *d: x.permute(*d))((x @ wqkv).reshape(B, S, 3, H, Dh), 2, 0, 3, 1, 4)
        q, k, v = qkv[0], qkv[1], qkv[2]
        scale = Dh ** -0.5
        scores = torch.einsum("bhqd,bhkd->bhqk", q, k) * scale
        p = _softmax(scores, axis=-1)
        o = torch.einsum("bhqk,bhkd->bhqd", p, v)
        return (lambda x, *d: x.permute(*d))(o, 0, 2, 1, 3).reshape(B, S, D) @ wo

    def _B_ref(x, w1, b1, w2, b2):
        y = _layernorm(x, axis=-1)
        h = _gelu(torch.einsum("bsd,df->bsf", y, w1) + b1)
        return torch.einsum("bsf,fd->bsd", h, w2) + b2

    x = A_x
    xn = _layernorm(x, axis=-1)
    s = _A_ref(xn, A_wqkv, A_wo)
    g = _silu(_B_ref(xn, B_w1, B_b1, B_w2, B_b2))
    return x + s * g
```

```cpp
#include <hip/hip_runtime.h>
#include <hip/hip_bf16.h>
#include <stdint.h>

typedef __attribute__((ext_vector_type(8))) short bf16x8;
typedef __attribute__((ext_vector_type(4))) short bf16x4;
typedef __attribute__((ext_vector_type(16))) float f32x16;
typedef __attribute__((ext_vector_type(4))) float f32x4;
typedef __attribute__((ext_vector_type(2))) float f32x2;
typedef __attribute__((ext_vector_type(4))) unsigned u32x4;
typedef __attribute__((ext_vector_type(2))) unsigned u32x2;
typedef __attribute__((ext_vector_type(2))) __bf16 bf2;
typedef __attribute__((address_space(3))) char* lds_ptr;
#define LDS_AS __attribute__((address_space(3)))
#define DI __device__ __forceinline__

#define MFMA32(a, b, c) __builtin_amdgcn_mfma_f32_32x32x16_bf16((a), (b), (c), 0, 0, 0)
#define MFMA32K8(a, b, c) __builtin_amdgcn_mfma_f32_32x32x8bf16_1k((a), (b), (c), 0, 0, 0)
#define MFMA16(a, b, c) __builtin_amdgcn_mfma_f32_16x16x32_bf16((a), (b), (c), 0, 0, 0)
#define SBAR() __builtin_amdgcn_sched_barrier(0)
#define PIN(x) asm volatile("" : "+v"(x))
#define WAIT_BAR(N) asm volatile("s_waitcnt vmcnt(" #N ") lgkmcnt(0)\n\ts_barrier" ::: "memory")

constexpr int S_ = 2048, D_ = 640, NH = 32, F_ = 1536;
constexpr float LN_EPS = 1e-5f;
constexpr float QSCALE = 0.22360679774997896f * 1.4426950408889634f;

constexpr size_t WS_Q = 0;
constexpr size_t WS_K = WS_Q + (size_t)NH * 64 * 1280;
constexpr size_t WS_V = WS_K + (size_t)NH * 64 * 1280;
constexpr size_t WS_H = WS_V + (size_t)NH * 64 * 1280;
constexpr size_t WS_O = WS_H + (size_t)128 * 48 * 1024;
constexpr size_t WS_G = WS_O + (size_t)128 * 20 * 1024;
constexpr size_t WS_END = WS_G + (size_t)S_ * D_ * 2;

DI unsigned cvtpk(float lo, float hi) {
  f32x2 f = {lo, hi};
  return __builtin_bit_cast(unsigned, __builtin_convertvector(f, bf2));
}
DI float bf_lo(unsigned u) { return __uint_as_float(u << 16); }
DI float bf_hi(unsigned u) { return __uint_as_float(u & 0xffff0000u); }
DI float dot2(unsigned a, unsigned b, float c) {
  return __builtin_amdgcn_fdot2_f32_bf16(__builtin_bit_cast(bf2, a), __builtin_bit_cast(bf2, b), c, false);
}
DI float xhalf(float v) {
  return __shfl_xor(v, 32);
}
DI void glds16(unsigned voff, const void* sbase, unsigned ldsdst) {
  unsigned keep;
  asm volatile("s_mov_b32 %0, m0\n\ts_mov_b32 m0, %3\n\ts_nop 0\n\tglobal_load_lds_dwordx4 %1, %2\n\ts_mov_b32 m0, %0"
               : "=&s"(keep) : "v"(voff), "s"(sbase), "s"(ldsdst) : "memory");
}
DI void glds16_masked(unsigned voff, const void* sbase, unsigned ldsdst, unsigned mlo, unsigned mhi) {
  unsigned keep;
  asm volatile("s_mov_b32 %0, m0\n\ts_mov_b32 m0, %3\n\ts_mov_b32 exec_lo, %4\n\ts_mov_b32 exec_hi, %5\n\t"
               "global_load_lds_dwordx4 %1, %2\n\ts_mov_b64 exec, -1\n\ts_mov_b32 m0, %0"
               : "=&s"(keep) : "v"(voff), "s"(sbase), "s"(ldsdst), "s"(mlo), "s"(mhi) : "memory");
}
typedef short v4i16_t __attribute__((ext_vector_type(4)));
DI bf16x4 tr_read(lds_ptr p) {
  return __builtin_bit_cast(bf16x4, __builtin_amdgcn_ds_read_tr16_b64_v4i16((LDS_AS v4i16_t*)p));
}
DI bf16x8 cat8(bf16x4 a, bf16x4 b) { return __builtin_shufflevector(a, b, 0, 1, 2, 3, 4, 5, 6, 7); }

constexpr int K1_SLOT = 49152;
constexpr int K1_STAT = 3 * K1_SLOT;
constexpr int K1_LDS = K1_STAT + 512 + 512 + 1024 + 1024 + 2048;

template <int TYPE, int NTW>
DI void k1_body(lds_ptr lds, const char* __restrict__ x, const char* __restrict__ W, const int ldw, const int n0,
                const int rb, const unsigned short* __restrict__ b1, char* __restrict__ ws) {
  const int tid = threadIdx.x, lane = tid & 63;
  const int wid = __builtin_amdgcn_readfirstlane(tid >> 6);
  const int mi = wid & 3, nh = wid >> 2;
  const int r = lane & 31, h = lane >> 5;
  const unsigned lds0 = (unsigned)(uintptr_t)lds;

  const unsigned vx0 = (unsigned)((lane >> 3) * 1280 + (((lane & 7) ^ (lane >> 4)) << 4));
  const unsigned vx1 = (unsigned)((8 + (lane >> 3)) * 1280 + (((lane & 7) ^ (4 + (lane >> 4))) << 4));
  const unsigned vw = (unsigned)((lane >> 3) * ldw * 2 + ((((((lane >> 2) ^ (lane >> 4)) & 1) << 2) | (lane & 3)) << 4));
  const char* xs = x + (size_t)(rb * 128 + 16 * wid) * 1280;
  const char* wsrc = W + ((size_t)(8 * wid) * ldw + n0) * 2;
#define K1_ISSUE(c, slot)                                                                         \
  do {                                                                                            \
    const unsigned sl_ = lds0 + (unsigned)(slot);                                                 \
    glds16(vx0, xs + (c) * 128, sl_ + (unsigned)(2 * wid) * 1024u);                               \
    glds16(vx1, xs + (c) * 128, sl_ + (unsigned)(2 * wid + 1) * 1024u);                           \
    _Pragma("unroll") for (int P_ = 0; P_ < NTW; ++P_)                                            \
        glds16(vw, wsrc + ((size_t)(c) * 64 * ldw + P_ * 64) * 2, sl_ + 16384u + P_ * 8192u + (unsigned)wid * 1024u); \
  } while (0)

  unsigned short bias_raw = 0;
  if (TYPE == 2) {
    if (tid < 64 * NTW) bias_raw = b1[n0 + tid];
  }
  K1_ISSUE(0, 0);
  K1_ISSUE(1, K1_SLOT);
  K1_ISSUE(2, 2 * K1_SLOT);

  LDS_AS float* st_sum = (LDS_AS float*)(lds + K1_STAT);
  LDS_AS float* st_sq = st_sum + 128;
  LDS_AS float* st_cs = st_sum + 256;
  LDS_AS float* st_b = st_sum + 512;
  LDS_AS f32x2* st_w = (LDS_AS f32x2*)(st_sum + 768) + wid * 32;

  unsigned xa[4];
#pragma unroll
  for (int ks = 0; ks < 4; ++ks) xa[ks] = (unsigned)((mi * 32 + r) * 128 + (((2 * ks + h) ^ ((r >> 1) & 7)) << 4));
  unsigned wa[NTW];
  int tglob[NTW];
  {
    const int q = (lane & 15) >> 2, p = lane & 3, gp = (lane >> 4) & 1;
#pragma unroll
    for (int j = 0; j < NTW; ++j) {
      int jj = j + mi;
      jj = jj >= NTW ? jj - NTW : jj;
      jj = jj >= NTW ? jj - NTW : jj;
      const int T = nh * NTW + jj;
      tglob[j] = T;
      wa[j] = (unsigned)(16384 + (T >> 1) * 8192 + (8 * h + q) * 128 + 64 * ((T & 1) ^ ((q >> 1) & 1)) + (16 * gp + 4 * p) * 2);
    }
  }
  f32x16 acc[NTW];
#pragma unroll
  for (int j = 0; j < NTW; ++j)
#pragma unroll
    for (int i = 0; i < 16; ++i) acc[j][i] = 0.f;
  float s_a0 = 0.f, s_a1 = 0.f, s_b0 = 0.f, s_b1 = 0.f, c_0 = 0.f, c_1 = 0.f;
  const unsigned ONES = 0x3f803f80u;

  int slot = 0;
#pragma unroll 1
  for (int c = 0; c < 10; ++c) {
    if (c == 0) {
      if (NTW == 4) WAIT_BAR(12); else WAIT_BAR(10);
    } else if (c < 9) {
      if (NTW == 4) WAIT_BAR(6); else WAIT_BAR(5);
    } else {
      WAIT_BAR(0);
    }
    if (c >= 1 && c + 2 < 10) {
      const int ps = slot == 0 ? 2 * K1_SLOT : slot - K1_SLOT;
      K1_ISSUE(c + 2, ps);
    }
    lds_ptr sp = lds + slot;
#pragma unroll
    for (int ks = 0; ks < 4; ++ks) {
      const bf16x8 xf = *(const LDS_AS bf16x8*)(sp + xa[ks]);
      bf16x8 wf[NTW];
#pragma unroll
      for (int j = 0; j < NTW; ++j) wf[j] = cat8(tr_read(sp + wa[j] + ks * 2048), tr_read(sp + wa[j] + ks * 2048 + 512));
#pragma unroll
      for (int j = 0; j < NTW; ++j) {
        if (TYPE == 1) acc[j] = MFMA32(xf, wf[j], acc[j]);
        else acc[j] = MFMA32(wf[j], xf, acc[j]);
      }
      const u32x4 xu = __builtin_bit_cast(u32x4, xf), wu = __builtin_bit_cast(u32x4, wf[0]);
      c_0 = dot2(wu[0], ONES, c_0); c_1 = dot2(wu[1], ONES, c_1);
      c_0 = dot2(wu[2], ONES, c_0); c_1 = dot2(wu[3], ONES, c_1);
      if (TYPE == 1) {
        s_a0 = dot2(xu[0], ONES, s_a0); s_a1 = dot2(xu[1], ONES, s_a1); s_a0 = dot2(xu[2], ONES, s_a0); s_a1 = dot2(xu[3], ONES, s_a1);
        s_b0 = dot2(xu[0], xu[0], s_b0); s_b1 = dot2(xu[1], xu[1], s_b1); s_b0 = dot2(xu[2], xu[2], s_b0); s_b1 = dot2(xu[3], xu[3], s_b1);
      } else {
        const unsigned m0 = nh ? xu[0] : ONES, m1 = nh ? xu[1] : ONES, m2 = nh ? xu[2] : ONES, m3 = nh ? xu[3] : ONES;
        s_a0 = dot2(xu[0], m0, s_a0); s_a1 = dot2(xu[1], m1, s_a1); s_a0 = dot2(xu[2], m2, s_a0); s_a1 = dot2(xu[3], m3, s_a1);
      }
    }
    slot = slot == 2 * K1_SLOT ? 0 : slot + K1_SLOT;
  }
#undef K1_ISSUE

  {
    if (TYPE == 2) {
      if (tid < 64 * NTW) st_b[tid] = __uint_as_float((unsigned)bias_raw << 16);
    }
    float cs = c_0 + c_1;
    cs += xhalf(cs);
    if (h == 0) st_cs[tglob[0] * 32 + r] = cs;
    float sa = s_a0 + s_a1;
    sa += xhalf(sa);
    if (TYPE == 1) {
      float sb = s_b0 + s_b1;
      sb += xhalf(sb);
      const float mu = sa * (1.0f / D_);
      const float var = fmaxf(sb * (1.0f / D_) - mu * mu, 0.f);
      const f32x2 v = {mu, __builtin_amdgcn_rsqf(var + LN_EPS)};
      if (h == 0) st_w[r] = v;
    } else {
      if (h == 0) (nh ? st_sq : st_sum)[mi * 32 + r] = sa;
    }
  }
  asm volatile("s_waitcnt lgkmcnt(0)\n\ts_barrier" ::: "memory");

  const int tq = rb * 4 + mi;
  if (TYPE == 1) {
    float mu[16], rs[16];
#pragma unroll
    for (int g = 0; g < 4; ++g)
#pragma unroll
      for (int e = 0; e < 4; ++e) {
        const f32x2 v = st_w[8 * g + 4 * h + e];
        mu[4 * g + e] = v[0]; rs[4 * g + e] = v[1];
      }
#pragma unroll
    for (int j = 0; j < NTW; ++j) {
      const int nl = tglob[j] * 32 + r;
      const float cs = st_cs[nl];
      const int nn = n0 - 1280 + nl;
      const int head = (nn * 3277) >> 16;
      const int d = nn - head * 20;
      char* dst = ws + WS_V + (size_t)(head * 64 + tq) * 1280 + (h * 20 + d) * 16;
#pragma unroll
      for (int s = 0; s < 2; ++s) {
        u32x4 o;
#pragma unroll
        for (int e = 0; e < 4; ++e) {
          const int i = 8 * s + 2 * e;
          o[e] = cvtpk((acc[j][i] - mu[i] * cs) * rs[i], (acc[j][i + 1] - mu[i + 1] * cs) * rs[i + 1]);
        }
        *(u32x4*)(dst + s * 640) = o;
      }
    }
  } else {
    const float mu = st_sum[mi * 32 + r] * (1.0f / D_);
    const float var = fmaxf(st_sq[mi * 32 + r] * (1.0f / D_) - mu * mu, 0.f);
    const float rs = __builtin_amdgcn_rsqf(var + LN_EPS);
    if (TYPE == 0) {
#pragma unroll
      for (int j = 0; j < NTW; ++j) {
        const int nb = n0 + tglob[j] * 32;
        const bool isq = nb < 640;
        const float sc = isq ? rs * QSCALE : rs;
        char* base = ws + (isq ? WS_Q : WS_K) + (size_t)tq * 1280;
        const int g0 = ((isq ? nb : nb - 640) >> 2) + h;
#pragma unroll
        for (int g = 0; g < 4; ++g) {
          const f32x4 cs = *(const LDS_AS f32x4*)(st_cs + tglob[j] * 32 + 8 * g + 4 * h);
          u32x2 o;
          o[0] = cvtpk((acc[j][4 * g] - mu * cs[0]) * sc, (acc[j][4 * g + 1] - mu * cs[1]) * sc);
          o[1] = cvtpk((acc[j][4 * g + 2] - mu * cs[2]) * sc, (acc[j][4 * g + 3] - mu * cs[3]) * sc);
          const int G = g0 + 2 * g;
          const int head = (G * 205) >> 10;
          const int dg = G - head * 5;
          const int off = dg < 4 ? (dg >> 1) * 512 + r * 16 + (dg & 1) * 8 : 1024 + r * 8;
          *(u32x2*)(base + (size_t)head * (64 * 1280) + off) = o;
        }
      }
    } else {
      const float sc = rs * __builtin_amdgcn_rsqf(var * rs * rs + LN_EPS);
      const int tok = rb * 128 + mi * 32 + r;
      constexpr float GK1 = -2.0f * 1.4426950408889634f * 0.7978845608028654f, GK2 = GK1 * 0.044715f;
#pragma unroll
      for (int j = 0; j < NTW; ++j) {
        const int kstep = (n0 >> 5) + tglob[j];
        char* dst = ws + WS_H + ((size_t)(tok >> 4) * 48 + kstep) * 1024 + (h * 16 + (tok & 15)) * 16;
#pragma unroll
        for (int u = 0; u < 2; ++u) {
          float v[8];
#pragma unroll
          for (int gg = 0; gg < 2; ++gg) {
            const int g = 2 * u + gg;
            const f32x4 cs = *(const LDS_AS f32x4*)(st_cs + tglob[j] * 32 + 8 * g + 4 * h);
            const f32x4 bb = *(const LDS_AS f32x4*)(st_b + tglob[j] * 32 + 8 * g + 4 * h);
#pragma unroll
            for (int e = 0; e < 4; ++e) {
              const float t = (acc[j][4 * g + e] - mu * cs[e]) * sc + bb[e];
              const float ex = __builtin_amdgcn_exp2f(t * (GK1 + GK2 * t * t));
              v[4 * gg + e] = t * __builtin_amdgcn_rcpf(1.0f + ex);
            }
          }
          u32x4 o;
#pragma unroll
          for (int e = 0; e < 4; ++e) o[e] = cvtpk(v[2 * e], v[2 * e + 1]);
          *(u32x4*)(dst + u * 512) = o;
        }
      }
    }
  }
}

__global__ __launch_bounds__(512, 2) void k1_proj(const char* __restrict__ x, const char* __restrict__ wqkv,
                                                  const char* __restrict__ w1, const unsigned short* __restrict__ b1,
                                                  char* __restrict__ ws) {
  extern __shared__ __attribute__((aligned(16))) char smem[];
  lds_ptr lds = (lds_ptr)smem;
  const int bid = blockIdx.x, xg = bid & 7, idx = bid >> 3;
  const int rb = (xg & 1) * 8 + (idx & 7);
  const int cb = (xg >> 1) * 4 + (idx >> 3);
  if (cb < 5) k1_body<0, 4>(lds, x, wqkv, 1920, cb * 256, rb, b1, ws);
  else if (cb == 5) k1_body<1, 4>(lds, x, wqkv, 1920, 1280, rb, b1, ws);
  else if (cb < 8) k1_body<1, 3>(lds, x, wqkv, 1920, 1536 + (cb - 6) * 192, rb, b1, ws);
  else k1_body<2, 3>(lds, x, w1, 1536, (cb - 8) * 192, rb, b1, ws);
}

constexpr int G80_SLOT = 20480;

DI void g80_issue(const char* wsrc, unsigned v0, unsigned v1, unsigned v2, unsigned dst) {
  glds16(v0, wsrc, dst);
  glds16(v1, wsrc, dst + 1024u);
  glds16_masked(v2, wsrc, dst + 2048u, 0xffffffffu, 0u);
}
DI unsigned g80_voff(int c16) {
  const int row = (c16 * 6554) >> 16;
  return (unsigned)(row * 1280 + (c16 - row * 10) * 16);
}
DI void g80_compute(lds_ptr sp, unsigned wa, const bf16x8 a0, const bf16x8 a1, f32x4 (&acc)[5]) {
#pragma unroll
  for (int j = 0; j < 5; ++j) {
    const bf16x8 wf = cat8(tr_read(sp + wa + j * 32), tr_read(sp + wa + j * 32 + 1280));
    acc[j] = MFMA16(wf, a0, acc[j]);
  }
#pragma unroll
  for (int j = 0; j < 5; ++j) {
    const bf16x8 wf = cat8(tr_read(sp + wa + 5120 + j * 32), tr_read(sp + wa + 5120 + j * 32 + 1280));
    acc[j] = MFMA16(wf, a1, acc[j]);
  }
}

DI void mlp2_body(lds_ptr lds, const int blk, const char* __restrict__ w2, const unsigned short* __restrict__ b2,
                  char* __restrict__ ws) {
  const int tid = threadIdx.x, lane = tid & 63;
  const int wid = __builtin_amdgcn_readfirstlane(tid >> 6);
  const int mi = wid & 3, kh = wid >> 2;
  const int xg = blk & 7, idx = blk >> 3;
  const int rbk = xg * 4 + (idx & 3), cbk = idx >> 2;
  const unsigned lds0 = (unsigned)(uintptr_t)lds;
  __builtin_amdgcn_s_setprio(2);
  const unsigned v0 = g80_voff(wid * 160 + lane), v1 = g80_voff(wid * 160 + 64 + lane), v2 = g80_voff(wid * 160 + 128 + lane);
  const char* wsrc = w2 + cbk * 160;
  const unsigned dst = lds0 + (unsigned)wid * 2560u;
  const char* asrc = ws + WS_H + ((size_t)(rbk * 4 + mi) * 48 + 2 * kh) * 1024 + lane * 16;
  const int gq = lane >> 4;
  const unsigned wa = (unsigned)((kh * 64 + 4 * ((gq & 1) + 4 * (gq >> 1)) + ((lane & 15) >> 2)) * 160 + (lane & 3) * 8);
  f32x4 acc[5];
#pragma unroll
  for (int j = 0; j < 5; ++j) acc[j] = f32x4{0.f, 0.f, 0.f, 0.f};
  g80_issue(wsrc, v0, v1, v2, dst);
  bf16x8 a0 = *(const bf16x8*)(asrc), a1 = *(const bf16x8*)(asrc + 1024);
  int slot = 0;
#pragma unroll 1
  for (int c = 0; c < 12; ++c) {
    PIN(a0); PIN(a1);
    WAIT_BAR(0);
    bf16x8 n0 = a0, n1 = a1;
    if (c + 1 < 12) {
      g80_issue(wsrc + (size_t)(c + 1) * 128 * 1280, v0, v1, v2, dst + (unsigned)(slot ^ G80_SLOT));
      n0 = *(const bf16x8*)(asrc + (c + 1) * 4096);
      n1 = *(const bf16x8*)(asrc + (c + 1) * 4096 + 1024);
    }
    g80_compute(lds + slot, wa, a0, a1, acc);
    a0 = n0; a1 = n1;
    slot ^= G80_SLOT;
  }
  asm volatile("s_waitcnt lgkmcnt(0)\n\ts_barrier" ::: "memory");
  LDS_AS f32x4* red = (LDS_AS f32x4*)lds + (mi * 5) * 64 + lane;
  if (kh == 1) {
#pragma unroll
    for (int j = 0; j < 5; ++j) red[j * 64] = acc[j];
  }
  asm volatile("s_waitcnt lgkmcnt(0)\n\ts_barrier" ::: "memory");
  if (kh == 0) {
    char* gd = ws + WS_G + ((size_t)((rbk * 8 + cbk) * 4 + mi) * 5) * 512 + lane * 8;
#pragma unroll
    for (int j = 0; j < 5; ++j) {
      const f32x4 o = red[j * 64];
      const u32x2 bb = *(const u32x2*)(b2 + cbk * 80 + j * 16 + 4 * gq);
      float t[4] = {acc[j][0] + o[0] + bf_lo(bb[0]), acc[j][1] + o[1] + bf_hi(bb[0]), acc[j][2] + o[2] + bf_lo(bb[1]),
                    acc[j][3] + o[3] + bf_hi(bb[1])};
#pragma unroll
      for (int e = 0; e < 4; ++e)
        t[e] = t[e] * __builtin_amdgcn_rcpf(1.0f + __builtin_amdgcn_exp2f(t[e] * -1.4426950408889634f));
      u32x2 g = {cvtpk(t[0], t[1]), cvtpk(t[2], t[3])};
      *(u32x2*)(gd + j * 512) = g;
    }
  }
}

constexpr int AT_TILE = 3584;
constexpr int AT_SLOT = 32768;
constexpr int AT_RED = 28672;
constexpr int K2_LDS = 2 * AT_SLOT;

DI void attn_store(char* ws, const f32x16& o, const float inv, const int head, const int tok, const int h) {
  char* base = ws + WS_O + (size_t)(tok >> 4) * (20 * 1024) + (tok & 15) * 16;
#pragma unroll
  for (int g = 0; g < 3; ++g) {
    if (g < 2 || h == 0) {
      const int Gc = head * 5 + 2 * g + h;
      const int G8 = Gc & 7;
      const int off = (Gc >> 3) * 1024 + (((G8 >> 2) * 2 + (G8 & 1)) * 256) + ((G8 >> 1) & 1) * 8;
      u32x2 v = {cvtpk(o[4 * g] * inv, o[4 * g + 1] * inv), cvtpk(o[4 * g + 2] * inv, o[4 * g + 3] * inv)};
      *(u32x2*)(base + off) = v;
    }
  }
}

DI void attn_body(lds_ptr lds, const int blk, char* __restrict__ ws) {
  const int tid = threadIdx.x, lane = tid & 63;
  const int wid = __builtin_amdgcn_readfirstlane(tid >> 6);
  const int r = lane & 31, h = lane >> 5;
  const int xg = blk & 7, idx = blk >> 3;
  const int head = xg * 4 + (idx >> 3), qb = idx & 7;
  const unsigned lds0 = (unsigned)(uintptr_t)lds;
  const char* Kh = ws + WS_K + (size_t)head * (64 * 1280);
  const char* Vh = ws + WS_V + (size_t)head * (64 * 1280);
  const char* Qt = ws + WS_Q + (size_t)(head * 64 + qb * 8 + wid) * 1280;

  const unsigned vlin = (unsigned)lane * 16u;
  const unsigned vv = (unsigned)(h * 20 + r) * 16u;
#define AT_ISSUE(c, slot)                                                                    \
  do {                                                                                       \
    const int it_ = (c) * 8 + wid;                                                           \
    const char* ks_ = Kh + (size_t)((it_ + 1) & 63) * 1280;                                  \
    const char* vs_ = Vh + (size_t)it_ * 1280;                                               \
    const unsigned d_ = lds0 + (unsigned)(slot) + (unsigned)wid * AT_TILE;                   \
    glds16(vlin, ks_, d_);                                                                   \
    glds16_masked(vlin, ks_ + 1024, d_ + 1024u, 0xffffu, 0u);                                \
    glds16_masked(vv, vs_, d_ + 1536u, 0xfffffu, 0xfffffu);                                  \
    glds16_masked(vv, vs_ + 640, d_ + 2560u, 0xfffffu, 0xfffffu);                            \
  } while (0)
  AT_ISSUE(0, 0);
  AT_ISSUE(1, AT_SLOT);

  bf16x8 qa = *(const bf16x8*)(Qt + lane * 16);
  u32x2 qbv = *(const u32x2*)(Qt + 1024 + r * 8);
  bf16x8 ka0 = *(const bf16x8*)(Kh + lane * 16);
  u32x2 kb0 = *(const u32x2*)(Kh + 1024 + r * 8);
  float kmax = 0.f;
#pragma unroll
  for (int i = 0; i < 4; ++i) {
    const int key = i * 512 + tid;
    const char* kp = Kh + (size_t)(key >> 5) * 1280;
    const u32x4 a = *(const u32x4*)(kp + (key & 31) * 16), b = *(const u32x4*)(kp + 512 + (key & 31) * 16);
    const u32x2 c = *(const u32x2*)(kp + 1024 + (key & 31) * 8);
    float n0 = dot2(a[0], a[0], 0.f), n1 = dot2(a[1], a[1], 0.f);
    n0 = dot2(a[2], a[2], n0); n1 = dot2(a[3], a[3], n1);
    n0 = dot2(b[0], b[0], n0); n1 = dot2(b[1], b[1], n1);
    n0 = dot2(b[2], b[2], n0); n1 = dot2(b[3], b[3], n1);
    n0 = dot2(c[0], c[0], n0); n1 = dot2(c[1], c[1], n1);
    const float n = n0 + n1;
    kmax = (n > kmax || n != n) ? n : kmax;
  }
#pragma unroll
  for (int o = 32; o >= 1; o >>= 1) {
    const float t = __shfl_xor(kmax, o);
    kmax = (t > kmax || t != t) ? t : kmax;
  }
  LDS_AS float* red = (LDS_AS float*)(lds + AT_RED);
  if (lane == 0) red[wid] = kmax;
#pragma unroll
  for (int sl = 0; sl < 2; ++sl) {
    lds_ptr tp = lds + sl * AT_SLOT + wid * AT_TILE;
    if (lane < 32) *(LDS_AS u32x2*)(tp + 1024 + 256 + lane * 8) = u32x2{0x00003f80u, 0u};
    if (lane < 48) {
      const int blk4 = lane / 12, dd = 20 + lane % 12;
      const unsigned val = dd == 20 ? 0x3f803f80u : 0u;
      *(LDS_AS u32x4*)(tp + 1536 + blk4 * 512 + dd * 16) = u32x4{val, val, val, val};
    }
  }
  float qn;
  {
    const u32x4 a = __builtin_bit_cast(u32x4, qa);
    float n0 = dot2(a[0], a[0], 0.f), n1 = dot2(a[1], a[1], 0.f);
    n0 = dot2(a[2], a[2], n0); n1 = dot2(a[3], a[3], n1);
    if (h == 0) { n0 = dot2(qbv[0], qbv[0], n0); n1 = dot2(qbv[1], qbv[1], n1); }
    qn = n0 + n1;
    qn += xhalf(qn);
  }
  WAIT_BAR(4);
  {
    float m = red[0];
#pragma unroll
    for (int i = 1; i < 8; ++i) { const float t = red[i]; m = (t > m || t != t) ? t : m; }
    const float bound = __builtin_amdgcn_sqrtf(qn * m) * 1.01f;
    if (h == 1) { qbv[0] = cvtpk(-bound, 0.f); qbv[1] = 0u; kb0[0] = 0x00003f80u; kb0[1] = 0u; }
  }
  const bf16x4 qb4 = __builtin_bit_cast(bf16x4, qbv);
  f32x16 o;
#pragma unroll
  for (int i = 0; i < 16; ++i) o[i] = 0.f;
  f32x16 zero = o;
  f32x16 sA = MFMA32(ka0, qa, zero);
  sA = MFMA32K8(__builtin_bit_cast(bf16x4, kb0), qb4, sA);
  f32x16 sB;

  unsigned a16 = vlin, a8 = (unsigned)lane * 8u;
  bf16x8 kaA, v0A, v1A, kaB, v0B, v1B;
  bf16x4 kbA, kbB;
#define AT_READ(X, j)                                                              \
  do {                                                                             \
    ka##X = *(const LDS_AS bf16x8*)(lds + a16 + (j) * AT_TILE);                    \
    kb##X = *(const LDS_AS bf16x4*)(lds + a8 + (j) * AT_TILE + 1024);              \
    v0##X = *(const LDS_AS bf16x8*)(lds + a16 + (j) * AT_TILE + 1536);             \
    v1##X = *(const LDS_AS bf16x8*)(lds + a16 + (j) * AT_TILE + 2560);             \
  } while (0)
#define EX4(S, i)                                                                  \
  do {                                                                             \
    S[i] = __builtin_amdgcn_exp2f(S[i]); S[i + 1] = __builtin_amdgcn_exp2f(S[i + 1]); \
    S[i + 2] = __builtin_amdgcn_exp2f(S[i + 2]); S[i + 3] = __builtin_amdgcn_exp2f(S[i + 3]); \
  } while (0)
#define AT_STEP(SC, SN, X)                                                         \
  do {                                                                             \
    u32x4 p0, p1;                                                                  \
    SN = MFMA32(ka##X, qa, zero); SBAR();                                          \
    EX4(SC, 0); p0[0] = cvtpk(SC[0], SC[1]); p0[1] = cvtpk(SC[2], SC[3]); PIN(p0); SBAR(); \
    SN = MFMA32K8(kb##X, qb4, SN); SBAR();                                         \
    EX4(SC, 4); p0[2] = cvtpk(SC[4], SC[5]); p0[3] = cvtpk(SC[6], SC[7]);          \
    EX4(SC, 8); PIN(p0); PIN(SC); SBAR();                                          \
    o = MFMA32(v0##X, __builtin_bit_cast(bf16x8, p0), o); SBAR();                  \
    p1[0] = cvtpk(SC[8], SC[9]); p1[1] = cvtpk(SC[10], SC[11]);                    \
    EX4(SC, 12); p1[2] = cvtpk(SC[12], SC[13]); p1[3] = cvtpk(SC[14], SC[15]); PIN(p1); SBAR(); \
    o = MFMA32(v1##X, __builtin_bit_cast(bf16x8, p1), o); SBAR();                  \
  } while (0)

  AT_READ(A, 0);
#pragma unroll 1
  for (int c = 0; c < 8; ++c) {
    AT_READ(B, 1); SBAR(); AT_STEP(sA, sB, A);
    AT_READ(A, 2); SBAR(); AT_STEP(sB, sA, B);
    AT_READ(B, 3); SBAR(); AT_STEP(sA, sB, A);
    AT_READ(A, 4); SBAR(); AT_STEP(sB, sA, B);
    AT_READ(B, 5); SBAR(); AT_STEP(sA, sB, A);
    AT_READ(A, 6); SBAR(); AT_STEP(sB, sA, B);
    AT_READ(B, 7); SBAR(); AT_STEP(sA, sB, A);
    if (c < 7) {
      WAIT_BAR(0);
      if (c < 6) AT_ISSUE(c + 2, (c & 1) * AT_SLOT);
      a16 ^= AT_SLOT; a8 ^= AT_SLOT;
      AT_READ(A, 0);
    }
    SBAR(); AT_STEP(sB, sA, B);
  }
#undef AT_ISSUE
#undef AT_READ
#undef AT_STEP

  const int tok = qb * 256 + wid * 32 + r;
  float l = o[8];
  l = __shfl(l, 32 + r);
  if (__builtin_expect(__any(!(l >= 8.6736174e-19f) || !(l < 3.0e38f)), 0)) {
    if (h == 1) { qbv[0] = 0u; }
    const bf16x4 qz = __builtin_bit_cast(bf16x4, qbv);
    float m = -3.0e38f;
#pragma unroll
    for (int i = 0; i < 16; ++i) o[i] = 0.f;
    const unsigned onev = r == 20 ? 0x3f803f80u : 0u;
#pragma unroll 1
    for (int t = 0; t < 64; ++t) {
      const bf16x8 ka = *(const bf16x8*)(Kh + t * 1280 + lane * 16);
      u32x2 kb = *(const u32x2*)(Kh + t * 1280 + 1024 + r * 8);
      u32x4 v0 = {onev, onev, onev, onev}, v1 = v0;
      if (r < 20) {
        v0 = *(const u32x4*)(Vh + t * 1280 + vv);
        v1 = *(const u32x4*)(Vh + t * 1280 + 640 + vv);
      }
      f32x16 s = MFMA32(ka, qa, zero);
      s = MFMA32K8(__builtin_bit_cast(bf16x4, kb), qz, s);
      float tm = s[0];
#pragma unroll
      for (int i = 1; i < 16; ++i) tm = fmaxf(tm, s[i]);
      tm = fmaxf(tm, xhalf(tm));
      const float mn = fmaxf(m, tm);
      const float al = __builtin_amdgcn_exp2f(m - mn);
      m = mn;
      u32x4 p0, p1;
#pragma unroll
      for (int i = 0; i < 16; ++i) { o[i] *= al; s[i] = __builtin_amdgcn_exp2f(s[i] - mn); }
#pragma unroll
      for (int e = 0; e < 4; ++e) { p0[e] = cvtpk(s[2 * e], s[2 * e + 1]); p1[e] = cvtpk(s[8 + 2 * e], s[9 + 2 * e]); }
      o = MFMA32(__builtin_bit_cast(bf16x8, v0), __builtin_bit_cast(bf16x8, p0), o);
      o = MFMA32(__builtin_bit_cast(bf16x8, v1), __builtin_bit_cast(bf16x8, p1), o);
    }
    l = __shfl(o[8], 32 + r);
  }
  attn_store(ws, o, 1.0f / l, head, tok, h);
#undef EX4
}

__global__ __launch_bounds__(512, 4) void k2_attn_mlp(const char* __restrict__ w2, const unsigned short* __restrict__ b2,
                                                      char* __restrict__ ws) {
  extern __shared__ __attribute__((aligned(16))) char smem[];
  lds_ptr lds = (lds_ptr)smem;
  if (blockIdx.x < 256) attn_body(lds, blockIdx.x, ws);
  else mlp2_body(lds, blockIdx.x - 256, w2, b2, ws);
}

constexpr int K3_LDS = 5 * G80_SLOT;

__global__ __launch_bounds__(512, 2) void k3_out(const char* __restrict__ x, const char* __restrict__ wo,
                                                 const char* __restrict__ ws, char* __restrict__ out) {
  extern __shared__ __attribute__((aligned(16))) char smem[];
  lds_ptr lds = (lds_ptr)smem;
  const int tid = threadIdx.x, lane = tid & 63;
  const int wid = __builtin_amdgcn_readfirstlane(tid >> 6);
  const int mi = wid & 3, kh = wid >> 2;
  const int blk = blockIdx.x, xg = blk & 7, idx = blk >> 3;
  const int rbk = xg * 4 + (idx & 3), cbk = idx >> 2;
  const unsigned lds0 = (unsigned)(uintptr_t)lds;
  const unsigned v0 = g80_voff(wid * 160 + lane), v1 = g80_voff(wid * 160 + 64 + lane), v2 = g80_voff(wid * 160 + 128 + lane);
  const char* wsrc = wo + cbk * 160;
  const unsigned dst = lds0 + (unsigned)wid * 2560u;
  const char* asrc = ws + WS_O + ((size_t)(rbk * 4 + mi) * 20 + 2 * kh) * 1024 + lane * 16;
  const int gq = lane >> 4;
  const unsigned wa = (unsigned)((kh * 64 + 4 * ((gq & 1) + 4 * (gq >> 1)) + ((lane & 15) >> 2)) * 160 + (lane & 3) * 8);
#pragma unroll
  for (int c = 0; c < 5; ++c) g80_issue(wsrc + (size_t)c * 128 * 1280, v0, v1, v2, dst + (unsigned)c * G80_SLOT);
  bf16x8 a[10];
#pragma unroll
  for (int c = 0; c < 5; ++c) {
    a[2 * c] = *(const bf16x8*)(asrc + c * 4096);
    a[2 * c + 1] = *(const bf16x8*)(asrc + c * 4096 + 1024);
  }
  const int tok = rbk * 64 + mi * 16 + (lane & 15);
  const size_t xo = (size_t)tok * 1280 + (cbk * 80 + 4 * gq) * 2;
  const char* gs = ws + WS_G + ((size_t)((rbk * 8 + cbk) * 4 + mi) * 5) * 512 + lane * 8;
  u32x2 xv[5], gv[5];
  if (kh == 0) {
#pragma unroll
    for (int j = 0; j < 5; ++j) {
      xv[j] = *(const u32x2*)(x + xo + j * 32);
      gv[j] = *(const u32x2*)(gs + j * 512);
    }
  }
  f32x4 acc[5];
#pragma unroll
  for (int j = 0; j < 5; ++j) acc[j] = f32x4{0.f, 0.f, 0.f, 0.f};
  WAIT_BAR(0);
#pragma unroll
  for (int c = 0; c < 5; ++c) g80_compute(lds + c * G80_SLOT, wa, a[2 * c], a[2 * c + 1], acc);
  asm volatile("s_waitcnt lgkmcnt(0)\n\ts_barrier" ::: "memory");
  LDS_AS f32x4* red = (LDS_AS f32x4*)lds + (mi * 5) * 64 + lane;
  if (kh == 1) {
#pragma unroll
    for (int j = 0; j < 5; ++j) red[j * 64] = acc[j];
  }
  asm volatile("s_waitcnt lgkmcnt(0)\n\ts_barrier" ::: "memory");
  if (kh == 0) {
#pragma unroll
    for (int j = 0; j < 5; ++j) {
      const f32x4 o = red[j * 64];
      const float r0 = bf_lo(xv[j][0]) + (acc[j][0] + o[0]) * bf_lo(gv[j][0]);
      const float r1 = bf_hi(xv[j][0]) + (acc[j][1] + o[1]) * bf_hi(gv[j][0]);
      const float r2 = bf_lo(xv[j][1]) + (acc[j][2] + o[2]) * bf_lo(gv[j][1]);
      const float r3 = bf_hi(xv[j][1]) + (acc[j][3] + o[3]) * bf_hi(gv[j][1]);
      u32x2 v = {cvtpk(r0, r1), cvtpk(r2, r3)};
      *(u32x2*)(out + xo + j * 32) = v;
    }
  }
}

extern "C" void kernel_launch(void* const* d_in, const int* in_sizes, int n_in, void* d_out, int out_size, void* d_ws,
                              size_t ws_size, hipStream_t stream) {
  (void)in_sizes; (void)n_in; (void)out_size; (void)ws_size;
  const char* x = (const char*)d_in[0];
  const char* wqkv = (const char*)d_in[1];
  const char* wo = (const char*)d_in[2];
  const char* w1 = (const char*)d_in[4];
  const unsigned short* b1 = (const unsigned short*)d_in[5];
  const char* w2 = (const char*)d_in[6];
  const unsigned short* b2 = (const unsigned short*)d_in[7];
  char* ws = (char*)d_ws;
  (void)hipFuncSetAttribute((const void*)k1_proj, hipFuncAttributeMaxDynamicSharedMemorySize, K1_LDS);
  (void)hipFuncSetAttribute((const void*)k2_attn_mlp, hipFuncAttributeMaxDynamicSharedMemorySize, K2_LDS);
  (void)hipFuncSetAttribute((const void*)k3_out, hipFuncAttributeMaxDynamicSharedMemorySize, K3_LDS);
  k1_proj<<<256, 512, K1_LDS, stream>>>(x, wqkv, w1, b1, ws);
  k2_attn_mlp<<<512, 512, K2_LDS, stream>>>(w2, b2, ws);
  k3_out<<<256, 512, K3_LDS, stream>>>(x, wo, ws, (char*)d_out);
}
```

```cpp
#include <hip/hip_runtime.h>
#include <hip/hip_bf16.h>
#include <stdint.h>

typedef __attribute__((ext_vector_type(8))) short bf16x8;
typedef __attribute__((ext_vector_type(4))) short bf16x4;
typedef __attribute__((ext_vector_type(16))) float f32x16;
typedef __attribute__((ext_vector_type(4))) float f32x4;
typedef __attribute__((ext_vector_type(2))) float f32x2;
typedef __attribute__((ext_vector_type(4))) unsigned u32x4;
typedef __attribute__((ext_vector_type(2))) unsigned u32x2;
typedef __attribute__((ext_vector_type(2))) __bf16 bf2;
typedef __attribute__((address_space(3))) char* lds_ptr;
#define LDS_AS __attribute__((address_space(3)))
#define DI __device__ __forceinline__

#define MFMA32(a, b, c) __builtin_amdgcn_mfma_f32_32x32x16_bf16((a), (b), (c), 0, 0, 0)
#define MFMA32K8(a, b, c) __builtin_amdgcn_mfma_f32_32x32x8bf16_1k((a), (b), (c), 0, 0, 0)
#define MFMA16(a, b, c) __builtin_amdgcn_mfma_f32_16x16x32_bf16((a), (b), (c), 0, 0, 0)
#define SBAR() __builtin_amdgcn_sched_barrier(0)
#define PIN(x) asm volatile("" : "+v"(x))
#define WAIT_BAR(N) asm volatile("s_waitcnt vmcnt(" #N ") lgkmcnt(0)\n\ts_barrier" ::: "memory")

constexpr int S_ = 2048, D_ = 640, NH = 32, F_ = 1536;
constexpr float LN_EPS = 1e-5f;
constexpr float QSCALE = 0.22360679774997896f * 1.4426950408889634f;

constexpr size_t WS_Q = 0;
constexpr size_t WS_K = WS_Q + (size_t)NH * 64 * 1280;
constexpr size_t WS_V = WS_K + (size_t)NH * 64 * 1280;
constexpr size_t WS_H = WS_V + (size_t)NH * 64 * 1280;
constexpr size_t WS_O = WS_H + (size_t)128 * 48 * 1024;
constexpr size_t WS_G = WS_O + (size_t)128 * 20 * 1024;
constexpr size_t WS_RS = WS_G + (size_t)S_ * D_ * 2;
constexpr size_t WS_CS1 = WS_RS + (size_t)S_ * 8;
constexpr size_t WS_KMX = WS_CS1 + (size_t)F_ * 4;
constexpr size_t WS_CNT = WS_KMX + 10 * 64 * 4;
constexpr size_t WS_END = WS_CNT + 32 * 256;

#define STAMP(kern, i) do { } while (0)
#define STAMPC(kern, i) do { } while (0)
#define STAMP2(kern, i) do { } while (0)

#define STPOL 0
DI void st16(void* p, u32x4 v) {
  *(u32x4*)p = v;
}
DI void st8(void* p, u32x2 v) {
  *(u32x2*)p = v;
}
DI unsigned cvtpk(float lo, float hi) {
  f32x2 f = {lo, hi};
  return __builtin_bit_cast(unsigned, __builtin_convertvector(f, bf2));
}
DI float bf_lo(unsigned u) { return __uint_as_float(u << 16); }
DI float bf_hi(unsigned u) { return __uint_as_float(u & 0xffff0000u); }
DI float dot2(unsigned a, unsigned b, float c) {
  return __builtin_amdgcn_fdot2_f32_bf16(__builtin_bit_cast(bf2, a), __builtin_bit_cast(bf2, b), c, false);
}
DI float xhalf(float v) {
  return __shfl_xor(v, 32);
}
DI void glds16(unsigned voff, const void* sbase, unsigned ldsdst) {
  unsigned keep;
  asm volatile("s_mov_b32 %0, m0\n\ts_mov_b32 m0, %3\n\ts_nop 0\n\tglobal_load_lds_dwordx4 %1, %2\n\ts_mov_b32 m0, %0"
               : "=&s"(keep) : "v"(voff), "s"(sbase), "s"(ldsdst) : "memory");
}
DI void glds16_masked(unsigned voff, const void* sbase, unsigned ldsdst, unsigned mlo, unsigned mhi) {
  unsigned keep;
  asm volatile("s_mov_b32 %0, m0\n\ts_mov_b32 m0, %3\n\ts_mov_b32 exec_lo, %4\n\ts_mov_b32 exec_hi, %5\n\t"
               "global_load_lds_dwordx4 %1, %2\n\ts_mov_b64 exec, -1\n\ts_mov_b32 m0, %0"
               : "=&s"(keep) : "v"(voff), "s"(sbase), "s"(ldsdst), "s"(mlo), "s"(mhi) : "memory");
}
typedef short v4i16_t __attribute__((ext_vector_type(4)));
DI bf16x4 tr_read(lds_ptr p) {
  return __builtin_bit_cast(bf16x4, __builtin_amdgcn_ds_read_tr16_b64_v4i16((LDS_AS v4i16_t*)p));
}
DI bf16x8 cat8(bf16x4 a, bf16x4 b) { return __builtin_shufflevector(a, b, 0, 1, 2, 3, 4, 5, 6, 7); }

constexpr int KA_BUF = 45056;
constexpr int KA_STAT = 3 * KA_BUF;
constexpr int KA_SCS = KA_STAT + 1536, KA_SC1 = KA_SCS + 4096, KA_SB1 = KA_SC1 + 12288;
constexpr int KA_LDS = KA_SB1 + 384;
#define KABL 0
#define LBAR(N) asm volatile("s_waitcnt lgkmcnt(" #N ")\n\ts_barrier" ::: "memory")

typedef __amdgpu_buffer_rsrc_t srd_t;
DI srd_t make_srd(const void* p, unsigned bytes) { return __builtin_amdgcn_make_buffer_rsrc((void*)p, (short)0, (int)bytes, 0x00020000); }
DI u32x4 bload16(srd_t r, unsigned voff, unsigned soff) { return __builtin_amdgcn_raw_buffer_load_b128(r, (int)voff, (int)soff, 0); }
template <int CTRL> DI float dpp_add(float v) {
  return v + __uint_as_float(__builtin_amdgcn_update_dpp(0u, __float_as_uint(v), CTRL, 0xf, 0xf, true));
}
DI float x32_add(float v) {
  const auto rr = __builtin_amdgcn_permlane32_swap(__float_as_uint(v), __float_as_uint(v), false, false);
  return __uint_as_float(rr[0]) + __uint_as_float(rr[1]);
}

DI void ka_helper(lds_ptr lds, const char* __restrict__ x, const char* __restrict__ wqkv, const char* __restrict__ w1,
                  const int n0, const int f0, const int rb) {
  const int tid = threadIdx.x - 512, lane = tid & 63;
  const int w = __builtin_amdgcn_readfirstlane(tid >> 6);
  const unsigned lds0 = (unsigned)(uintptr_t)lds;
  const unsigned vx0 = (unsigned)((lane >> 3) * 1280 + (((lane & 7) ^ (lane >> 4)) << 4));
  const unsigned vx1 = (unsigned)((8 + (lane >> 3)) * 1280 + (((lane & 7) ^ (4 + (lane >> 4))) << 4));
  const unsigned vw = (unsigned)((lane >> 3) * 3840 + ((((((lane >> 2) ^ (lane >> 4)) & 1) << 2) | (lane & 3)) << 4));
  const int p0 = w * 96 + lane, p1 = w * 96 + 64 + (lane & 31);
  const int r0 = (p0 * 5462) >> 16, r1 = (p1 * 5462) >> 16;
  const unsigned vh0 = (unsigned)(r0 * 3072 + (p0 - r0 * 12) * 16), vh1 = (unsigned)(r1 * 3072 + (p1 - r1 * 12) * 16);
  const char* xs = x + (size_t)(rb * 128 + 16 * w) * 1280;
  const char* wsrc = wqkv + ((size_t)(8 * w) * 1920 + n0) * 2;
  const char* hsrc = w1 + f0 * 2;
#define KA_PIECE(p, c, buf)                                                                       \
  do {                                                                                            \
    const unsigned b_ = lds0 + (unsigned)(buf);                                                   \
    if ((p) == 0) glds16(vx0, xs + (c) * 128, b_ + (unsigned)(2 * w) * 1024u);                    \
    if ((p) == 1) glds16(vw, wsrc + (size_t)(c) * (64 * 3840), b_ + 16384u + (unsigned)w * 1024u); \
    if ((p) == 2) glds16(vh0, hsrc + (size_t)(c) * (64 * 3072), b_ + 32768u + (unsigned)w * 1536u); \
    if ((p) == 3) glds16(vx1, xs + (c) * 128, b_ + (unsigned)(2 * w + 1) * 1024u);                \
    if ((p) == 4) glds16(vw, wsrc + (size_t)(c) * (64 * 3840) + 128, b_ + 24576u + (unsigned)w * 1024u); \
    if ((p) == 5) glds16_masked(vh1, hsrc + (size_t)(c) * (64 * 3072), b_ + 32768u + (unsigned)w * 1536u + 1024u, 0xffffffffu, 0u); \
  } while (0)
#define KA_ISSUE(c, buf) do { _Pragma("unroll") for (int p_ = 0; p_ < 6; ++p_) KA_PIECE(p_, c, buf); } while (0)
  KA_ISSUE(0, 0);
  KA_ISSUE(1, KA_BUF);
  KA_ISSUE(2, 2 * KA_BUF);
  const unsigned sxa = (unsigned)tid * 16u;
  const unsigned swa = 16384u + (unsigned)(tid >> 8) * 8192u + (unsigned)(tid & 255) * 16u;
  const unsigned sha = 32768u + (unsigned)tid * 16u;
  float ra[2] = {0.f, 0.f}, rq[2] = {0.f, 0.f}, cs[8], ch[8];
#pragma unroll
  for (int i = 0; i < 8; ++i) { cs[i] = 0.f; ch[i] = 0.f; }
  WAIT_BAR(12);
  int buf = 0;
#pragma unroll 1
  for (int c = 0; c < 10; ++c) {
    lds_ptr sp = lds + buf;
    u32x4 vx[2], vq[2], vh[2];
    vx[0] = *(const LDS_AS u32x4*)(sp + sxa); vx[1] = *(const LDS_AS u32x4*)(sp + sxa + 8192);
    vq[0] = *(const LDS_AS u32x4*)(sp + swa); vq[1] = *(const LDS_AS u32x4*)(sp + swa + 4096);
    if (w < 6) { vh[0] = *(const LDS_AS u32x4*)(sp + sha); vh[1] = *(const LDS_AS u32x4*)(sp + sha + 6144); }
    else { vh[0] = u32x4{0u, 0u, 0u, 0u}; vh[1] = vh[0]; }
    if (KABL & 2) WAIT_BAR(0); else WAIT_BAR(6);
    const int cn = c + 3 < 10 ? c + 3 : 9;
#pragma unroll
    for (int e = 0; e < 4; ++e)
#pragma unroll
      for (int i = 0; i < 2; ++i) {
        if (2 * e + i < 6) { if (c < 9 && !(KABL & 2)) KA_PIECE(2 * e + i, cn, buf); SBAR(); }
        if (KABL & 1) { PIN(vx[i]); PIN(vq[i]); PIN(vh[i]); continue; }
        const float lo = bf_lo(vx[i][e]), hi = bf_hi(vx[i][e]);
        ra[i] += lo; rq[i] = __builtin_fmaf(lo, lo, rq[i]);
        ra[i] += hi; rq[i] = __builtin_fmaf(hi, hi, rq[i]);
        cs[2 * e] += bf_lo(vq[i][e]); cs[2 * e + 1] += bf_hi(vq[i][e]);
        ch[2 * e] += bf_lo(vh[i][e]); ch[2 * e + 1] += bf_hi(vh[i][e]);
        PIN(ra[i]); PIN(rq[i]); PIN(cs[2 * e]); PIN(cs[2 * e + 1]); PIN(ch[2 * e]); PIN(ch[2 * e + 1]);
        SBAR();
      }
    buf = buf == 2 * KA_BUF ? 0 : buf + KA_BUF;
  }
#undef KA_ISSUE
#undef KA_PIECE
#pragma unroll
  for (int i = 0; i < 2; ++i) {
    ra[i] = dpp_add<0x141>(dpp_add<0x4E>(dpp_add<0xB1>(ra[i])));
    rq[i] = dpp_add<0x141>(dpp_add<0x4E>(dpp_add<0xB1>(rq[i])));
  }
  LDS_AS f32x2* st_w = (LDS_AS f32x2*)(lds + KA_STAT);
  LDS_AS float* st_var = (LDS_AS float*)(lds + KA_STAT + 1024);
  LDS_AS float* st_cs = (LDS_AS float*)(lds + KA_SCS);
  LDS_AS float* st_c1 = (LDS_AS float*)(lds + KA_SC1);
  if ((tid & 7) < 2) {
    const int i = tid & 1;
    const float mu = (i ? ra[1] : ra[0]) * (1.0f / D_);
    const float var = fmaxf((i ? rq[1] : rq[0]) * (1.0f / D_) - mu * mu, 0.f);
    st_w[i * 64 + (tid >> 3)] = f32x2{mu, __builtin_amdgcn_rsqf(var + LN_EPS)};
    st_var[i * 64 + (tid >> 3)] = var;
  }
#pragma unroll
  for (int i = 0; i < 8; ++i) cs[i] = x32_add(dpp_add<0x128>(cs[i]));
  if ((lane & 40) == 0) {
    const int cg = (tid >> 8) * 8 + ((((lane >> 2) ^ (lane >> 4)) & 1) << 2) + (lane & 3);
    LDS_AS f32x4* d = (LDS_AS f32x4*)(st_cs + ((w & 3) * 2 + ((lane >> 4) & 1)) * 128 + cg * 8);
    d[0] = f32x4{cs[0], cs[1], cs[2], cs[3]};
    d[1] = f32x4{cs[4], cs[5], cs[6], cs[7]};
  }
  if (tid < 384) {
    LDS_AS f32x4* d = (LDS_AS f32x4*)(st_c1 + tid * 8);
    d[0] = f32x4{ch[0], ch[1], ch[2], ch[3]};
    d[1] = f32x4{ch[4], ch[5], ch[6], ch[7]};
  }
  LBAR(0);
  if (tid < 128) {
    float t = 0.f;
#pragma unroll
    for (int i = 0; i < 8; ++i) t += st_cs[i * 128 + tid];
    st_cs[tid] = t;
  } else if (tid < 224) {
    float t = 0.f;
#pragma unroll
    for (int i = 0; i < 32; ++i) t += st_c1[i * 96 + tid - 128];
    st_c1[tid - 128] = t;
  }
  LBAR(0);
}

template <int TYPE, int nh>
DI void ka_consumer(lds_ptr lds, const char* __restrict__ x, const char* __restrict__ wqkv, const char* __restrict__ wh1,
                    const unsigned short* __restrict__ b1, const int n0, const int f0, const int rb, const int cb,
                    char* __restrict__ ws) {
  constexpr int NHT = nh ? 1 : 2;
  const int tid = threadIdx.x, lane = tid & 63;
  const int mi = __builtin_amdgcn_readfirstlane(tid >> 6) & 3;
  const int r = lane & 31, h = lane >> 5;
  const bool has_qkv = cb < 15;
  STAMP(0, 0);
#define KA_WARM 0
  unsigned warm = 0;
  unsigned braw = 0;
  if (nh == 0 && tid < 96) braw = b1[f0 + tid];
  if (KA_WARM) {
    const char* p;
    if (tid < 224) { const int l = (cb & 3) * 224 + tid; p = x + (size_t)(rb * 128 + (l & 127)) * 1280 + (3 + (l >> 7)) * 128; }
    else if (tid < 336) { const int l = (rb & 7) * 112 + tid - 224; p = wqkv + (size_t)(192 + (l >> 1)) * 3840 + n0 * 2 + (l & 1) * 128; }
    else { const int l = (rb & 7) * 112 + (tid < 448 ? tid - 336 : 0); p = wh1 + (size_t)(192 + (l >> 1)) * 3072 + f0 * 2 + (l & 1) * 128; }
    warm = *(const unsigned*)p;
  }
  unsigned xa[4];
#pragma unroll
  for (int i = 0; i < 4; ++i) xa[i] = (unsigned)((mi * 32 + r) * 128 + (((2 * i + h) ^ ((r >> 1) & 7)) << 4));
  unsigned wa0 = (unsigned)(16384 + nh * 8192 + (8 * h + ((lane & 15) >> 2)) * 128 + 64 * ((lane >> 3) & 1) +
                            (16 * ((lane >> 4) & 1) + 4 * (lane & 3)) * 2);
  unsigned wa1 = wa0 ^ 64u;
  unsigned ha = (unsigned)(32768 + (8 * h + ((lane & 15) >> 2)) * 192 + nh * 128 + (16 * ((lane >> 4) & 1) + 4 * (lane & 3)) * 2);
  f32x16 acc[2], ach[NHT];
#pragma unroll
  for (int i = 0; i < 16; ++i) {
    acc[0][i] = 0.f; acc[1][i] = 0.f;
#pragma unroll
    for (int j = 0; j < NHT; ++j) ach[j][i] = 0.f;
  }
  bf16x8 xf[2], w0, w1, wh[NHT];
#define LP(a) ((lds_ptr)(uintptr_t)(a))
#define KA_RDQ(i)                                                                                               \
  do {                                                                                                          \
    xf[(i) & 1] = *(const LDS_AS bf16x8*)LP(xa[i]);                                                             \
    w0 = cat8(tr_read(LP(wa0) + (i) * 2048), tr_read(LP(wa0) + (i) * 2048 + 512));                              \
    w1 = cat8(tr_read(LP(wa1) + (i) * 2048), tr_read(LP(wa1) + (i) * 2048 + 512));                              \
    SBAR();                                                                                                     \
  } while (0)
#define KA_RDH(i)                                                                                               \
  do {                                                                                                          \
    _Pragma("unroll") for (int j_ = 0; j_ < NHT; ++j_)                                                          \
      wh[j_] = cat8(tr_read(LP(ha) + (i) * 3072 + j_ * 64), tr_read(LP(ha) + (i) * 3072 + j_ * 64 + 768));      \
    SBAR();                                                                                                     \
  } while (0)
#define KA_MMQ(i)                                                                                               \
  do {                                                                                                          \
    if (TYPE == 1) { acc[0] = MFMA32(xf[(i) & 1], w0, acc[0]); acc[1] = MFMA32(xf[(i) & 1], w1, acc[1]); }      \
    else { acc[0] = MFMA32(w0, xf[(i) & 1], acc[0]); acc[1] = MFMA32(w1, xf[(i) & 1], acc[1]); }                \
    PIN(acc[0]); PIN(acc[1]); SBAR();                                                                                                     \
  } while (0)
#define KA_MMH(i)                                                                                               \
  do {                                                                                                          \
    _Pragma("unroll") for (int j_ = 0; j_ < NHT; ++j_) { ach[j_] = MFMA32(wh[j_], xf[(i) & 1], ach[j_]); PIN(ach[j_]); } \
    SBAR();                                                                                                     \
  } while (0)
  {
    const unsigned lds0 = (unsigned)(uintptr_t)lds;
#pragma unroll
    for (int i = 0; i < 4; ++i) xa[i] += lds0;
    wa0 += lds0; wa1 += lds0; ha += lds0;
  }
  LBAR(0);
  STAMP(0, 1);
  KA_RDQ(0); KA_RDH(0);
  int buf = 0;
#pragma unroll 1
  for (int c = 0; c < 10; ++c) {
    STAMP2(0, 6 + c);
    KA_MMQ(0); KA_RDQ(1); KA_MMH(0); KA_RDH(1);
    KA_MMQ(1); KA_RDQ(2); KA_MMH(1); KA_RDH(2);
    KA_MMQ(2); KA_RDQ(3); KA_MMH(2); KA_RDH(3);
    const int d = buf == 2 * KA_BUF ? -2 * KA_BUF : KA_BUF;
    buf += d;
#pragma unroll
    for (int i = 0; i < 4; ++i) { xa[i] += (unsigned)d; PIN(xa[i]); }
    wa0 += (unsigned)d; wa1 += (unsigned)d; ha += (unsigned)d;
    PIN(wa0); PIN(wa1); PIN(ha);
    SBAR();
    KA_MMQ(3);
    LBAR(0);
    KA_RDQ(0);
    KA_MMH(3); KA_RDH(0);
  }
#undef KA_RDQ
#undef KA_RDH
#undef KA_MMQ
#undef KA_MMH
  STAMP(0, 3);
  PIN(warm);
  if (nh == 0 && tid < 96) ((LDS_AS float*)(lds + KA_SB1))[tid] = __uint_as_float(braw << 16);
  LBAR(0);
  LBAR(0);
  STAMP(0, 5);
  LDS_AS f32x2* st_w = (LDS_AS f32x2*)(lds + KA_STAT) + mi * 32;
  LDS_AS float* st_var = (LDS_AS float*)(lds + KA_STAT + 1024);
  LDS_AS float* st_cs = (LDS_AS float*)(lds + KA_SCS) + nh * 64;
  const int tq = rb * 4 + mi;
  const f32x2 mr = st_w[r];
  const float mu = mr[0], rs = mr[1];
  if (has_qkv) {
    if (TYPE == 1) {
      float mu_[16], rs_[16];
#pragma unroll
      for (int g = 0; g < 4; ++g)
#pragma unroll
        for (int e = 0; e < 4; ++e) {
          const f32x2 v = st_w[8 * g + 4 * h + e];
          mu_[4 * g + e] = v[0]; rs_[4 * g + e] = v[1];
        }
#pragma unroll
      for (int j = 0; j < 2; ++j) {
        const float cs = st_cs[j * 32 + r];
        const int nn = n0 - 1280 + nh * 64 + j * 32 + r;
        const int head = (nn * 3277) >> 16;
        const int d = nn - head * 20;
        char* dst = ws + WS_V + (size_t)tq * 1280 + (unsigned)(head * (64 * 1280) + (h * 20 + d) * 16);
#pragma unroll
        for (int s = 0; s < 2; ++s) {
          u32x4 o;
#pragma unroll
          for (int e = 0; e < 4; ++e) {
            const int i = 8 * s + 2 * e;
            o[e] = cvtpk((acc[j][i] - mu_[i] * cs) * rs_[i], (acc[j][i + 1] - mu_[i + 1] * cs) * rs_[i + 1]);
          }
          st16(dst + s * 640, o);
        }
      }
    } else {
      const bool isq = n0 < 640;
      const float sc = isq ? rs * QSCALE : rs;
      char* base = ws + (isq ? WS_Q : WS_K) + (size_t)tq * 1280;
      float gmax = 0.f;
#pragma unroll
      for (int j = 0; j < 2; ++j) {
        const int g0 = (((isq ? n0 : n0 - 640) + nh * 64 + j * 32) >> 2) + h;
#pragma unroll
        for (int g = 0; g < 4; ++g) {
          const f32x4 cs = *(const LDS_AS f32x4*)(st_cs + j * 32 + 8 * g + 4 * h);
          const float v0 = (acc[j][4 * g] - mu * cs[0]) * sc, v1 = (acc[j][4 * g + 1] - mu * cs[1]) * sc;
          const float v2 = (acc[j][4 * g + 2] - mu * cs[2]) * sc, v3 = (acc[j][4 * g + 3] - mu * cs[3]) * sc;
          const u32x2 o = {cvtpk(v0, v1), cvtpk(v2, v3)};
          const float gn = dot2(o[1], o[1], dot2(o[0], o[0], 0.f));
          gmax = (gn > gmax || gn != gn) ? gn : gmax;
          const int G = g0 + 2 * g;
          const int head = (G * 205) >> 10;
          const int dg = G - head * 5;
          const unsigned off = (unsigned)(head * (64 * 1280) + (dg < 4 ? (dg >> 1) * 512 + r * 16 + (dg & 1) * 8 : 1024 + r * 8));
          st8(base + off, o);
        }
      }
      if (!isq) {
#pragma unroll
        for (int o = 32; o >= 1; o >>= 1) {
          const float t = __shfl_xor(gmax, o);
          gmax = (t > gmax || t != t) ? t : gmax;
        }
        if (lane == 0) *(float*)(ws + WS_KMX + (size_t)((((n0 - 640) >> 6) + nh) * 64 + tq) * 4) = gmax;
      }
    }
  }
  {
    const float sc = rs * __builtin_amdgcn_rsqf(st_var[mi * 32 + r] * rs * rs + LN_EPS);
    LDS_AS float* st_c1 = (LDS_AS float*)(lds + KA_SC1) + nh * 64;
    LDS_AS float* st_b = (LDS_AS float*)(lds + KA_SB1) + nh * 64;
    const int tok = rb * 128 + mi * 32 + r;
    constexpr float GK1 = -2.0f * 1.4426950408889634f * 0.7978845608028654f, GK2 = GK1 * 0.044715f;
    char* dst = ws + WS_H + ((size_t)(tok >> 4) * 48 + (f0 >> 5) + nh * 2) * 1024 + (h * 16 + (tok & 15)) * 16;
#pragma unroll
    for (int j = 0; j < NHT; ++j)
#pragma unroll
      for (int u = 0; u < 2; ++u) {
        float v[8];
#pragma unroll
        for (int gg = 0; gg < 2; ++gg) {
          const int g = 2 * u + gg;
          const f32x4 cs = *(const LDS_AS f32x4*)(st_c1 + j * 32 + 8 * g + 4 * h);
          const f32x4 bb = *(const LDS_AS f32x4*)(st_b + j * 32 + 8 * g + 4 * h);
#pragma unroll
          for (int e = 0; e < 4; ++e) {
            const float t = (ach[j][4 * g + e] - mu * cs[e]) * sc + bb[e];
            const float ex = __builtin_amdgcn_exp2f(t * (GK1 + GK2 * t * t));
            v[4 * gg + e] = t * __builtin_amdgcn_rcpf(1.0f + ex);
          }
        }
        st16(dst + j * 1024 + u * 512, u32x4{cvtpk(v[0], v[1]), cvtpk(v[2], v[3]), cvtpk(v[4], v[5]), cvtpk(v[6], v[7])});
      }
  }
  STAMP(0, 4);
}

__global__ __launch_bounds__(1024, 4) void ka_proj(const char* __restrict__ x, const char* __restrict__ wqkv,
                                                   const char* __restrict__ w1, const unsigned short* __restrict__ b1,
                                                   char* __restrict__ ws) {
  extern __shared__ __attribute__((aligned(16))) char smem[];
  lds_ptr lds = (lds_ptr)smem;
  const int bid = blockIdx.x, xg = bid & 7, idx = bid >> 3;
  const int rb = (xg & 1) * 8 + (idx & 7), cb = (xg >> 1) * 4 + (idx >> 3);
  const int n0 = (cb < 15 ? cb : 14) * 128, f0 = cb * 96;
  if (bid == 0 && threadIdx.x < 32) *(unsigned*)(ws + WS_CNT + threadIdx.x * 256) = 0u;
  const int role = __builtin_amdgcn_readfirstlane(threadIdx.x >> 8);
  if (role >= 2) ka_helper(lds, x, wqkv, w1, n0, f0, rb);
  else if (cb < 10) {
    if (role) ka_consumer<0, 1>(lds, x, wqkv, w1, b1, n0, f0, rb, cb, ws);
    else ka_consumer<0, 0>(lds, x, wqkv, w1, b1, n0, f0, rb, cb, ws);
  } else {
    if (role) ka_consumer<1, 1>(lds, x, wqkv, w1, b1, n0, f0, rb, cb, ws);
    else ka_consumer<1, 0>(lds, x, wqkv, w1, b1, n0, f0, rb, cb, ws);
  }
}

typedef __attribute__((address_space(1))) unsigned gu32;
DI void st8_dev(void* p, u32x2 v) { asm volatile("global_store_dwordx2 %0, %1, off sc1" :: "v"(p), "v"(v) : "memory"); }
DI u32x4 bload16_dev(srd_t r, unsigned voff, unsigned soff) { return __builtin_amdgcn_raw_buffer_load_b128(r, (int)voff, (int)soff, 16); }
DI void ld16_asm(bf16x8& d, unsigned voff, const void* sbase) {
  asm volatile("global_load_dwordx4 %0, %1, %2" : "=v"(d) : "v"(voff), "s"(sbase) : "memory");
}
DI void publish(char* ws, int cnt) {
  asm volatile("s_waitcnt vmcnt(0) lgkmcnt(0)\n\ts_barrier" ::: "memory");
  if (threadIdx.x == 0) __hip_atomic_fetch_add((gu32*)(ws + WS_CNT + cnt * 256), 1u, __ATOMIC_RELAXED, __HIP_MEMORY_SCOPE_AGENT);
}
DI void await(char* ws, int cnt, unsigned want, int wid) {
  if (wid == 0) {
    for (unsigned spins = 0; spins < (1u << 16); ++spins) {
      const unsigned v = __hip_atomic_load((gu32*)(ws + WS_CNT + cnt * 256), __ATOMIC_RELAXED, __HIP_MEMORY_SCOPE_AGENT);
      if ((unsigned)__builtin_amdgcn_readfirstlane(v) >= want) break;
      __builtin_amdgcn_s_sleep(1);
    }
  }
  asm volatile("s_waitcnt vmcnt(0)\n\ts_barrier" ::: "memory");
}

constexpr int AT_TILE = 3584;
constexpr int AT_SLOT = 28672;
constexpr int KB_WR = 2 * AT_SLOT;
constexpr int KB_GRP = 40960;
constexpr int KB_RED = KB_WR + 2 * KB_GRP;
constexpr int KB_LDS = KB_RED + 20480;

DI float nanmax(float a, float b) { return (b > a || b != b) ? b : a; }

DI void attn_store(char* ws, const f32x16& o, const float inv, const int head, const int tok, const int h) {
  char* base = ws + WS_O + (size_t)(tok >> 4) * (20 * 1024) + (tok & 15) * 16;
#pragma unroll
  for (int g = 0; g < 3; ++g) {
    if (g < 2 || h == 0) {
      const int Gc = head * 5 + 2 * g + h;
      const int G8 = Gc & 7;
      const int off = (Gc >> 3) * 1024 + (((G8 >> 2) * 2 + (G8 & 1)) * 256) + ((G8 >> 1) & 1) * 8;
      u32x2 v = {cvtpk(o[4 * g] * inv, o[4 * g + 1] * inv), cvtpk(o[4 * g + 2] * inv, o[4 * g + 3] * inv)};
      st8_dev(base + off, v);
    }
  }
}

__global__ __launch_bounds__(512, 2) void kb_main(const char* __restrict__ x, const char* __restrict__ w2,
                                                  const unsigned short* __restrict__ b2, const char* __restrict__ wo,
                                                  char* __restrict__ ws, char* __restrict__ out) {
  extern __shared__ __attribute__((aligned(16))) char smem[];
  lds_ptr lds = (lds_ptr)smem;
  const int tid = threadIdx.x, lane = tid & 63;
  const int wid = __builtin_amdgcn_readfirstlane(tid >> 6);
  const int r = lane & 31, h = lane >> 5;
  const int xg = blockIdx.x & 7, idx = blockIdx.x >> 3;
  const int head = xg * 4 + (idx >> 3), qb = idx & 7;
  const int rbk = xg * 4 + (idx & 3), cbk = idx >> 2;
  const int mi = wid & 3, kh = wid >> 2, gq = lane >> 4;
  const unsigned lds0 = (unsigned)(uintptr_t)lds;
  const char* Kh = ws + WS_K + (size_t)head * (64 * 1280);
  const char* Vh = ws + WS_V + (size_t)head * (64 * 1280);
  const char* Qt = ws + WS_Q + (size_t)(head * 64 + qb * 8 + wid) * 1280;
  STAMP(1, 0);

  const unsigned vlin = (unsigned)lane * 16u;
  const unsigned vv = (unsigned)(h * 20 + r) * 16u;
  const unsigned vv2 = vv + (unsigned)(WS_V - WS_K) - 1280u - 1536u, vv3 = vv2 + 640u - 1024u;
  const unsigned long long mk16 = 0xffffull, mk20 = 0x000fffff000fffffull;
#define SET_M0(v) asm volatile("s_mov_b32 m0, %0" :: "s"(v) : "memory")
#define KV_M0(c) SET_M0(lds0 + (unsigned)(((c) & 1) * AT_SLOT + wid * AT_TILE))
#define KV_SRC(c) (Kh + (size_t)((c) * 8 + wid + 1) * 1280)
#define KV_P0(c) asm volatile("global_load_lds_dwordx4 %0, %1" :: "v"(vlin), "s"(KV_SRC(c)) : "memory")
#define KV_P1(c) asm volatile("s_mov_b64 exec, %2\n\tglobal_load_lds_dwordx4 %0, %1 offset:1024\n\ts_mov_b64 exec, -1" :: "v"(vlin), "s"(KV_SRC(c)), "s"(mk16) : "memory")
#define KV_P23(c) asm volatile("s_mov_b64 exec, %3\n\tglobal_load_lds_dwordx4 %0, %2 offset:1536\n\tglobal_load_lds_dwordx4 %1, %2 offset:2560\n\ts_mov_b64 exec, -1" :: "v"(vv2), "v"(vv3), "s"(KV_SRC(c)), "s"(mk20) : "memory")
  bf16x8 qa = *(const bf16x8*)(Qt + lane * 16);
  u32x2 qbv = *(const u32x2*)(Qt + 1024 + r * 8);
  bf16x8 ka0 = *(const bf16x8*)(Kh + lane * 16);
  u32x2 kb0 = *(const u32x2*)(Kh + 1024 + r * 8);
  float kmax = nanmax(*(const float*)(ws + WS_KMX + (size_t)(((head * 20) >> 6) * 64 + lane) * 4),
                      *(const float*)(ws + WS_KMX + (size_t)(((head * 20 + 19) >> 6) * 64 + lane) * 4));
  SBAR();
  KV_M0(0); asm volatile("s_nop 0"); KV_P0(0); KV_P1(0); KV_P23(0);
#pragma unroll
  for (int sl = 0; sl < 2; ++sl) {
    lds_ptr tp = lds + sl * AT_SLOT + wid * AT_TILE;
    if (lane < 32) *(LDS_AS u32x2*)(tp + 1024 + 256 + lane * 8) = u32x2{0x00003f80u, 0u};
    if (lane < 48) {
      const int blk4 = (lane * 43) >> 9, dd = 20 + lane - blk4 * 12;
      const unsigned val = dd == 20 ? 0x3f803f80u : 0u;
      *(LDS_AS u32x4*)(tp + 1536 + blk4 * 512 + dd * 16) = u32x4{val, val, val, val};
    }
  }
  unsigned vo[5];
#pragma unroll
  for (int i = 0; i < 5; ++i) {
    const int p = i * 64 + lane, row = (p * 6554) >> 16;
    vo[i] = (unsigned)(row * 1280 + (p - row * 10) * 16 - (i & 3) * 1024);
  }
#define W_SRC(W, grp) ((W) + cbk * 160 + (size_t)((grp) * 256 + wid * 32) * 1280)
#define W_M0(reg, hi) SET_M0(lds0 + (unsigned)(KB_WR + (reg) * KB_GRP + wid * 5120 + (hi) * 4096))
#define W_P(W, i, grp) asm volatile("global_load_lds_dwordx4 %0, %1 offset:%2" :: "v"(vo[i]), "s"(W_SRC(W, grp)), "i"(((i) & 3) * 1024) : "memory")
  const char* hsrc = ws + WS_H + ((size_t)(rbk * 4 + mi) * 48 + kh) * 1024 + 4096;
#define H_LOAD(D, j, grp) asm volatile("global_load_dwordx4 %0, %1, %2 offset:%3" : "=v"(D[j]) : "v"(vlin), "s"(hsrc + (grp) * 8192), "i"((j) * 2048 - 4096) : "memory")
  const unsigned wra = lds0 + (unsigned)(KB_WR + (kh * 32 + 4 * ((gq & 1) + 4 * (gq >> 1)) + ((lane & 15) >> 2)) * 160 + (lane & 3) * 8);
  const unsigned wrb = wra + KB_GRP;

#pragma unroll
  for (int o = 32; o >= 1; o >>= 1) kmax = nanmax(kmax, __shfl_xor(kmax, o));
  {
    const u32x4 a = __builtin_bit_cast(u32x4, qa);
    float n0 = dot2(a[0], a[0], 0.f), n1 = dot2(a[1], a[1], 0.f);
    n0 = dot2(a[2], a[2], n0); n1 = dot2(a[3], a[3], n1);
    if (h == 0) { n0 = dot2(qbv[0], qbv[0], n0); n1 = dot2(qbv[1], qbv[1], n1); }
    const float qn = x32_add(n0 + n1);
    const float bound = __builtin_amdgcn_sqrtf(qn * kmax * 5.0f) * 1.01f;
    if (h == 1) { qbv[0] = cvtpk(-bound, 0.f); qbv[1] = 0u; kb0[0] = 0x00003f80u; kb0[1] = 0u; }
  }
  const bf16x4 qb4 = __builtin_bit_cast(bf16x4, qbv);
  f32x16 o;
#pragma unroll
  for (int i = 0; i < 16; ++i) o[i] = 0.f;
  const f32x16 zero = o;
  f32x16 sA = MFMA32(ka0, qa, zero);
  sA = MFMA32K8(__builtin_bit_cast(bf16x4, kb0), qb4, sA);
  f32x16 sB;
  f32x4 acc[5];
#pragma unroll
  for (int j = 0; j < 5; ++j) acc[j] = f32x4{0.f, 0.f, 0.f, 0.f};
  WAIT_BAR(0);
  STAMP(1, 1);
  STAMPC(1, 14);

  unsigned a16 = lds0 + vlin, a8 = lds0 + (unsigned)lane * 8u;
  int sdelta = AT_SLOT;
  bf16x8 kaA, v0A, v1A, kaB, v0B, v1B;
  bf16x4 kbA, kbB;
  bf16x8 hA[4], hB[4];
  bf16x4 wl[5], wu[5];
  u32x4 p0, p1 = {0u, 0u, 0u, 0u};
  v1B = __builtin_bit_cast(bf16x8, p1);
#define RD_KA(X, j) ka##X = *(const LDS_AS bf16x8*)(LP(a16) + (j) * AT_TILE)
#define RD_KB(X, j) kb##X = *(const LDS_AS bf16x4*)(LP(a8) + (j) * AT_TILE + 1024)
#define RD_V0(X, j) v0##X = *(const LDS_AS bf16x8*)(LP(a16) + (j) * AT_TILE + 1536)
#define RD_V1(X, j) v1##X = *(const LDS_AS bf16x8*)(LP(a16) + (j) * AT_TILE + 2560)
#define W_LO(WA, j, t) wl[t] = tr_read(LP(WA) + (j) * 10240 + (t) * 32)
#define W_HI(WA, j, t) wu[t] = tr_read(LP(WA) + (j) * 10240 + (t) * 32 + 1280)
#define G_MM(HS, j, t) do { acc[t] = MFMA16(cat8(wl[t], wu[t]), HS[j], acc[t]); PIN(acc[t]); } while (0)
#define NOP_ do { } while (0)
#define EX1(S, i, SLOT) do { S[i] = __builtin_amdgcn_exp2f(S[i]); PIN(S); SBAR(); SLOT; SBAR(); } while (0)
#define AT_STEP(SC, SN, X, Y, JN, PRE, A0, A1, A2, A3, A4, A5, V0_, V1_, M1, M2, M3)                     \
  do {                                                                                                   \
    PRE; SBAR();                                                                                         \
    EX1(SC, 0, RD_KA(Y, JN));                                                                            \
    EX1(SC, 1, o = MFMA32(v1##Y, __builtin_bit_cast(bf16x8, p1), o));                                    \
    EX1(SC, 2, RD_V1(Y, JN));                                                                            \
    EX1(SC, 3, RD_KB(Y, JN));                                                                            \
    p0[0] = cvtpk(SC[0], SC[1]); p0[1] = cvtpk(SC[2], SC[3]); PIN(p0); SBAR();                           \
    SN = MFMA32(ka##X, qa, zero); SBAR();                                                                \
    EX1(SC, 4, RD_V0(Y, JN));                                                                            \
    EX1(SC, 5, M1);                                                                                      \
    EX1(SC, 6, A0);                                                                                      \
    EX1(SC, 7, A1);                                                                                      \
    p0[2] = cvtpk(SC[4], SC[5]); p0[3] = cvtpk(SC[6], SC[7]); PIN(p0); SBAR();                           \
    SN = MFMA32K8(kb##X, qb4, SN); SBAR();                                                               \
    EX1(SC, 8, A2);                                                                                      \
    EX1(SC, 9, M2);                                                                                      \
    EX1(SC, 10, A3);                                                                                     \
    EX1(SC, 11, A4);                                                                                     \
    p1[0] = cvtpk(SC[8], SC[9]); p1[1] = cvtpk(SC[10], SC[11]); PIN(p1); SBAR();                         \
    o = MFMA32(v0##X, __builtin_bit_cast(bf16x8, p0), o); SBAR();                                        \
    EX1(SC, 12, A5);                                                                                     \
    EX1(SC, 13, M3);                                                                                     \
    EX1(SC, 14, V0_);                                                                                    \
    EX1(SC, 15, V1_);                                                                                    \
    p1[2] = cvtpk(SC[12], SC[13]); p1[3] = cvtpk(SC[14], SC[15]); PIN(p1); SBAR();                       \
  } while (0)
#define FLIP() do { a16 += (unsigned)sdelta; a8 += (unsigned)sdelta; sdelta = -sdelta; } while (0)
#define SYNC_H(HN) asm volatile("s_waitcnt vmcnt(3) lgkmcnt(0)\n\ts_barrier" : "+v"(HN[0]) :: "memory")
#define WAIT_H(HC, j, N) asm volatile("s_waitcnt vmcnt(" #N ")" : "+v"(HC[j]) :: "memory")
#define STEP_E(JN, PRE, WC, HC, j, X0, X1, V0_, V1_)                                                     \
  AT_STEP(sA, sB, A, B, JN, PRE, W_LO(WC, j, 3), W_HI(WC, j, 3), W_LO(WC, j, 4), W_HI(WC, j, 4), X0, X1, V0_, V1_, \
          G_MM(HC, j, 0), G_MM(HC, j, 1), G_MM(HC, j, 2))
#define STEP_O(JN, PRE, WA, jn, HC, j, X0, V0_, V1_)                                                     \
  AT_STEP(sB, sA, B, A, JN, PRE, W_LO(WA, jn, 0), W_HI(WA, jn, 0), W_LO(WA, jn, 1), W_HI(WA, jn, 1), W_LO(WA, jn, 2), \
          W_HI(WA, jn, 2), V0_, V1_, G_MM(HC, j, 3), X0, G_MM(HC, j, 4))
#define CHUNK_G(c, WC, HC, WN, HN, RN, NW, NG, HG)                                                       \
  do {                                                                                                   \
    STEP_E(1, NOP_, WC, HC, 0, W_M0(RN, 0), W_P(NW, 0, NG), W_P(NW, 1, NG), W_P(NW, 2, NG));             \
    STEP_O(2, NOP_, WC, 1, HC, 0, W_P(NW, 3, NG), W_M0(RN, 1), W_P(NW, 4, NG));                          \
    STEP_E(3, WAIT_H(HC, 1, 7), WC, HC, 1, KV_M0((c) + 1), KV_P0((c) + 1), KV_P1((c) + 1), KV_P23((c) + 1)); \
    STEP_O(4, NOP_, WC, 2, HC, 1, H_LOAD(HN, 0, HG), H_LOAD(HN, 1, HG), H_LOAD(HN, 2, HG));              \
    STEP_E(5, WAIT_H(HC, 2, 13), WC, HC, 2, H_LOAD(HN, 3, HG), NOP_, NOP_, NOP_);                        \
    STEP_O(6, NOP_, WC, 3, HC, 2, NOP_, NOP_, NOP_);                                                     \
    STEP_E(7, WAIT_H(HC, 3, 13), WC, HC, 3, NOP_, NOP_, NOP_, NOP_);                                     \
    STEP_O(0, SYNC_H(HN); STAMP2(1, 5 + (c)); FLIP(), WN, 0, HC, 3, NOP_, NOP_, NOP_);                   \
  } while (0)
#define PLAIN_E(JN, X0, X1, X2, X3) AT_STEP(sA, sB, A, B, JN, NOP_, X0, X1, X2, X3, NOP_, NOP_, NOP_, NOP_, NOP_, NOP_, NOP_)
#define PLAIN_O(JN, X0, X1, X2, X3) AT_STEP(sB, sA, B, A, JN, NOP_, X0, X1, X2, X3, NOP_, NOP_, NOP_, NOP_, NOP_, NOP_, NOP_)

  RD_KA(A, 0); RD_KB(A, 0); RD_V0(A, 0); RD_V1(A, 0);
  PLAIN_E(1, W_M0(0, 0), W_P(w2, 0, 0), W_P(w2, 1, 0), W_P(w2, 2, 0));
  PLAIN_O(2, W_P(w2, 3, 0), W_M0(0, 1), W_P(w2, 4, 0), KV_M0(1));
  PLAIN_E(3, KV_P0(1), KV_P1(1), KV_P23(1), NOP_);
  PLAIN_O(4, H_LOAD(hA, 0, 0), H_LOAD(hA, 1, 0), H_LOAD(hA, 2, 0), H_LOAD(hA, 3, 0));
  PLAIN_E(5, NOP_, NOP_, NOP_, NOP_);
  PLAIN_O(6, NOP_, NOP_, NOP_, NOP_);
  PLAIN_E(7, NOP_, NOP_, NOP_, NOP_);
  AT_STEP(sB, sA, B, A, 0, SYNC_H(hA); STAMP2(1, 5); FLIP(), W_LO(wra, 0, 0), W_HI(wra, 0, 0), W_LO(wra, 0, 1), W_HI(wra, 0, 1),
          W_LO(wra, 0, 2), W_HI(wra, 0, 2), NOP_, NOP_, NOP_, NOP_, NOP_);
#pragma unroll 1
  for (int c = 1; c < 7; c += 2) {
    CHUNK_G(c, wra, hA, wrb, hB, 1, w2, c, c);
    const char* wn = c < 5 ? w2 : wo;
    CHUNK_G(c + 1, wrb, hB, wra, hA, 0, wn, (c < 5 ? c + 1 : 0), (c < 5 ? c + 1 : 5));
  }
  const int tok = rbk * 64 + mi * 16 + (lane & 15);
  const size_t xo = (size_t)tok * 1280 + (cbk * 80 + 4 * gq) * 2;
  LDS_AS f32x4* red = (LDS_AS f32x4*)(lds + KB_RED) + (mi * 5) * 64 + lane;
  u32x2 gv[5], xv[5];
  PLAIN_E(1, NOP_,
          _Pragma("unroll") for (int j = 0; j < 5; ++j) gv[j] = *(const u32x2*)(b2 + cbk * 80 + j * 16 + 4 * gq),
          _Pragma("unroll") for (int j = 0; j < 5; ++j) xv[j] = *(const u32x2*)(x + xo + j * 32),
          if (kh == 1) { _Pragma("unroll") for (int j = 0; j < 5; ++j) red[j * 64] = acc[j]; });
  PLAIN_O(2, NOP_, NOP_, NOP_, NOP_);
  PLAIN_E(3, NOP_, NOP_, NOP_, NOP_);
  PLAIN_O(4, asm volatile("s_waitcnt vmcnt(0)" : "+v"(hA[1]), "+v"(hA[2]), "+v"(hA[3]) :: "memory");
          _Pragma("unroll") for (int j = 0; j < 5; ++j) { PIN(gv[j]); PIN(xv[j]); } W_M0(1, 0), W_P(wo, 0, 1), W_P(wo, 1, 1), W_P(wo, 2, 1));
  PLAIN_E(5, W_P(wo, 3, 1), W_M0(1, 1), W_P(wo, 4, 1), NOP_);
  PLAIN_O(6, NOP_, NOP_, NOP_, NOP_);
  PLAIN_E(7, NOP_, NOP_, NOP_, NOP_);
  PLAIN_O(7, NOP_, NOP_, NOP_, NOP_);
  o = MFMA32(v1B, __builtin_bit_cast(bf16x8, p1), o);

  STAMP(1, 3);
  STAMPC(1, 15);
  const int qtok = qb * 256 + wid * 32 + r;
  float l = __shfl(o[8], 32 + r);
  if (__builtin_expect(__any(!(l >= 7.888609e-31f) || !(l < 3.0e38f)), 0)) {
    if (h == 1) { qbv[0] = 0u; }
    const bf16x4 qz = __builtin_bit_cast(bf16x4, qbv);
    float m = -3.0e38f;
#pragma unroll
    for (int i = 0; i < 16; ++i) o[i] = 0.f;
    const unsigned onev = r == 20 ? 0x3f803f80u : 0u;
#pragma unroll 1
    for (int t = 0; t < 64; ++t) {
      const bf16x8 ka = *(const bf16x8*)(Kh + t * 1280 + lane * 16);
      u32x2 kb = *(const u32x2*)(Kh + t * 1280 + 1024 + r * 8);
      u32x4 v0 = {onev, onev, onev, onev}, v1 = v0;
      if (r < 20) {
        v0 = *(const u32x4*)(Vh + t * 1280 + vv);
        v1 = *(const u32x4*)(Vh + t * 1280 + 640 + vv);
      }
      f32x16 s = MFMA32(ka, qa, zero);
      s = MFMA32K8(__builtin_bit_cast(bf16x4, kb), qz, s);
      float tm = s[0];
#pragma unroll
      for (int i = 1; i < 16; ++i) tm = fmaxf(tm, s[i]);
      tm = fmaxf(tm, xhalf(tm));
      const float mn = fmaxf(m, tm);
      const float al = __builtin_amdgcn_exp2f(m - mn);
      m = mn;
      u32x4 q0, q1;
#pragma unroll
      for (int i = 0; i < 16; ++i) { o[i] *= al; s[i] = __builtin_amdgcn_exp2f(s[i] - mn); }
#pragma unroll
      for (int e = 0; e < 4; ++e) { q0[e] = cvtpk(s[2 * e], s[2 * e + 1]); q1[e] = cvtpk(s[8 + 2 * e], s[9 + 2 * e]); }
      o = MFMA32(__builtin_bit_cast(bf16x8, v0), __builtin_bit_cast(bf16x8, q0), o);
      o = MFMA32(__builtin_bit_cast(bf16x8, v1), __builtin_bit_cast(bf16x8, q1), o);
    }
    l = __shfl(o[8], 32 + r);
  }
  attn_store(ws, o, __builtin_amdgcn_rcpf(l), head, qtok, h);
  publish(ws, qb);
  STAMP(1, 4);
  if (kh == 0) {
#pragma unroll
    for (int j = 0; j < 5; ++j) {
      const f32x4 og = red[j * 64];
      const u32x2 bb = gv[j];
      float t[4] = {acc[j][0] + og[0] + bf_lo(bb[0]), acc[j][1] + og[1] + bf_hi(bb[0]), acc[j][2] + og[2] + bf_lo(bb[1]),
                    acc[j][3] + og[3] + bf_hi(bb[1])};
#pragma unroll
      for (int e = 0; e < 4; ++e)
        t[e] = t[e] * __builtin_amdgcn_rcpf(1.0f + __builtin_amdgcn_exp2f(t[e] * -1.4426950408889634f));
      gv[j] = u32x2{cvtpk(t[0], t[1]), cvtpk(t[2], t[3])};
      PIN(gv[j]);
    }
  }
  {
    const char* s_ = wo + cbk * 160 + (size_t)(512 + wid * 16) * 1280;
    SET_M0(lds0 + (unsigned)(wid * 2560));
    asm volatile("s_nop 0\n\tglobal_load_lds_dwordx4 %0, %3\n\tglobal_load_lds_dwordx4 %1, %3 offset:1024\n\t"
                 "s_mov_b32 exec_hi, 0\n\tglobal_load_lds_dwordx4 %2, %3 offset:2048\n\ts_mov_b64 exec, -1"
                 :: "v"(vo[0]), "v"(vo[1]), "v"(vo[2]), "s"(s_) : "memory");
  }
#pragma unroll
  for (int j = 0; j < 5; ++j) acc[j] = f32x4{0.f, 0.f, 0.f, 0.f};
  STAMP(1, 6);
  await(ws, rbk >> 2, 32u, wid);
  STAMP(1, 7);
  {
    const srd_t ro = make_srd(ws + WS_O, (unsigned)(WS_G - WS_O));
    const unsigned oa = (unsigned)(((rbk * 4 + mi) * 20 + kh) * 1024);
    u32x4 af[10];
#pragma unroll
    for (int c = 0; c < 10; ++c) af[c] = bload16_dev(ro, vlin, oa + c * 2048u);
    SBAR();
#pragma unroll
    for (int c = 0; c < 10; ++c) {
      const unsigned wa = c < 8 ? wra : wra - (unsigned)KB_WR;
      const int cc = c < 8 ? c : c - 8;
#pragma unroll
      for (int t = 0; t < 5; ++t) { W_LO(wa, cc, t); W_HI(wa, cc, t); }
#pragma unroll
      for (int t = 0; t < 5; ++t) acc[t] = MFMA16(cat8(wl[t], wu[t]), __builtin_bit_cast(bf16x8, af[c]), acc[t]);
    }
  }
  if (kh == 1) {
#pragma unroll
    for (int j = 0; j < 5; ++j) red[j * 64] = acc[j];
  }
  LBAR(0);
  if (kh == 0) {
#pragma unroll
    for (int j = 0; j < 5; ++j) {
      const f32x4 og = red[j * 64];
      const float r0 = bf_lo(xv[j][0]) + (acc[j][0] + og[0]) * bf_lo(gv[j][0]);
      const float r1 = bf_hi(xv[j][0]) + (acc[j][1] + og[1]) * bf_hi(gv[j][0]);
      const float r2 = bf_lo(xv[j][1]) + (acc[j][2] + og[2]) * bf_lo(gv[j][1]);
      const float r3 = bf_hi(xv[j][1]) + (acc[j][3] + og[3]) * bf_hi(gv[j][1]);
      *(u32x2*)(out + xo + j * 32) = u32x2{cvtpk(r0, r1), cvtpk(r2, r3)};
    }
  }
  STAMP(1, 8);
}

extern "C" void kernel_launch(void* const* d_in, const int* in_sizes, int n_in, void* d_out, int out_size, void* d_ws,
                              size_t ws_size, hipStream_t stream) {
  (void)in_sizes; (void)n_in; (void)out_size; (void)ws_size;
  const char* x = (const char*)d_in[0];
  const char* wqkv = (const char*)d_in[1];
  const char* wo = (const char*)d_in[2];
  const char* w1 = (const char*)d_in[4];
  const unsigned short* b1 = (const unsigned short*)d_in[5];
  const char* w2 = (const char*)d_in[6];
  const unsigned short* b2 = (const unsigned short*)d_in[7];
  char* ws = (char*)d_ws;
  (void)hipFuncSetAttribute((const void*)ka_proj, hipFuncAttributeMaxDynamicSharedMemorySize, KA_LDS);
  (void)hipFuncSetAttribute((const void*)kb_main, hipFuncAttributeMaxDynamicSharedMemorySize, KB_LDS);
  ka_proj<<<256, 1024, KA_LDS, stream>>>(x, wqkv, w1, b1, ws);
  kb_main<<<256, 512, KB_LDS, stream>>>(x, w2, b2, wo, ws, (char*)d_out);
}
```

```cpp
#include <hip/hip_runtime.h>
#include <hip/hip_bf16.h>
#include <stdint.h>

typedef __attribute__((ext_vector_type(8))) short bf16x8;
typedef __attribute__((ext_vector_type(4))) short bf16x4;
typedef __attribute__((ext_vector_type(16))) float f32x16;
typedef __attribute__((ext_vector_type(4))) float f32x4;
typedef __attribute__((ext_vector_type(2))) float f32x2;
typedef __attribute__((ext_vector_type(4))) unsigned u32x4;
typedef __attribute__((ext_vector_type(2))) unsigned u32x2;
typedef __attribute__((ext_vector_type(2))) __bf16 bf2;
typedef __attribute__((address_space(3))) char* lds_ptr;
#define LDS_AS __attribute__((address_space(3)))
#define DI __device__ __forceinline__

#define MFMA32(a, b, c) __builtin_amdgcn_mfma_f32_32x32x16_bf16((a), (b), (c), 0, 0, 0)
#define MFMA32K8(a, b, c) __builtin_amdgcn_mfma_f32_32x32x8bf16_1k((a), (b), (c), 0, 0, 0)
#define MFMA16(a, b, c) __builtin_amdgcn_mfma_f32_16x16x32_bf16((a), (b), (c), 0, 0, 0)
#define SBAR() __builtin_amdgcn_sched_barrier(0)
#define PIN(x) asm volatile("" : "+v"(x))
#define WAIT_BAR(N) asm volatile("s_waitcnt vmcnt(" #N ") lgkmcnt(0)\n\ts_barrier" ::: "memory")

constexpr int S_ = 2048, D_ = 640, NH = 32, F_ = 1536;
constexpr float LN_EPS = 1e-5f;
constexpr float QSCALE = 0.22360679774997896f * 1.4426950408889634f;

constexpr size_t WS_Q = 0;
constexpr size_t WS_K = WS_Q + (size_t)NH * 64 * 1280;
constexpr size_t WS_V = WS_K + (size_t)NH * 64 * 1280;
constexpr size_t WS_H = WS_V + (size_t)NH * 64 * 1280;
constexpr size_t WS_O = WS_H + (size_t)128 * 48 * 1024;
constexpr size_t WS_G = WS_O + (size_t)128 * 20 * 1024;
constexpr size_t WS_RS = WS_G + (size_t)S_ * D_ * 2;
constexpr size_t WS_CS1 = WS_RS + (size_t)S_ * 8;
constexpr size_t WS_KMX = WS_CS1 + (size_t)F_ * 4;
constexpr size_t WS_CNT = WS_KMX + 10 * 64 * 4;
constexpr size_t WS_END = WS_CNT + 32 * 256;

#define STAMP(kern, i) do { } while (0)
#define STAMPC(kern, i) do { } while (0)
#define STAMP2(kern, i) do { } while (0)

#define STPOL 2
DI void st16(void* p, u32x4 v) {
  asm volatile("global_store_dwordx4 %0, %1, off sc1\n\ts_nop 1" :: "v"(p), "v"(v) : "memory");
}
DI void st8(void* p, u32x2 v) {
  asm volatile("global_store_dwordx2 %0, %1, off sc1" :: "v"(p), "v"(v) : "memory");
}
DI unsigned cvtpk(float lo, float hi) {
  f32x2 f = {lo, hi};
  return __builtin_bit_cast(unsigned, __builtin_convertvector(f, bf2));
}
DI float bf_lo(unsigned u) { return __uint_as_float(u << 16); }
DI float bf_hi(unsigned u) { return __uint_as_float(u & 0xffff0000u); }
DI float dot2(unsigned a, unsigned b, float c) {
  return __builtin_amdgcn_fdot2_f32_bf16(__builtin_bit_cast(bf2, a), __builtin_bit_cast(bf2, b), c, false);
}
DI float xhalf(float v) {
  return __shfl_xor(v, 32);
}
DI void glds16(unsigned voff, const void* sbase, unsigned ldsdst) {
  unsigned keep;
  asm volatile("s_mov_b32 %0, m0\n\ts_mov_b32 m0, %3\n\ts_nop 0\n\tglobal_load_lds_dwordx4 %1, %2\n\ts_mov_b32 m0, %0"
               : "=&s"(keep) : "v"(voff), "s"(sbase), "s"(ldsdst) : "memory");
}
DI void glds16_masked(unsigned voff, const void* sbase, unsigned ldsdst, unsigned mlo, unsigned mhi) {
  unsigned keep;
  asm volatile("s_mov_b32 %0, m0\n\ts_mov_b32 m0, %3\n\ts_mov_b32 exec_lo, %4\n\ts_mov_b32 exec_hi, %5\n\t"
               "global_load_lds_dwordx4 %1, %2\n\ts_mov_b64 exec, -1\n\ts_mov_b32 m0, %0"
               : "=&s"(keep) : "v"(voff), "s"(sbase), "s"(ldsdst), "s"(mlo), "s"(mhi) : "memory");
}
typedef short v4i16_t __attribute__((ext_vector_type(4)));
DI bf16x4 tr_read(lds_ptr p) {
  return __builtin_bit_cast(bf16x4, __builtin_amdgcn_ds_read_tr16_b64_v4i16((LDS_AS v4i16_t*)p));
}
DI bf16x8 cat8(bf16x4 a, bf16x4 b) { return __builtin_shufflevector(a, b, 0, 1, 2, 3, 4, 5, 6, 7); }

constexpr int KA_BUF = 45056;
constexpr int KA_STAT = 3 * KA_BUF;
constexpr int KA_SCS = KA_STAT + 2048, KA_SB1 = KA_SCS + 3584, KA_SMR = KA_SB1 + 384;
constexpr int KA_LDS = KA_SMR + 2048;
#define KABL 0
#define LBAR(N) asm volatile("s_waitcnt lgkmcnt(" #N ")\n\ts_barrier" ::: "memory")
#define LP(a) ((lds_ptr)(uintptr_t)(a))
#define SET_M0(v) asm volatile("s_mov_b32 m0, %0\n\ts_nop 0" :: "s"(v) : "memory")
#define NOP_ do { } while (0)
#define MFMA4(a, b, c) __builtin_amdgcn_mfma_f32_4x4x4bf16_1k((a), (b), (c), 0, 0, 0)

typedef __amdgpu_buffer_rsrc_t srd_t;
DI srd_t make_srd(const void* p, unsigned bytes) { return __builtin_amdgcn_make_buffer_rsrc((void*)p, (short)0, (int)bytes, 0x00020000); }
DI float x32_add(float v) {
  const auto rr = __builtin_amdgcn_permlane32_swap(__float_as_uint(v), __float_as_uint(v), false, false);
  return __uint_as_float(rr[0]) + __uint_as_float(rr[1]);
}
DI bf16x4 lo4(bf16x8 v) { return __builtin_shufflevector(v, v, 0, 1, 2, 3); }
DI bf16x4 hi4(bf16x8 v) { return __builtin_shufflevector(v, v, 4, 5, 6, 7); }

template <int TYPE, int nh>
DI void ka_body(lds_ptr lds, const char* __restrict__ x, const char* __restrict__ wqkv, const char* __restrict__ wh1,
                const unsigned short* __restrict__ b1, const char* __restrict__ w2, const char* __restrict__ wo,
                const int n0, const int f0, const int rb, const int cb, char* __restrict__ ws) {
  constexpr int NHT = nh ? 1 : 2;
  const int tid = threadIdx.x, lane = tid & 63;
  const int w = __builtin_amdgcn_readfirstlane(tid >> 6);
  const int mi = w & 3;
  const int r = lane & 31, h = lane >> 5;
  const unsigned lds0 = (unsigned)(uintptr_t)lds;
  STAMP(0, 0);
  const unsigned vx0 = (unsigned)((lane >> 3) * 1280 + (((lane & 7) ^ (lane >> 4)) << 4));
  const unsigned vx1 = (unsigned)((8 + (lane >> 3)) * 1280 + (((lane & 7) ^ (4 + (lane >> 4))) << 4) - 1024);
  const unsigned vw = (unsigned)((lane >> 3) * 3840 + ((((((lane >> 2) ^ (lane >> 4)) & 1) << 2) | (lane & 3)) << 4));
  const int p0 = w * 96 + lane, p1 = w * 96 + 64 + r;
  const int r0 = (p0 * 5462) >> 16, r1 = (p1 * 5462) >> 16;
  const unsigned vh0 = (unsigned)(r0 * 3072 + (p0 - r0 * 12) * 16), vh1 = (unsigned)(r1 * 3072 + (p1 - r1 * 12) * 16 - 1024);
  const char* xs = x + (size_t)(rb * 128 + 16 * w) * 1280;
  const char* wsrc = wqkv + ((size_t)(8 * w) * 1920 + n0) * 2;
  const char* hsrc = wh1 + f0 * 2;
  const unsigned mx = lds0 + (unsigned)w * 2048u, mw = lds0 + 16384u + (unsigned)w * 1024u, mh = lds0 + 32768u + (unsigned)w * 1536u;
#define KA_ROT 0
  const int rot = KA_ROT ? (rb * 3 + cb * 7) % 10 : 0;
#define KC(c) ((c) + rot >= 10 ? (c) + rot - 10 : (c) + rot)
#define KA_DX(c, buf) do { SET_M0(mx + (unsigned)(buf)); asm volatile("global_load_lds_dwordx4 %0, %2\n\tglobal_load_lds_dwordx4 %1, %2 offset:1024" :: "v"(vx0), "v"(vx1), "s"(xs + KC(c) * 128) : "memory"); } while (0)
#define KA_DW(c, buf, hf) do { SET_M0(mw + (unsigned)(buf) + (hf) * 8192u); asm volatile("global_load_lds_dwordx4 %0, %1" :: "v"(vw), "s"(wsrc + (size_t)KC(c) * (64 * 3840) + (hf) * 128) : "memory"); } while (0)
#define KA_DH(c, buf) do { SET_M0(mh + (unsigned)(buf)); asm volatile("global_load_lds_dwordx4 %0, %2\n\ts_mov_b32 exec_hi, 0\n\tglobal_load_lds_dwordx4 %1, %2 offset:1024\n\ts_mov_b32 exec_hi, -1" :: "v"(vh0), "v"(vh1), "s"(hsrc + (size_t)KC(c) * (64 * 3072)) : "memory"); } while (0)
#define KA_ISSUE(c, buf) do { KA_DX(c, buf); KA_DW(c, buf, 0); KA_DW(c, buf, 1); KA_DH(c, buf); } while (0)
  unsigned braw = 0, warm = 0;
  if (nh == 0 && tid < 96) braw = b1[f0 + tid];
  SBAR();
  KA_ISSUE(0, 0);
  KA_ISSUE(1, KA_BUF);
  KA_ISSUE(2, 2 * KA_BUF);
#define KA_WARM 0
  unsigned warm2 = 0;
  if (KA_WARM) {
    const char* p;
    if (tid < 224) { const int l = (cb & 3) * 224 + tid; p = x + (size_t)(rb * 128 + (l & 127)) * 1280 + (3 + (l >> 7)) * 128; }
    else if (tid < 336) { const int l = (rb & 7) * 112 + tid - 224; p = wqkv + (size_t)(192 + (l >> 1)) * 3840 + n0 * 2 + (l & 1) * 128; }
    else { const int l = (rb & 7) * 112 + (tid < 448 ? tid - 336 : 0); p = wh1 + (size_t)(192 + (l >> 1)) * 3072 + f0 * 2 + (l & 1) * 128; }
    asm volatile("global_load_dword %0, %1, off" : "=v"(warm2) : "v"(p) : "memory");
  }

  unsigned xa[4], wa0[4], ha[4];
  {
    const unsigned wb = lds0 + (unsigned)(16384 + nh * 8192 + (8 * h + ((lane & 15) >> 2)) * 128 + 64 * ((lane >> 3) & 1) +
                                          (16 * ((lane >> 4) & 1) + 4 * (lane & 3)) * 2);
    const unsigned hb = lds0 + (unsigned)(32768 + (8 * h + ((lane & 15) >> 2)) * 192 + nh * 128 + (16 * ((lane >> 4) & 1) + 4 * (lane & 3)) * 2);
#pragma unroll
    for (int i = 0; i < 4; ++i) {
      const int ks = i ^ mi;
      xa[i] = lds0 + (unsigned)((mi * 32 + r) * 128 + (((2 * ks + h) ^ ((r >> 1) & 7)) << 4));
      wa0[i] = wb + (unsigned)(ks * 2048);
      ha[i] = hb + (unsigned)(ks * 3072);
    }
  }
  f32x16 acc[2], ach[NHT];
  f32x4 cq[2], chh[NHT], rsum = {0.f, 0.f, 0.f, 0.f}, rsq = rsum;
#pragma unroll
  for (int i = 0; i < 16; ++i) {
    acc[0][i] = 0.f; acc[1][i] = 0.f;
#pragma unroll
    for (int j = 0; j < NHT; ++j) ach[j][i] = 0.f;
  }
  cq[0] = rsum; cq[1] = rsum;
#pragma unroll
  for (int j = 0; j < NHT; ++j) chh[j] = rsum;
  const bf16x4 ones = {0x3f80, 0x3f80, 0x3f80, 0x3f80};
  bf16x8 xf[3], w0[3], w1[3], wh[3][NHT];
  unsigned wa1[4];
#pragma unroll
  for (int i = 0; i < 4; ++i) wa1[i] = wa0[i] ^ 64u;
#define KA_READ(B, i)                                                                                           \
  do {                                                                                                          \
    constexpr int s_ = ((B) + (i)) % 3;                                                                         \
    if ((KABL & 8) && started) break;                                                                           \
    xf[s_] = *(const LDS_AS bf16x8*)(LP(xa[i]) + BUFOFF(B));                                                    \
    if ((KABL & 16) && started) break;                                                                          \
    w0[s_] = cat8(tr_read(LP(wa0[i]) + BUFOFF(B)), tr_read(LP(wa0[i]) + BUFOFF(B) + 512));                      \
    w1[s_] = cat8(tr_read(LP(wa1[i]) + BUFOFF(B)), tr_read(LP(wa1[i]) + BUFOFF(B) + 512));                      \
    _Pragma("unroll") for (int j_ = 0; j_ < NHT; ++j_)                                                          \
      wh[s_][j_] = cat8(tr_read(LP(ha[i]) + BUFOFF(B) + j_ * 64), tr_read(LP(ha[i]) + BUFOFF(B) + j_ * 64 + 768)); \
  } while (0)
#define SGB(m, n) __builtin_amdgcn_sched_group_barrier((m), (n), 0)
#define KA_STEP(B, i, RD)                                                                                       \
  do {                                                                                                          \
    constexpr int b_ = ((B) + (i)) % 3;                                                                         \
    if constexpr (NHT == 2) asm volatile("" : "+v"(xf[b_]), "+v"(w0[b_]), "+v"(w1[b_]), "+v"(wh[b_][0]), "+v"(wh[b_][NHT - 1])); \
    else asm volatile("" : "+v"(xf[b_]), "+v"(w0[b_]), "+v"(w1[b_]), "+v"(wh[b_][0]));                          \
    SBAR();                                                                                                     \
    RD;                                                                                                         \
    if (TYPE == 1) { acc[0] = MFMA32(xf[b_], w0[b_], acc[0]); acc[1] = MFMA32(xf[b_], w1[b_], acc[1]); }        \
    else { acc[0] = MFMA32(w0[b_], xf[b_], acc[0]); acc[1] = MFMA32(w1[b_], xf[b_], acc[1]); }                  \
    _Pragma("unroll") for (int j_ = 0; j_ < NHT; ++j_) ach[j_] = MFMA32(wh[b_][j_], xf[b_], ach[j_]);           \
    SGB(0x8, 1); SGB(0x100, 3); SGB(0x8, 1); SGB(0x100, 2); SGB(0x8, 1); SGB(0x100, 2);                         \
    if (NHT == 2) { SGB(0x8, 1); SGB(0x100, 2); }                                                               \
    if constexpr (NHT == 2) asm volatile("" : "+v"(acc[0]), "+v"(acc[1]), "+v"(ach[0]), "+v"(ach[NHT - 1]));    \
    else asm volatile("" : "+v"(acc[0]), "+v"(acc[1]), "+v"(ach[0]));                                           \
    SBAR();                                                                                                     \
    if ((i) == 0 && !(KABL & 1)) {                                                                              \
      cq[0] = MFMA4(ones, lo4(w0[b_]), cq[0]); cq[1] = MFMA4(ones, lo4(w1[b_]), cq[1]);                         \
      _Pragma("unroll") for (int j_ = 0; j_ < NHT; ++j_) chh[j_] = MFMA4(ones, lo4(wh[b_][j_]), chh[j_]);       \
      cq[0] = MFMA4(ones, hi4(w0[b_]), cq[0]); cq[1] = MFMA4(ones, hi4(w1[b_]), cq[1]);                         \
      _Pragma("unroll") for (int j_ = 0; j_ < NHT; ++j_) chh[j_] = MFMA4(ones, hi4(wh[b_][j_]), chh[j_]);       \
      if constexpr (NHT == 2) asm volatile("" : "+v"(cq[0]), "+v"(cq[1]), "+v"(chh[0]), "+v"(chh[NHT - 1]));    \
      else asm volatile("" : "+v"(cq[0]), "+v"(cq[1]), "+v"(chh[0]));                                           \
    }                                                                                                           \
    if ((nh ? (i) < 2 : (i) >= 2) && !(KABL & 2)) {                                                             \
      rsum = MFMA4(ones, lo4(xf[b_]), rsum); rsq = MFMA4(lo4(xf[b_]), lo4(xf[b_]), rsq);                        \
      rsum2 = MFMA4(ones, hi4(xf[b_]), rsum2); rsq2 = MFMA4(hi4(xf[b_]), hi4(xf[b_]), rsq2);                    \
      asm volatile("" : "+v"(rsum), "+v"(rsq), "+v"(rsum2), "+v"(rsq2));                                        \
    }                                                                                                           \
    SBAR();                                                                                                     \
  } while (0)
#define BUFOFF(B) ((B) * KA_BUF)
#define KA_CHUNK(c, B, BN, WAITN, PREV, MORE)                                                                   \
  do {                                                                                                          \
    KA_STEP(B, 0, KA_READ(B, 2));                                                                               \
    if ((PREV) && !(KABL & 4)) { KA_DH((c) + 2, BUFOFF(((B) + 2) % 3)); SBAR(); }                               \
    KA_STEP(B, 1, KA_READ(B, 3));                                                                               \
    if (!(KABL & 32)) WAIT_BAR(WAITN);                                                                          \
    KA_STEP(B, 2, KA_READ(BN, 0));                                                                              \
    if ((MORE) && !(KABL & 4)) { KA_DX((c) + 3, BUFOFF(B)); SBAR(); }                                           \
    KA_STEP(B, 3, KA_READ(BN, 1));                                                                              \
    if ((MORE) && !(KABL & 4)) { KA_DW((c) + 3, BUFOFF(B), 0); KA_DW((c) + 3, BUFOFF(B), 1); SBAR(); }          \
  } while (0)
  f32x4 rsum2 = rsum, rsq2 = rsum;
  if (KA_WARM) WAIT_BAR(13); else WAIT_BAR(12);
  STAMP(0, 1);
  STAMPC(0, 14);
  bool started = false;
  KA_READ(0, 0);
  KA_READ(0, 1);
  if (KABL & 24) { KA_READ(0, 2); started = true; }
  if (KA_WARM) KA_CHUNK(0, 0, 1, 7, false, true); else KA_CHUNK(0, 0, 1, 6, false, true);
#pragma unroll 1
  for (int c = 1; c < 7; c += 3) {
    KA_CHUNK(c, 1, 2, 6, true, true);
    KA_CHUNK(c + 1, 2, 0, 6, true, true);
    KA_CHUNK(c + 2, 0, 1, 6, true, true);
  }
  KA_CHUNK(7, 1, 2, 6, true, false);
  KA_CHUNK(8, 2, 0, 0, false, false);
  KA_STEP(0, 0, KA_READ(0, 2));
  KA_STEP(0, 1, KA_READ(0, 3));
  KA_STEP(0, 2, NOP_);
  KA_STEP(0, 3, NOP_);
  asm volatile("s_nop 7" : "+v"(rsum), "+v"(rsq), "+v"(rsum2), "+v"(rsq2));
  rsum += rsum2; rsq += rsq2;
#undef KA_READ
#undef KA_STEP
#undef KA_CHUNK
#undef KA_ISSUE
  STAMP(0, 3);
  STAMPC(0, 15);

  if (nh == 1) {
    const int l0_ = blockIdx.x * 85 + (tid - 256 < 85 ? tid - 256 : 0);
    const char* p = l0_ < 15360 ? w2 + (size_t)l0_ * 128 : wo + (size_t)(l0_ - 15360) * 128;
    if (w < 6) asm volatile("global_load_dword %0, %1, off" : "=v"(warm) : "v"(p) : "memory");
  }
  LDS_AS f32x2* st_r = (LDS_AS f32x2*)(lds + KA_STAT);
  LDS_AS float* st_cs = (LDS_AS float*)(lds + KA_SCS);
  LDS_AS float* st_b = (LDS_AS float*)(lds + KA_SB1);
  {
    const int d = lane & 3;
    const float sq = d == 0 ? rsq[0] : d == 1 ? rsq[1] : d == 2 ? rsq[2] : rsq[3];
    const f32x2 rv = {x32_add(rsum[0]), x32_add(sq)};
    const float c0 = x32_add(cq[0][0]), c1 = x32_add(cq[1][0]);
    float c2[NHT];
#pragma unroll
    for (int j = 0; j < NHT; ++j) c2[j] = x32_add(chh[j][0]);
    if (h == 0) {
      st_r[nh * 128 + mi * 32 + r] = rv;
      st_cs[mi * 224 + nh * 64 + r] = c0;
      st_cs[mi * 224 + nh * 64 + 32 + r] = c1;
#pragma unroll
      for (int j = 0; j < NHT; ++j) st_cs[mi * 224 + 128 + nh * 64 + j * 32 + r] = c2[j];
    }
    PIN(warm2);
    if (nh == 0 && tid < 96) st_b[tid] = __uint_as_float(braw << 16);
  }
  LBAR(0);
  STAMP(0, 5);
  const f32x2 ra = st_r[mi * 32 + r], rb2 = st_r[128 + mi * 32 + r];
  const float mu = (ra[0] + rb2[0]) * (1.0f / D_);
  const float var = fmaxf((ra[1] + rb2[1]) * (1.0f / D_) - mu * mu, 0.f);
  const float rs = __builtin_amdgcn_rsqf(var + LN_EPS);
  const int tq = rb * 4 + mi;
#define CS4(off) (*(const LDS_AS f32x4*)(st_cs + (off)) + *(const LDS_AS f32x4*)(st_cs + 224 + (off)) + \
                  *(const LDS_AS f32x4*)(st_cs + 448 + (off)) + *(const LDS_AS f32x4*)(st_cs + 672 + (off)))
  if (cb < 15) {
    if (TYPE == 1) {
      LDS_AS f32x2* st_w = (LDS_AS f32x2*)(lds + KA_SMR) + w * 32;
      if (h == 0) st_w[r] = f32x2{mu, rs};
      float mu_[16], rs_[16];
#pragma unroll
      for (int g = 0; g < 4; ++g)
#pragma unroll
        for (int e = 0; e < 4; ++e) {
          const f32x2 v = st_w[8 * g + 4 * h + e];
          mu_[4 * g + e] = v[0]; rs_[4 * g + e] = v[1];
        }
#pragma unroll
      for (int j = 0; j < 2; ++j) {
        const int nl = nh * 64 + j * 32 + r;
        const float cs = st_cs[nl] + st_cs[224 + nl] + st_cs[448 + nl] + st_cs[672 + nl];
        const int nn = n0 - 1280 + nl;
        const int head = (nn * 3277) >> 16;
        const int d = nn - head * 20;
        char* dst = ws + WS_V + (size_t)tq * 1280 + (unsigned)(head * (64 * 1280) + (h * 20 + d) * 16);
#pragma unroll
        for (int s = 0; s < 2; ++s) {
          u32x4 o;
#pragma unroll
          for (int e = 0; e < 4; ++e) {
            const int i = 8 * s + 2 * e;
            o[e] = cvtpk((acc[j][i] - mu_[i] * cs) * rs_[i], (acc[j][i + 1] - mu_[i + 1] * cs) * rs_[i + 1]);
          }
          st16(dst + s * 640, o);
        }
      }
    } else {
      const bool isq = n0 < 640;
      const float sc = isq ? rs * QSCALE : rs;
      char* base = ws + (isq ? WS_Q : WS_K) + (size_t)tq * 1280;
      float gmax = 0.f;
#pragma unroll
      for (int j = 0; j < 2; ++j) {
        const int g0 = (((isq ? n0 : n0 - 640) + nh * 64 + j * 32) >> 2) + h;
#pragma unroll
        for (int g = 0; g < 4; ++g) {
          const f32x4 cs = CS4(nh * 64 + j * 32 + 8 * g + 4 * h);
          const float v0 = (acc[j][4 * g] - mu * cs[0]) * sc, v1 = (acc[j][4 * g + 1] - mu * cs[1]) * sc;
          const float v2 = (acc[j][4 * g + 2] - mu * cs[2]) * sc, v3 = (acc[j][4 * g + 3] - mu * cs[3]) * sc;
          const u32x2 o = {cvtpk(v0, v1), cvtpk(v2, v3)};
          const float gn = dot2(o[1], o[1], dot2(o[0], o[0], 0.f));
          gmax = (gn > gmax || gn != gn) ? gn : gmax;
          const int G = g0 + 2 * g;
          const int head = (G * 205) >> 10;
          const int dg = G - head * 5;
          const unsigned off = (unsigned)(head * (64 * 1280) + (dg < 4 ? (dg >> 1) * 512 + r * 16 + (dg & 1) * 8 : 1024 + r * 8));
          st8(base + off, o);
        }
      }
      if (!isq) {
#pragma unroll
        for (int o = 32; o >= 1; o >>= 1) {
          const float t = __shfl_xor(gmax, o);
          gmax = (t > gmax || t != t) ? t : gmax;
        }
        if (lane == 0) *(float*)(ws + WS_KMX + (size_t)((((n0 - 640) >> 6) + nh) * 64 + tq) * 4) = gmax;
      }
    }
  }
  {
    const float sc = rs * __builtin_amdgcn_rsqf(var * rs * rs + LN_EPS);
    const int tok = rb * 128 + mi * 32 + r;
    constexpr float GK1 = -2.0f * 1.4426950408889634f * 0.7978845608028654f, GK2 = GK1 * 0.044715f;
    char* dst = ws + WS_H + ((size_t)(tok >> 4) * 48 + (f0 >> 5) + nh * 2) * 1024 + (h * 16 + (tok & 15)) * 16;
#pragma unroll
    for (int j = 0; j < NHT; ++j)
#pragma unroll
      for (int u = 0; u < 2; ++u) {
        float v[8];
#pragma unroll
        for (int gg = 0; gg < 2; ++gg) {
          const int g = 2 * u + gg;
          const f32x4 cs = CS4(128 + nh * 64 + j * 32 + 8 * g + 4 * h);
          const f32x4 bb = *(const LDS_AS f32x4*)(st_b + nh * 64 + j * 32 + 8 * g + 4 * h);
#pragma unroll
          for (int e = 0; e < 4; ++e) {
            const float t = (ach[j][4 * g + e] - mu * cs[e]) * sc + bb[e];
            const float ex = __builtin_amdgcn_exp2f(t * (GK1 + GK2 * t * t));
            v[4 * gg + e] = t * __builtin_amdgcn_rcpf(1.0f + ex);
          }
        }
        st16(dst + j * 1024 + u * 512, u32x4{cvtpk(v[0], v[1]), cvtpk(v[2], v[3]), cvtpk(v[4], v[5]), cvtpk(v[6], v[7])});
      }
  }
#undef CS4
  STAMP(0, 4);
  asm volatile("s_waitcnt vmcnt(0)" : "+v"(warm) :: "memory");
}

__global__ __launch_bounds__(512, 2) void ka_proj(const char* __restrict__ x, const char* __restrict__ wqkv,
                                                  const char* __restrict__ w1, const unsigned short* __restrict__ b1,
                                                  const char* __restrict__ w2, const char* __restrict__ wo,
                                                  char* __restrict__ ws) {
  extern __shared__ __attribute__((aligned(16))) char smem[];
  lds_ptr lds = (lds_ptr)smem;
  const int bid = blockIdx.x, xg = bid & 7, idx = bid >> 3;
  const int rb = (xg & 1) * 8 + (idx & 7), cb = (xg >> 1) * 4 + (idx >> 3);
  const int n0 = (cb < 15 ? cb : 14) * 128, f0 = cb * 96;
  if (bid == 0 && threadIdx.x < 32) *(unsigned*)(ws + WS_CNT + threadIdx.x * 256) = 0u;
  const int nh = __builtin_amdgcn_readfirstlane(threadIdx.x >> 8);
  if (cb < 10) {
    if (nh) ka_body<0, 1>(lds, x, wqkv, w1, b1, w2, wo, n0, f0, rb, cb, ws);
    else ka_body<0, 0>(lds, x, wqkv, w1, b1, w2, wo, n0, f0, rb, cb, ws);
  } else {
    if (nh) ka_body<1, 1>(lds, x, wqkv, w1, b1, w2, wo, n0, f0, rb, cb, ws);
    else ka_body<1, 0>(lds, x, wqkv, w1, b1, w2, wo, n0, f0, rb, cb, ws);
  }
}

typedef __attribute__((address_space(1))) unsigned gu32;
DI void st8_dev(void* p, u32x2 v) { asm volatile("global_store_dwordx2 %0, %1, off sc1" :: "v"(p), "v"(v) : "memory"); }
DI u32x4 bload16_dev(srd_t r, unsigned voff, unsigned soff) { return __builtin_amdgcn_raw_buffer_load_b128(r, (int)voff, (int)soff, 16); }
DI void ld16_asm(bf16x8& d, unsigned voff, const void* sbase) {
  asm volatile("global_load_dwordx4 %0, %1, %2" : "=v"(d) : "v"(voff), "s"(sbase) : "memory");
}
DI void publish(char* ws, int cnt) {
  asm volatile("s_waitcnt vmcnt(0) lgkmcnt(0)\n\ts_barrier" ::: "memory");
  if (threadIdx.x == 0) __hip_atomic_fetch_add((gu32*)(ws + WS_CNT + cnt * 256), 1u, __ATOMIC_RELAXED, __HIP_MEMORY_SCOPE_AGENT);
}
DI void await(char* ws, int cnt, unsigned want, int wid) {
  if (wid == 0) {
    for (unsigned spins = 0; spins < (1u << 16); ++spins) {
      const unsigned v = __hip_atomic_load((gu32*)(ws + WS_CNT + cnt * 256), __ATOMIC_RELAXED, __HIP_MEMORY_SCOPE_AGENT);
      if ((unsigned)__builtin_amdgcn_readfirstlane(v) >= want) break;
      __builtin_amdgcn_s_sleep(1);
    }
  }
  asm volatile("s_waitcnt vmcnt(0)\n\ts_barrier" ::: "memory");
}

constexpr int AT_TILE = 3584;
constexpr int AT_SLOT = 28672;
constexpr int KB_WR = 2 * AT_SLOT;
constexpr int KB_GRP = 40960;
constexpr int KB_RED = KB_WR + 2 * KB_GRP;
constexpr int KB_LDS = KB_RED + 20480;

DI float nanmax(float a, float b) { return (b > a || b != b) ? b : a; }

DI void attn_store(char* ws, const f32x16& o, const float inv, const int head, const int tok, const int h) {
  char* base = ws + WS_O + (size_t)(tok >> 4) * (20 * 1024) + (tok & 15) * 16;
#pragma unroll
  for (int g = 0; g < 3; ++g) {
    if (g < 2 || h == 0) {
      const int Gc = head * 5 + 2 * g + h;
      const int G8 = Gc & 7;
      const int off = (Gc >> 3) * 1024 + (((G8 >> 2) * 2 + (G8 & 1)) * 256) + ((G8 >> 1) & 1) * 8;
      u32x2 v = {cvtpk(o[4 * g] * inv, o[4 * g + 1] * inv), cvtpk(o[4 * g + 2] * inv, o[4 * g + 3] * inv)};
      st8_dev(base + off, v);
    }
  }
}

__global__ __launch_bounds__(512, 2) void kb_main(const char* __restrict__ x, const char* __restrict__ w2,
                                                  const unsigned short* __restrict__ b2, const char* __restrict__ wo,
                                                  char* __restrict__ ws, char* __restrict__ out) {
  extern __shared__ __attribute__((aligned(16))) char smem[];
  lds_ptr lds = (lds_ptr)smem;
  const int tid = threadIdx.x, lane = tid & 63;
  const int wid = __builtin_amdgcn_readfirstlane(tid >> 6);
  const int r = lane & 31, h = lane >> 5;
  const int xg = blockIdx.x & 7, idx = blockIdx.x >> 3;
  const int head = xg * 4 + (idx >> 3), qb = idx & 7;
  const int rbk = xg * 4 + (idx & 3), cbk = idx >> 2;
  const int mi = wid & 3, kh = wid >> 2, gq = lane >> 4;
  const unsigned lds0 = (unsigned)(uintptr_t)lds;
  const char* Kh = ws + WS_K + (size_t)head * (64 * 1280);
  const char* Vh = ws + WS_V + (size_t)head * (64 * 1280);
  const char* Qt = ws + WS_Q + (size_t)(head * 64 + qb * 8 + wid) * 1280;
  STAMP(1, 0);

  const unsigned vlin = (unsigned)lane * 16u;
  const unsigned vv = (unsigned)(h * 20 + r) * 16u;
  const unsigned vv2 = vv + (unsigned)(WS_V - WS_K) - 1280u - 1536u, vv3 = vv2 + 640u - 1024u;
  const unsigned long long mk16 = 0xffffull, mk20 = 0x000fffff000fffffull;
#define KV_M0(c) SET_M0(lds0 + (unsigned)(((c) & 1) * AT_SLOT + wid * AT_TILE))
#define KV_SRC(c) (Kh + (size_t)((c) * 8 + wid + 1) * 1280)
#define KV_P0(c) asm volatile("global_load_lds_dwordx4 %0, %1" :: "v"(vlin), "s"(KV_SRC(c)) : "memory")
#define KV_P1(c) asm volatile("s_mov_b64 exec, %2\n\tglobal_load_lds_dwordx4 %0, %1 offset:1024\n\ts_mov_b64 exec, -1" :: "v"(vlin), "s"(KV_SRC(c)), "s"(mk16) : "memory")
#define KV_P23(c) asm volatile("s_mov_b64 exec, %3\n\tglobal_load_lds_dwordx4 %0, %2 offset:1536\n\tglobal_load_lds_dwordx4 %1, %2 offset:2560\n\ts_mov_b64 exec, -1" :: "v"(vv2), "v"(vv3), "s"(KV_SRC(c)), "s"(mk20) : "memory")
  bf16x8 qa = *(const bf16x8*)(Qt + lane * 16);
  u32x2 qbv = *(const u32x2*)(Qt + 1024 + r * 8);
  bf16x8 ka0 = *(const bf16x8*)(Kh + lane * 16);
  u32x2 kb0 = *(const u32x2*)(Kh + 1024 + r * 8);
  float kmax = nanmax(*(const float*)(ws + WS_KMX + (size_t)(((head * 20) >> 6) * 64 + lane) * 4),
                      *(const float*)(ws + WS_KMX + (size_t)(((head * 20 + 19) >> 6) * 64 + lane) * 4));
  SBAR();
  KV_M0(0); KV_P0(0); KV_P1(0); KV_P23(0);
#pragma unroll
  for (int sl = 0; sl < 2; ++sl) {
    lds_ptr tp = lds + sl * AT_SLOT + wid * AT_TILE;
    if (lane < 32) *(LDS_AS u32x2*)(tp + 1024 + 256 + lane * 8) = u32x2{0x00003f80u, 0u};
    if (lane < 48) {
      const int blk4 = (lane * 43) >> 9, dd = 20 + lane - blk4 * 12;
      const unsigned val = dd == 20 ? 0x3f803f80u : 0u;
      *(LDS_AS u32x4*)(tp + 1536 + blk4 * 512 + dd * 16) = u32x4{val, val, val, val};
    }
  }
  unsigned vo[5];
#pragma unroll
  for (int i = 0; i < 5; ++i) {
    const int p = i * 64 + lane, row = (p * 6554) >> 16;
    vo[i] = (unsigned)(row * 1280 + (p - row * 10) * 16 - (i & 3) * 1024);
  }
#define BABL_W 0
#define W_SRC(W, grp) ((W) + cbk * 160 + (size_t)((grp) * 256 + wid * 32) * 1280)
#define W_M0(reg, hi) SET_M0(lds0 + (unsigned)(KB_WR + (reg) * KB_GRP + wid * 5120 + (hi) * 4096))
#define W_P(W, i, grp) if (!(BABL_W)) asm volatile("global_load_lds_dwordx4 %0, %1 offset:%2" :: "v"(vo[i]), "s"(W_SRC(W, grp)), "i"(((i) & 3) * 1024) : "memory")
  const char* hsrc = ws + WS_H + ((size_t)(rbk * 4 + mi) * 48 + kh) * 1024 + 4096;
#define H_LOAD(D, j, grp) asm volatile("global_load_dwordx4 %0, %1, %2 offset:%3" : "=v"(D[j]) : "v"(vlin), "s"(hsrc + (grp) * 8192), "i"((j) * 2048 - 4096) : "memory")
  const unsigned wra = lds0 + (unsigned)(KB_WR + (kh * 32 + 4 * ((gq & 1) + 4 * (gq >> 1)) + ((lane & 15) >> 2)) * 160 + (lane & 3) * 8);
  const unsigned wrb = wra + KB_GRP;

#pragma unroll
  for (int o = 32; o >= 1; o >>= 1) kmax = nanmax(kmax, __shfl_xor(kmax, o));
  {
    const u32x4 a = __builtin_bit_cast(u32x4, qa);
    float n0 = dot2(a[0], a[0], 0.f), n1 = dot2(a[1], a[1], 0.f);
    n0 = dot2(a[2], a[2], n0); n1 = dot2(a[3], a[3], n1);
    if (h == 0) { n0 = dot2(qbv[0], qbv[0], n0); n1 = dot2(qbv[1], qbv[1], n1); }
    const float qn = x32_add(n0 + n1);
    const float bound = __builtin_amdgcn_sqrtf(qn * kmax * 5.0f) * 1.01f;
    if (h == 1) { qbv[0] = cvtpk(-bound, 0.f); qbv[1] = 0u; kb0[0] = 0x00003f80u; kb0[1] = 0u; }
  }
  const bf16x4 qb4 = __builtin_bit_cast(bf16x4, qbv);
  f32x16 o;
#pragma unroll
  for (int i = 0; i < 16; ++i) o[i] = 0.f;
  const f32x16 zero = o;
  f32x16 sA = MFMA32(ka0, qa, zero);
  sA = MFMA32K8(__builtin_bit_cast(bf16x4, kb0), qb4, sA);
  f32x16 sB;
  f32x4 acc[5];
#pragma unroll
  for (int j = 0; j < 5; ++j) acc[j] = f32x4{0.f, 0.f, 0.f, 0.f};
  WAIT_BAR(0);
  STAMP(1, 1);
  STAMPC(1, 14);
  STAMP2(2, 0);

  unsigned a16 = lds0 + vlin, a8 = lds0 + (unsigned)lane * 8u;
  int sdelta = AT_SLOT;
  bf16x8 kaA, v0A, v1A, kaB, v0B, v1B;
  bf16x4 kbA, kbB;
  bf16x8 hA[4], hB[4];
  bf16x4 wl[5], wu[5];
  u32x4 p0, p1 = {0u, 0u, 0u, 0u};
  v1B = __builtin_bit_cast(bf16x8, p1);
#define RD_KA(X, j) ka##X = *(const LDS_AS bf16x8*)(LP(a16) + (j) * AT_TILE)
#define RD_KB(X, j) kb##X = *(const LDS_AS bf16x4*)(LP(a8) + (j) * AT_TILE + 1024)
#define RD_V0(X, j) v0##X = *(const LDS_AS bf16x8*)(LP(a16) + (j) * AT_TILE + 1536)
#define RD_V1(X, j) v1##X = *(const LDS_AS bf16x8*)(LP(a16) + (j) * AT_TILE + 2560)
#define W_LO(WA, j, t) wl[t] = tr_read(LP(WA) + (j) * 10240 + (t) * 32)
#define W_HI(WA, j, t) wu[t] = tr_read(LP(WA) + (j) * 10240 + (t) * 32 + 1280)
#define BABL 0
#define G_MM(HS, j, t) do { if (BABL & 1) { PIN(wl[t]); PIN(wu[t]); PIN(HS[j]); } else { acc[t] = MFMA16(cat8(wl[t], wu[t]), HS[j], acc[t]); } } while (0)
#define EX2(S, i) do { S[i] = __builtin_amdgcn_exp2f(S[i]); S[i + 1] = __builtin_amdgcn_exp2f(S[i + 1]); } while (0)
#define MID() do { __builtin_amdgcn_sched_group_barrier(0x400, 2, 0); __builtin_amdgcn_sched_group_barrier(0x008, 1, 0); \
                    __builtin_amdgcn_sched_group_barrier(0x400, 2, 0); __builtin_amdgcn_sched_group_barrier(0x002, 2, 0); } while (0)
#define AT_STEP(SC, SN, X, Y, JN, PRE, WP, A0, A1, A2, A3, A4, A5, V0_, V1_, V2_, M1, M2, M3)            \
  do {                                                                                                   \
    PRE;                                                                                                 \
    asm volatile("" : "+v"(ka##X), "+v"(kb##X), "+v"(v0##X), "+v"(v1##X)); WP; SBAR();                   \
    EX2(SC, 0); PIN(SC); SBAR();                                                                         \
    o = MFMA32(v1##Y, __builtin_bit_cast(bf16x8, p1), o); SBAR();                                        \
    RD_KA(Y, JN); RD_KB(Y, JN); RD_V0(Y, JN); RD_V1(Y, JN); A0; A1; A2; A3; A4; A5; V0_; V1_; V2_; SBAR(); \
    M1; EX2(SC, 2); EX2(SC, 4); p0[0] = cvtpk(SC[0], SC[1]); p0[1] = cvtpk(SC[2], SC[3]); MID();         \
    asm volatile("" : "+v"(p0), "+v"(SC), "+v"(acc[0]), "+v"(acc[3])); SBAR();                           \
    SN = MFMA32(ka##X, qa, zero); SBAR();                                                                \
    M2; EX2(SC, 6); EX2(SC, 8); p0[2] = cvtpk(SC[4], SC[5]); p0[3] = cvtpk(SC[6], SC[7]); MID();         \
    asm volatile("" : "+v"(p0), "+v"(SC), "+v"(acc[1])); SBAR();                                         \
    SN = MFMA32K8(kb##X, qb4, SN); SBAR();                                                               \
    M3; EX2(SC, 10); EX2(SC, 12); p1[0] = cvtpk(SC[8], SC[9]); p1[1] = cvtpk(SC[10], SC[11]); MID();     \
    asm volatile("" : "+v"(p1), "+v"(SC), "+v"(acc[2]), "+v"(acc[4])); SBAR();                           \
    o = MFMA32(v0##X, __builtin_bit_cast(bf16x8, p0), o); SBAR();                                        \
    EX2(SC, 14); p1[2] = cvtpk(SC[12], SC[13]); p1[3] = cvtpk(SC[14], SC[15]); PIN(p1); SBAR();          \
  } while (0)
#define WP_E asm volatile("" : "+v"(wl[0]), "+v"(wu[0]), "+v"(wl[1]), "+v"(wu[1]), "+v"(wl[2]), "+v"(wu[2]))
#define WP_O asm volatile("" : "+v"(wl[3]), "+v"(wu[3]), "+v"(wl[4]), "+v"(wu[4]))
#define FLIP() do { a16 += (unsigned)sdelta; a8 += (unsigned)sdelta; sdelta = -sdelta; } while (0)
#define SYNC_H(HN) asm volatile("s_waitcnt vmcnt(3) lgkmcnt(0)\n\ts_barrier" : "+v"(HN[0]) :: "memory")
#define WAIT_H(HC, j, N) asm volatile("s_waitcnt vmcnt(" #N ")" : "+v"(HC[j]) :: "memory")
#define STEP_E(JN, PRE, WC, HC, j, X0, X1, V0_, V1_)                                                     \
  AT_STEP(sA, sB, A, B, JN, PRE, WP_E, W_LO(WC, j, 3), W_HI(WC, j, 3), W_LO(WC, j, 4), W_HI(WC, j, 4), X0, X1, V0_, V1_, NOP_, \
          G_MM(HC, j, 0), G_MM(HC, j, 1), G_MM(HC, j, 2))
#define STEP_O(JN, PRE, WA, jn, HC, j, X0, V0_, V1_)                                                     \
  AT_STEP(sB, sA, B, A, JN, PRE, WP_O, W_LO(WA, jn, 0), W_HI(WA, jn, 0), W_LO(WA, jn, 1), W_HI(WA, jn, 1), W_LO(WA, jn, 2), \
          W_HI(WA, jn, 2), X0, V0_, V1_, G_MM(HC, j, 3), NOP_, G_MM(HC, j, 4))
#define CHUNK_G(c, WC, HC, WN, HN, RN, NW, NG, HG)                                                       \
  do {                                                                                                   \
    STEP_E(1, NOP_, WC, HC, 0, W_M0(RN, 0), W_P(NW, 0, NG), W_P(NW, 1, NG), W_P(NW, 2, NG));             \
    STEP_O(2, NOP_, WC, 1, HC, 0, W_P(NW, 3, NG), W_M0(RN, 1), W_P(NW, 4, NG));                          \
    STEP_E(3, WAIT_H(HC, 1, 7), WC, HC, 1, KV_M0((c) + 1), KV_P0((c) + 1), KV_P1((c) + 1), KV_P23((c) + 1)); \
    STEP_O(4, NOP_, WC, 2, HC, 1, H_LOAD(HN, 0, HG), H_LOAD(HN, 1, HG), H_LOAD(HN, 2, HG));              \
    STEP_E(5, WAIT_H(HC, 2, 13), WC, HC, 2, H_LOAD(HN, 3, HG), NOP_, NOP_, NOP_);                        \
    STEP_O(6, NOP_, WC, 3, HC, 2, NOP_, NOP_, NOP_);                                                     \
    STEP_E(7, WAIT_H(HC, 3, 13), WC, HC, 3, NOP_, NOP_, NOP_, NOP_);                                     \
    STEP_O(0, SYNC_H(HN); STAMP2(2, 1 + (c)); FLIP(), WN, 0, HC, 3, NOP_, NOP_, NOP_);                   \
  } while (0)
#define PLAIN_E(JN, X0, X1, X2, X3) AT_STEP(sA, sB, A, B, JN, NOP_, NOP_, X0, X1, X2, X3, NOP_, NOP_, NOP_, NOP_, NOP_, NOP_, NOP_, NOP_)
#define PLAIN_O(JN, X0, X1, X2, X3) AT_STEP(sB, sA, B, A, JN, NOP_, NOP_, X0, X1, X2, X3, NOP_, NOP_, NOP_, NOP_, NOP_, NOP_, NOP_, NOP_)

  RD_KA(A, 0); RD_KB(A, 0); RD_V0(A, 0); RD_V1(A, 0);
  PLAIN_E(1, W_M0(0, 0), W_P(w2, 0, 0), W_P(w2, 1, 0), W_P(w2, 2, 0));
  PLAIN_O(2, W_P(w2, 3, 0), W_M0(0, 1), W_P(w2, 4, 0), KV_M0(1));
  PLAIN_E(3, KV_P0(1), KV_P1(1), KV_P23(1), NOP_);
  PLAIN_O(4, H_LOAD(hA, 0, 0), H_LOAD(hA, 1, 0), H_LOAD(hA, 2, 0), H_LOAD(hA, 3, 0));
  PLAIN_E(5, NOP_, NOP_, NOP_, NOP_);
  PLAIN_O(6, NOP_, NOP_, NOP_, NOP_);
  PLAIN_E(7, NOP_, NOP_, NOP_, NOP_);
  AT_STEP(sB, sA, B, A, 0, SYNC_H(hA); STAMP2(2, 1); FLIP(), NOP_, W_LO(wra, 0, 0), W_HI(wra, 0, 0), W_LO(wra, 0, 1), W_HI(wra, 0, 1),
          W_LO(wra, 0, 2), W_HI(wra, 0, 2), NOP_, NOP_, NOP_, NOP_, NOP_, NOP_);
#pragma unroll 1
  for (int c = 1; c < 7; c += 2) {
    CHUNK_G(c, wra, hA, wrb, hB, 1, w2, c, c);
    const char* wn = c < 5 ? w2 : wo;
    CHUNK_G(c + 1, wrb, hB, wra, hA, 0, wn, (c < 5 ? c + 1 : 0), (c < 5 ? c + 1 : 5));
  }
  const int tok = rbk * 64 + mi * 16 + (lane & 15);
  const size_t xo = (size_t)tok * 1280 + (cbk * 80 + 4 * gq) * 2;
  LDS_AS f32x4* red = (LDS_AS f32x4*)(lds + KB_RED) + (mi * 5) * 64 + lane;
  u32x2 gv[5], xv[5];
  PLAIN_E(1, NOP_,
          _Pragma("unroll") for (int j = 0; j < 5; ++j) gv[j] = *(const u32x2*)(b2 + cbk * 80 + j * 16 + 4 * gq),
          _Pragma("unroll") for (int j = 0; j < 5; ++j) xv[j] = *(const u32x2*)(x + xo + j * 32),
          if (kh == 1) { _Pragma("unroll") for (int j = 0; j < 5; ++j) red[j * 64] = acc[j]; });
  PLAIN_O(2, NOP_, NOP_, NOP_, NOP_);
  PLAIN_E(3, NOP_, NOP_, NOP_, NOP_);
  PLAIN_O(4, asm volatile("s_waitcnt vmcnt(0)" : "+v"(hA[1]), "+v"(hA[2]), "+v"(hA[3]) :: "memory");
          _Pragma("unroll") for (int j = 0; j < 5; ++j) { PIN(gv[j]); PIN(xv[j]); } W_M0(1, 0), W_P(wo, 0, 1), W_P(wo, 1, 1), W_P(wo, 2, 1));
  PLAIN_E(5, W_P(wo, 3, 1), W_M0(1, 1), W_P(wo, 4, 1), NOP_);
  PLAIN_O(6, NOP_, NOP_, NOP_, NOP_);
  PLAIN_E(7, NOP_, NOP_, NOP_, NOP_);
  PLAIN_O(7, NOP_, NOP_, NOP_, NOP_);
  o = MFMA32(v1B, __builtin_bit_cast(bf16x8, p1), o);

  STAMP(1, 3);
  STAMPC(1, 15);
  STAMP2(2, 8);
  const int qtok = qb * 256 + wid * 32 + r;
  float l = __shfl(o[8], 32 + r);
  if (__builtin_expect(__any(!(l >= 7.888609e-31f) || !(l < 3.0e38f)), 0)) {
    if (h == 1) { qbv[0] = 0u; }
    const bf16x4 qz = __builtin_bit_cast(bf16x4, qbv);
    float m = -3.0e38f;
#pragma unroll
    for (int i = 0; i < 16; ++i) o[i] = 0.f;
    const unsigned onev = r == 20 ? 0x3f803f80u : 0u;
#pragma unroll 1
    for (int t = 0; t < 64; ++t) {
      const bf16x8 ka = *(const bf16x8*)(Kh + t * 1280 + lane * 16);
      u32x2 kb = *(const u32x2*)(Kh + t * 1280 + 1024 + r * 8);
      u32x4 v0 = {onev, onev, onev, onev}, v1 = v0;
      if (r < 20) {
        v0 = *(const u32x4*)(Vh + t * 1280 + vv);
        v1 = *(const u32x4*)(Vh + t * 1280 + 640 + vv);
      }
      f32x16 s = MFMA32(ka, qa, zero);
      s = MFMA32K8(__builtin_bit_cast(bf16x4, kb), qz, s);
      float tm = s[0];
#pragma unroll
      for (int i = 1; i < 16; ++i) tm = fmaxf(tm, s[i]);
      tm = fmaxf(tm, xhalf(tm));
      const float mn = fmaxf(m, tm);
      const float al = __builtin_amdgcn_exp2f(m - mn);
      m = mn;
      u32x4 q0, q1;
#pragma unroll
      for (int i = 0; i < 16; ++i) { o[i] *= al; s[i] = __builtin_amdgcn_exp2f(s[i] - mn); }
#pragma unroll
      for (int e = 0; e < 4; ++e) { q0[e] = cvtpk(s[2 * e], s[2 * e + 1]); q1[e] = cvtpk(s[8 + 2 * e], s[9 + 2 * e]); }
      o = MFMA32(__builtin_bit_cast(bf16x8, v0), __builtin_bit_cast(bf16x8, q0), o);
      o = MFMA32(__builtin_bit_cast(bf16x8, v1), __builtin_bit_cast(bf16x8, q1), o);
    }
    l = __shfl(o[8], 32 + r);
  }
  attn_store(ws, o, __builtin_amdgcn_rcpf(l), head, qtok, h);
  publish(ws, qb);
  STAMP(1, 4);
  if (kh == 0) {
#pragma unroll
    for (int j = 0; j < 5; ++j) {
      const f32x4 og = red[j * 64];
      const u32x2 bb = gv[j];
      float t[4] = {acc[j][0] + og[0] + bf_lo(bb[0]), acc[j][1] + og[1] + bf_hi(bb[0]), acc[j][2] + og[2] + bf_lo(bb[1]),
                    acc[j][3] + og[3] + bf_hi(bb[1])};
#pragma unroll
      for (int e = 0; e < 4; ++e)
        t[e] = t[e] * __builtin_amdgcn_rcpf(1.0f + __builtin_amdgcn_exp2f(t[e] * -1.4426950408889634f));
      gv[j] = u32x2{cvtpk(t[0], t[1]), cvtpk(t[2], t[3])};
      PIN(gv[j]);
    }
  }
  {
    const char* s_ = wo + cbk * 160 + (size_t)(512 + wid * 16) * 1280;
    SET_M0(lds0 + (unsigned)(wid * 2560));
    asm volatile("s_nop 0\n\tglobal_load_lds_dwordx4 %0, %3\n\tglobal_load_lds_dwordx4 %1, %3 offset:1024\n\t"
                 "s_mov_b32 exec_hi, 0\n\tglobal_load_lds_dwordx4 %2, %3 offset:2048\n\ts_mov_b64 exec, -1"
                 :: "v"(vo[0]), "v"(vo[1]), "v"(vo[2]), "s"(s_) : "memory");
  }
#pragma unroll
  for (int j = 0; j < 5; ++j) acc[j] = f32x4{0.f, 0.f, 0.f, 0.f};
  STAMP(1, 6);
  await(ws, rbk >> 2, 32u, wid);
  STAMP(1, 7);
  {
    const srd_t ro = make_srd(ws + WS_O, (unsigned)(WS_G - WS_O));
    const unsigned oa = (unsigned)(((rbk * 4 + mi) * 20 + kh) * 1024);
    u32x4 af[10];
#pragma unroll
    for (int c = 0; c < 10; ++c) af[c] = bload16_dev(ro, vlin, oa + c * 2048u);
    SBAR();
#pragma unroll
    for (int c = 0; c < 10; ++c) {
      const unsigned wa = c < 8 ? wra : wra - (unsigned)KB_WR;
      const int cc = c < 8 ? c : c - 8;
#pragma unroll
      for (int t = 0; t < 5; ++t) { W_LO(wa, cc, t); W_HI(wa, cc, t); }
#pragma unroll
      for (int t = 0; t < 5; ++t) acc[t] = MFMA16(cat8(wl[t], wu[t]), __builtin_bit_cast(bf16x8, af[c]), acc[t]);
    }
  }
  if (kh == 1) {
#pragma unroll
    for (int j = 0; j < 5; ++j) red[j * 64] = acc[j];
  }
  LBAR(0);
  if (kh == 0) {
#pragma unroll
    for (int j = 0; j < 5; ++j) {
      const f32x4 og = red[j * 64];
      const float r0 = bf_lo(xv[j][0]) + (acc[j][0] + og[0]) * bf_lo(gv[j][0]);
      const float r1 = bf_hi(xv[j][0]) + (acc[j][1] + og[1]) * bf_hi(gv[j][0]);
      const float r2 = bf_lo(xv[j][1]) + (acc[j][2] + og[2]) * bf_lo(gv[j][1]);
      const float r3 = bf_hi(xv[j][1]) + (acc[j][3] + og[3]) * bf_hi(gv[j][1]);
      *(u32x2*)(out + xo + j * 32) = u32x2{cvtpk(r0, r1), cvtpk(r2, r3)};
    }
  }
  STAMP(1, 8);
}

extern "C" void kernel_launch(void* const* d_in, const int* in_sizes, int n_in, void* d_out, int out_size, void* d_ws,
                              size_t ws_size, hipStream_t stream) {
  (void)in_sizes; (void)n_in; (void)out_size; (void)ws_size;
  const char* x = (const char*)d_in[0];
  const char* wqkv = (const char*)d_in[1];
  const char* wo = (const char*)d_in[2];
  const char* w1 = (const char*)d_in[4];
  const unsigned short* b1 = (const unsigned short*)d_in[5];
  const char* w2 = (const char*)d_in[6];
  const unsigned short* b2 = (const unsigned short*)d_in[7];
  char* ws = (char*)d_ws;
  (void)hipFuncSetAttribute((const void*)ka_proj, hipFuncAttributeMaxDynamicSharedMemorySize, KA_LDS);
  (void)hipFuncSetAttribute((const void*)kb_main, hipFuncAttributeMaxDynamicSharedMemorySize, KB_LDS);
  ka_proj<<<256, 512, KA_LDS, stream>>>(x, wqkv, w1, b1, w2, wo, ws);
  kb_main<<<256, 512, KB_LDS, stream>>>(x, w2, b2, wo, ws, (char*)d_out);
}
```

```cpp
#include <hip/hip_runtime.h>
#include <hip/hip_bf16.h>
#include <stdint.h>

typedef __attribute__((ext_vector_type(8))) short bf16x8;
typedef __attribute__((ext_vector_type(4))) short bf16x4;
typedef __attribute__((ext_vector_type(16))) float f32x16;
typedef __attribute__((ext_vector_type(4))) float f32x4;
typedef __attribute__((ext_vector_type(2))) float f32x2;
typedef __attribute__((ext_vector_type(4))) unsigned u32x4;
typedef __attribute__((ext_vector_type(2))) unsigned u32x2;
typedef __attribute__((ext_vector_type(2))) __bf16 bf2;
typedef __attribute__((address_space(3))) char* lds_ptr;
#define LDS_AS __attribute__((address_space(3)))
#define DI __device__ __forceinline__

#define MFMA32(a, b, c) __builtin_amdgcn_mfma_f32_32x32x16_bf16((a), (b), (c), 0, 0, 0)
#define MFMA32K8(a, b, c) __builtin_amdgcn_mfma_f32_32x32x8bf16_1k((a), (b), (c), 0, 0, 0)
#define MFMA16(a, b, c) __builtin_amdgcn_mfma_f32_16x16x32_bf16((a), (b), (c), 0, 0, 0)
#define SBAR() __builtin_amdgcn_sched_barrier(0)
#define PIN(x) asm volatile("" : "+v"(x))
#define WAIT_BAR(N) asm volatile("s_waitcnt vmcnt(" #N ") lgkmcnt(0)\n\ts_barrier" ::: "memory")

constexpr int S_ = 2048, D_ = 640, NH = 32, F_ = 1536;
constexpr float LN_EPS = 1e-5f;
constexpr float QSCALE = 0.22360679774997896f * 1.4426950408889634f;

constexpr size_t WS_Q = 0;
constexpr size_t WS_K = WS_Q + (size_t)NH * 64 * 1280;
constexpr size_t WS_V = WS_K + (size_t)NH * 64 * 1280;
constexpr size_t WS_H = WS_V + (size_t)NH * 64 * 1280;
constexpr size_t WS_O = WS_H + (size_t)128 * 48 * 1024;
constexpr size_t WS_G = WS_O + (size_t)128 * 20 * 1024;
constexpr size_t WS_RS = WS_G + (size_t)S_ * D_ * 2;
constexpr size_t WS_CS1 = WS_RS + (size_t)S_ * 8;
constexpr size_t WS_KMX = WS_CS1 + (size_t)F_ * 4;
constexpr size_t WS_CNT = WS_KMX + 10 * 64 * 4;
constexpr size_t WS_END = WS_CNT + 32 * 256;

#define STAMP(kern, i) do { } while (0)
#define STAMPC(kern, i) do { } while (0)
#define STAMP2(kern, i) do { } while (0)

#define STPOL 2
DI void st16(void* p, u32x4 v) {
  asm volatile("global_store_dwordx4 %0, %1, off sc1\n\ts_nop 1" :: "v"(p), "v"(v) : "memory");
}
DI void st8(void* p, u32x2 v) {
  asm volatile("global_store_dwordx2 %0, %1, off sc1" :: "v"(p), "v"(v) : "memory");
}
DI unsigned cvtpk(float lo, float hi) {
  f32x2 f = {lo, hi};
  return __builtin_bit_cast(unsigned, __builtin_convertvector(f, bf2));
}
DI float bf_lo(unsigned u) { return __uint_as_float(u << 16); }
DI float bf_hi(unsigned u) { return __uint_as_float(u & 0xffff0000u); }
DI float dot2(unsigned a, unsigned b, float c) {
  return __builtin_amdgcn_fdot2_f32_bf16(__builtin_bit_cast(bf2, a), __builtin_bit_cast(bf2, b), c, false);
}
DI float xhalf(float v) {
  return __shfl_xor(v, 32);
}
DI void glds16(unsigned voff, const void* sbase, unsigned ldsdst) {
  unsigned keep;
  asm volatile("s_mov_b32 %0, m0\n\ts_mov_b32 m0, %3\n\ts_nop 0\n\tglobal_load_lds_dwordx4 %1, %2\n\ts_mov_b32 m0, %0"
               : "=&s"(keep) : "v"(voff), "s"(sbase), "s"(ldsdst) : "memory");
}
DI void glds16_masked(unsigned voff, const void* sbase, unsigned ldsdst, unsigned mlo, unsigned mhi) {
  unsigned keep;
  asm volatile("s_mov_b32 %0, m0\n\ts_mov_b32 m0, %3\n\ts_mov_b32 exec_lo, %4\n\ts_mov_b32 exec_hi, %5\n\t"
               "global_load_lds_dwordx4 %1, %2\n\ts_mov_b64 exec, -1\n\ts_mov_b32 m0, %0"
               : "=&s"(keep) : "v"(voff), "s"(sbase), "s"(ldsdst), "s"(mlo), "s"(mhi) : "memory");
}
typedef short v4i16_t __attribute__((ext_vector_type(4)));
DI bf16x4 tr_read(lds_ptr p) {
  return __builtin_bit_cast(bf16x4, __builtin_amdgcn_ds_read_tr16_b64_v4i16((LDS_AS v4i16_t*)p));
}
DI bf16x8 cat8(bf16x4 a, bf16x4 b) { return __builtin_shufflevector(a, b, 0, 1, 2, 3, 4, 5, 6, 7); }

constexpr int KA_BUF = 45056;
constexpr int KA_STAT = 3 * KA_BUF;
constexpr int KA_SCS = KA_STAT + 2048, KA_SB1 = KA_SCS + 3584, KA_SMR = KA_SB1 + 384;
constexpr int KA_LDS = KA_SMR + 2048;
#define KABL 0
#define CODE_PF 1
#define LBAR(N) asm volatile("s_waitcnt lgkmcnt(" #N ")\n\ts_barrier" ::: "memory")
#define LP(a) ((lds_ptr)(uintptr_t)(a))
#define SET_M0(v) asm volatile("s_mov_b32 m0, %0\n\ts_nop 0" :: "s"(v) : "memory")
#define NOP_ do { } while (0)
#define MFMA4(a, b, c) __builtin_amdgcn_mfma_f32_4x4x4bf16_1k((a), (b), (c), 0, 0, 0)

typedef __amdgpu_buffer_rsrc_t srd_t;
DI srd_t make_srd(const void* p, unsigned bytes) { return __builtin_amdgcn_make_buffer_rsrc((void*)p, (short)0, (int)bytes, 0x00020000); }
DI float x32_add(float v) {
  const auto rr = __builtin_amdgcn_permlane32_swap(__float_as_uint(v), __float_as_uint(v), false, false);
  return __uint_as_float(rr[0]) + __uint_as_float(rr[1]);
}
DI float nanmax(float a, float b) { return (b > a || b != b) ? b : a; }
template <int CTRL> DI float dpp_get(float v) { return __uint_as_float(__builtin_amdgcn_update_dpp(0u, __float_as_uint(v), CTRL, 0xf, 0xf, true)); }
DI float wave_nanmax(float v) {
  v = nanmax(v, dpp_get<0xB1>(v));
  v = nanmax(v, dpp_get<0x4E>(v));
  v = nanmax(v, dpp_get<0x141>(v));
  v = nanmax(v, dpp_get<0x140>(v));
  const auto a = __builtin_amdgcn_permlane16_swap(__float_as_uint(v), __float_as_uint(v), false, false);
  v = nanmax(__uint_as_float(a[0]), __uint_as_float(a[1]));
  const auto b = __builtin_amdgcn_permlane32_swap(__float_as_uint(v), __float_as_uint(v), false, false);
  return nanmax(__uint_as_float(b[0]), __uint_as_float(b[1]));
}
DI bf16x4 lo4(bf16x8 v) { return __builtin_shufflevector(v, v, 0, 1, 2, 3); }
DI bf16x4 hi4(bf16x8 v) { return __builtin_shufflevector(v, v, 4, 5, 6, 7); }

template <int TYPE, int nh>
DI void ka_body(lds_ptr lds, const char* __restrict__ x, const char* __restrict__ wqkv, const char* __restrict__ wh1,
                const unsigned short* __restrict__ b1, const char* __restrict__ w2, const char* __restrict__ wo,
                const int n0, const int f0, const int rb, const int cb, char* __restrict__ ws,
                const unsigned long long pc, const unsigned span) {
  constexpr int NHT = nh ? 1 : 2;
  const int tid = threadIdx.x, lane = tid & 63;
  const int w = __builtin_amdgcn_readfirstlane(tid >> 6);
  const int mi = w & 3;
  const int r = lane & 31, h = lane >> 5;
  const unsigned lds0 = (unsigned)(uintptr_t)lds;
  STAMP(0, 0);
  const unsigned vx0 = (unsigned)((lane >> 3) * 1280 + (((lane & 7) ^ (lane >> 4)) << 4));
  const unsigned vx1 = (unsigned)((8 + (lane >> 3)) * 1280 + (((lane & 7) ^ (4 + (lane >> 4))) << 4) - 1024);
  const unsigned vw = (unsigned)((lane >> 3) * 3840 + ((((((lane >> 2) ^ (lane >> 4)) & 1) << 2) | (lane & 3)) << 4));
  const int p0 = w * 96 + lane, p1 = w * 96 + 64 + r;
  const int r0 = (p0 * 5462) >> 16, r1 = (p1 * 5462) >> 16;
  const unsigned vh0 = (unsigned)(r0 * 3072 + (p0 - r0 * 12) * 16), vh1 = (unsigned)(r1 * 3072 + (p1 - r1 * 12) * 16 - 1024);
  const char* xs = x + (size_t)(rb * 128 + 16 * w) * 1280;
  const char* wsrc = wqkv + ((size_t)(8 * w) * 1920 + n0) * 2;
  const char* hsrc = wh1 + f0 * 2;
  const unsigned mx = lds0 + (unsigned)w * 2048u, mw = lds0 + 16384u + (unsigned)w * 1024u, mh = lds0 + 32768u + (unsigned)w * 1536u;
#define KA_ROT 0
  const int rot = KA_ROT ? (rb * 3 + cb * 7) % 10 : 0;
#define KC(c) ((c) + rot >= 10 ? (c) + rot - 10 : (c) + rot)
#define KA_DX(c, buf) do { SET_M0(mx + (unsigned)(buf)); asm volatile("global_load_lds_dwordx4 %0, %2\n\tglobal_load_lds_dwordx4 %1, %2 offset:1024" :: "v"(vx0), "v"(vx1), "s"(xs + KC(c) * 128) : "memory"); } while (0)
#define KA_DW(c, buf, hf) do { SET_M0(mw + (unsigned)(buf) + (hf) * 8192u); asm volatile("global_load_lds_dwordx4 %0, %1" :: "v"(vw), "s"(wsrc + (size_t)KC(c) * (64 * 3840) + (hf) * 128) : "memory"); } while (0)
#define KA_DH(c, buf) do { SET_M0(mh + (unsigned)(buf)); asm volatile("global_load_lds_dwordx4 %0, %2\n\ts_mov_b32 exec_hi, 0\n\tglobal_load_lds_dwordx4 %1, %2 offset:1024\n\ts_mov_b32 exec_hi, -1" :: "v"(vh0), "v"(vh1), "s"(hsrc + (size_t)KC(c) * (64 * 3072)) : "memory"); } while (0)
#define KA_ISSUE(c, buf) do { KA_DX(c, buf); KA_DW(c, buf, 0); KA_DW(c, buf, 1); KA_DH(c, buf); } while (0)
  unsigned braw = 0, warm = 0;
  if (nh == 0 && tid < 96) braw = b1[f0 + tid];
  SBAR();
  KA_ISSUE(0, 0);
  unsigned cpf = 0;
  if (CODE_PF) {
    const unsigned off = (unsigned)tid * 128u < span ? (unsigned)tid * 128u : 0u;
    asm volatile("global_load_dword %0, %1, %2" : "=v"(cpf) : "v"(off), "s"(pc) : "memory");
  }
  KA_ISSUE(1, KA_BUF);
  KA_ISSUE(2, 2 * KA_BUF);
#define KA_WARM 0
  unsigned warm2 = 0;
  if (KA_WARM) {
    const char* p;
    if (tid < 224) { const int l = (cb & 3) * 224 + tid; p = x + (size_t)(rb * 128 + (l & 127)) * 1280 + (3 + (l >> 7)) * 128; }
    else if (tid < 336) { const int l = (rb & 7) * 112 + tid - 224; p = wqkv + (size_t)(192 + (l >> 1)) * 3840 + n0 * 2 + (l & 1) * 128; }
    else { const int l = (rb & 7) * 112 + (tid < 448 ? tid - 336 : 0); p = wh1 + (size_t)(192 + (l >> 1)) * 3072 + f0 * 2 + (l & 1) * 128; }
    asm volatile("global_load_dword %0, %1, off" : "=v"(warm2) : "v"(p) : "memory");
  }

  unsigned xa[4], wa0[4], ha[4];
  {
    const unsigned wb = lds0 + (unsigned)(16384 + nh * 8192 + (8 * h + ((lane & 15) >> 2)) * 128 + 64 * ((lane >> 3) & 1) +
                                          (16 * ((lane >> 4) & 1) + 4 * (lane & 3)) * 2);
    const unsigned hb = lds0 + (unsigned)(32768 + (8 * h + ((lane & 15) >> 2)) * 192 + nh * 128 + (16 * ((lane >> 4) & 1) + 4 * (lane & 3)) * 2);
#pragma unroll
    for (int i = 0; i < 4; ++i) {
      const int ks = i ^ mi;
      xa[i] = lds0 + (unsigned)((mi * 32 + r) * 128 + (((2 * ks + h) ^ ((r >> 1) & 7)) << 4));
      wa0[i] = wb + (unsigned)(ks * 2048);
      ha[i] = hb + (unsigned)(ks * 3072);
    }
  }
  const int tq = rb * 4 + mi;
  const bool isq = n0 < 640;
  unsigned so[TYPE == 1 ? 2 : 8];
  if (TYPE == 1) {
#pragma unroll
    for (int j = 0; j < 2; ++j) {
      const int nn = n0 - 1280 + nh * 64 + j * 32 + r;
      const int head = (nn * 3277) >> 16;
      so[j] = (unsigned)(head * (64 * 1280) + (h * 20 + nn - head * 20) * 16);
      PIN(so[j]);
    }
  } else {
#pragma unroll
    for (int j = 0; j < 8; ++j) {
      const int G = (((isq ? n0 : n0 - 640) + nh * 64) >> 2) + h + 2 * j;
      const int head = (G * 205) >> 10;
      const int dg = G - head * 5;
      so[j] = (unsigned)(head * (64 * 1280) + (dg < 4 ? (dg >> 1) * 512 + r * 16 + (dg & 1) * 8 : 1024 + r * 8));
      PIN(so[j]);
    }
  }
  f32x16 acc[2], ach[NHT];
  f32x4 cq[2], chh[NHT], rsum = {0.f, 0.f, 0.f, 0.f}, rsq = rsum;
#pragma unroll
  for (int i = 0; i < 16; ++i) {
    acc[0][i] = 0.f; acc[1][i] = 0.f;
#pragma unroll
    for (int j = 0; j < NHT; ++j) ach[j][i] = 0.f;
  }
  cq[0] = rsum; cq[1] = rsum;
#pragma unroll
  for (int j = 0; j < NHT; ++j) chh[j] = rsum;
  const bf16x4 ones = {0x3f80, 0x3f80, 0x3f80, 0x3f80};
  bf16x8 xf[3], w0[3], w1[3], wh[3][NHT];
  unsigned wa1[4];
#pragma unroll
  for (int i = 0; i < 4; ++i) wa1[i] = wa0[i] ^ 64u;
#define KA_READ(B, i)                                                                                           \
  do {                                                                                                          \
    constexpr int s_ = ((B) + (i)) % 3;                                                                         \
    if ((KABL & 8) && started) break;                                                                           \
    xf[s_] = *(const LDS_AS bf16x8*)(LP(xa[i]) + BUFOFF(B));                                                    \
    if ((KABL & 16) && started) break;                                                                          \
    w0[s_] = cat8(tr_read(LP(wa0[i]) + BUFOFF(B)), tr_read(LP(wa0[i]) + BUFOFF(B) + 512));                      \
    w1[s_] = cat8(tr_read(LP(wa1[i]) + BUFOFF(B)), tr_read(LP(wa1[i]) + BUFOFF(B) + 512));                      \
    _Pragma("unroll") for (int j_ = 0; j_ < NHT; ++j_)                                                          \
      wh[s_][j_] = cat8(tr_read(LP(ha[i]) + BUFOFF(B) + j_ * 64), tr_read(LP(ha[i]) + BUFOFF(B) + j_ * 64 + 768)); \
  } while (0)
#define SGB(m, n) __builtin_amdgcn_sched_group_barrier((m), (n), 0)
#define KA_STEP(B, i, RD)                                                                                       \
  do {                                                                                                          \
    constexpr int b_ = ((B) + (i)) % 3;                                                                         \
    if constexpr (NHT == 2) asm volatile("" : "+v"(xf[b_]), "+v"(w0[b_]), "+v"(w1[b_]), "+v"(wh[b_][0]), "+v"(wh[b_][NHT - 1])); \
    else asm volatile("" : "+v"(xf[b_]), "+v"(w0[b_]), "+v"(w1[b_]), "+v"(wh[b_][0]));                          \
    SBAR();                                                                                                     \
    RD;                                                                                                         \
    if (TYPE == 1) { acc[0] = MFMA32(xf[b_], w0[b_], acc[0]); acc[1] = MFMA32(xf[b_], w1[b_], acc[1]); }        \
    else { acc[0] = MFMA32(w0[b_], xf[b_], acc[0]); acc[1] = MFMA32(w1[b_], xf[b_], acc[1]); }                  \
    _Pragma("unroll") for (int j_ = 0; j_ < NHT; ++j_) ach[j_] = MFMA32(wh[b_][j_], xf[b_], ach[j_]);           \
    SGB(0x8, 1); SGB(0x100, 3); SGB(0x8, 1); SGB(0x100, 2); SGB(0x8, 1); SGB(0x100, 2);                         \
    if (NHT == 2) { SGB(0x8, 1); SGB(0x100, 2); }                                                               \
    if constexpr (NHT == 2) asm volatile("" : "+v"(acc[0]), "+v"(acc[1]), "+v"(ach[0]), "+v"(ach[NHT - 1]));    \
    else asm volatile("" : "+v"(acc[0]), "+v"(acc[1]), "+v"(ach[0]));                                           \
    SBAR();                                                                                                     \
    if ((i) == 0 && !(KABL & 1)) {                                                                              \
      cq[0] = MFMA4(ones, lo4(w0[b_]), cq[0]); cq[1] = MFMA4(ones, lo4(w1[b_]), cq[1]);                         \
      _Pragma("unroll") for (int j_ = 0; j_ < NHT; ++j_) chh[j_] = MFMA4(ones, lo4(wh[b_][j_]), chh[j_]);       \
      cq[0] = MFMA4(ones, hi4(w0[b_]), cq[0]); cq[1] = MFMA4(ones, hi4(w1[b_]), cq[1]);                         \
      _Pragma("unroll") for (int j_ = 0; j_ < NHT; ++j_) chh[j_] = MFMA4(ones, hi4(wh[b_][j_]), chh[j_]);       \
      if constexpr (NHT == 2) asm volatile("" : "+v"(cq[0]), "+v"(cq[1]), "+v"(chh[0]), "+v"(chh[NHT - 1]));    \
      else asm volatile("" : "+v"(cq[0]), "+v"(cq[1]), "+v"(chh[0]));                                           \
    }                                                                                                           \
    if ((nh ? (i) < 2 : (i) >= 2) && !(KABL & 2)) {                                                             \
      rsum = MFMA4(ones, lo4(xf[b_]), rsum); rsq = MFMA4(lo4(xf[b_]), lo4(xf[b_]), rsq);                        \
      rsum2 = MFMA4(ones, hi4(xf[b_]), rsum2); rsq2 = MFMA4(hi4(xf[b_]), hi4(xf[b_]), rsq2);                    \
      asm volatile("" : "+v"(rsum), "+v"(rsq), "+v"(rsum2), "+v"(rsq2));                                        \
    }                                                                                                           \
    SBAR();                                                                                                     \
  } while (0)
#define BUFOFF(B) ((B) * KA_BUF)
#define KA_CHUNK(c, B, BN, WAITN, PREV, MORE)                                                                   \
  do {                                                                                                          \
    KA_STEP(B, 0, KA_READ(B, 2));                                                                               \
    if ((PREV) && !(KABL & 4)) { KA_DH((c) + 2, BUFOFF(((B) + 2) % 3)); SBAR(); }                               \
    KA_STEP(B, 1, KA_READ(B, 3));                                                                               \
    if (!(KABL & 32)) WAIT_BAR(WAITN);                                                                          \
    KA_STEP(B, 2, KA_READ(BN, 0));                                                                              \
    if ((MORE) && !(KABL & 4)) { KA_DX((c) + 3, BUFOFF(B)); SBAR(); }                                           \
    KA_STEP(B, 3, KA_READ(BN, 1));                                                                              \
    if ((MORE) && !(KABL & 4)) { KA_DW((c) + 3, BUFOFF(B), 0); KA_DW((c) + 3, BUFOFF(B), 1); SBAR(); }          \
  } while (0)
  f32x4 rsum2 = rsum, rsq2 = rsum;
  if (KA_WARM + CODE_PF == 2) WAIT_BAR(14); else if (KA_WARM + CODE_PF == 1) WAIT_BAR(13); else WAIT_BAR(12);
  STAMP(0, 1);
  STAMPC(0, 14);
  bool started = false;
  KA_READ(0, 0);
  KA_READ(0, 1);
  if (KABL & 24) { KA_READ(0, 2); started = true; }
  if (KA_WARM) KA_CHUNK(0, 0, 1, 7, false, true); else KA_CHUNK(0, 0, 1, 6, false, true);
#pragma unroll 1
  for (int c = 1; c < 7; c += 3) {
    KA_CHUNK(c, 1, 2, 6, true, true);
    KA_CHUNK(c + 1, 2, 0, 6, true, true);
    KA_CHUNK(c + 2, 0, 1, 6, true, true);
  }
  KA_CHUNK(7, 1, 2, 6, true, false);
  KA_CHUNK(8, 2, 0, 0, false, false);
  KA_STEP(0, 0, KA_READ(0, 2));
  KA_STEP(0, 1, KA_READ(0, 3));
  KA_STEP(0, 2, NOP_);
  KA_STEP(0, 3, NOP_);
  asm volatile("s_nop 7" : "+v"(rsum), "+v"(rsq), "+v"(rsum2), "+v"(rsq2));
  rsum += rsum2; rsq += rsq2;
#undef KA_READ
#undef KA_STEP
#undef KA_CHUNK
#undef KA_ISSUE
  STAMP(0, 3);
  STAMPC(0, 15);

  if (nh == 1) {
    const int l0_ = blockIdx.x * 85 + (tid - 256 < 85 ? tid - 256 : 0);
    const char* p = l0_ < 15360 ? w2 + (size_t)l0_ * 128 : wo + (size_t)(l0_ - 15360) * 128;
    if (w < 6) asm volatile("global_load_dword %0, %1, off" : "=v"(warm) : "v"(p) : "memory");
  }
  LDS_AS f32x2* st_r = (LDS_AS f32x2*)(lds + KA_STAT);
  LDS_AS float* st_cs = (LDS_AS float*)(lds + KA_SCS);
  LDS_AS float* st_b = (LDS_AS float*)(lds + KA_SB1);
  {
    const int d = lane & 3;
    const float sq = d == 0 ? rsq[0] : d == 1 ? rsq[1] : d == 2 ? rsq[2] : rsq[3];
    const f32x2 rv = {x32_add(rsum[0]), x32_add(sq)};
    const float c0 = x32_add(cq[0][0]), c1 = x32_add(cq[1][0]);
    float c2[NHT];
#pragma unroll
    for (int j = 0; j < NHT; ++j) c2[j] = x32_add(chh[j][0]);
    if (h == 0) {
      st_r[nh * 128 + mi * 32 + r] = rv;
      st_cs[mi * 224 + nh * 64 + r] = c0;
      st_cs[mi * 224 + nh * 64 + 32 + r] = c1;
#pragma unroll
      for (int j = 0; j < NHT; ++j) st_cs[mi * 224 + 128 + nh * 64 + j * 32 + r] = c2[j];
    }
    PIN(warm2); PIN(cpf);
    if (nh == 0 && tid < 96) st_b[tid] = __uint_as_float(braw << 16);
  }
  LBAR(0);
  if (tid < 224) st_cs[tid] = (st_cs[tid] + st_cs[224 + tid]) + (st_cs[448 + tid] + st_cs[672 + tid]);
  const f32x2 ra = st_r[mi * 32 + r], rb2 = st_r[128 + mi * 32 + r];
  const float mu = (ra[0] + rb2[0]) * (1.0f / D_);
  const float var = fmaxf((ra[1] + rb2[1]) * (1.0f / D_) - mu * mu, 0.f);
  const float rs = __builtin_amdgcn_rsqf(var + LN_EPS);
  LDS_AS f32x2* st_w = (LDS_AS f32x2*)(lds + KA_SMR) + w * 32;
  if (TYPE == 1 && h == 0) st_w[r] = f32x2{mu * rs, rs};
  LBAR(0);
  STAMP(0, 5);
  if (cb < 15) {
    if (TYPE == 1) {
      char* dst = ws + WS_V + (size_t)tq * 1280;
      const float cs0 = st_cs[nh * 64 + r], cs1 = st_cs[nh * 64 + 32 + r];
#pragma unroll
      for (int s = 0; s < 2; ++s) {
        f32x2 mr[8];
#pragma unroll
        for (int e = 0; e < 8; ++e) mr[e] = st_w[16 * s + 8 * (e >> 2) + 4 * h + (e & 3)];
        u32x4 o0, o1;
#pragma unroll
        for (int e = 0; e < 4; ++e) {
          const int i = 8 * s + 2 * e;
          o0[e] = cvtpk(acc[0][i] * mr[2 * e][1] - mr[2 * e][0] * cs0, acc[0][i + 1] * mr[2 * e + 1][1] - mr[2 * e + 1][0] * cs0);
          o1[e] = cvtpk(acc[1][i] * mr[2 * e][1] - mr[2 * e][0] * cs1, acc[1][i + 1] * mr[2 * e + 1][1] - mr[2 * e + 1][0] * cs1);
        }
        st16(dst + so[0] + s * 640, o0);
        st16(dst + so[1] + s * 640, o1);
      }
    } else {
      const float sc = isq ? rs * QSCALE : rs, ms = mu * sc;
      char* base = ws + (isq ? WS_Q : WS_K) + (size_t)tq * 1280;
      float gmax = 0.f;
#pragma unroll
      for (int j = 0; j < 2; ++j)
#pragma unroll
        for (int g = 0; g < 4; ++g) {
          const f32x4 cs = *(const LDS_AS f32x4*)(st_cs + nh * 64 + j * 32 + 8 * g + 4 * h);
          const u32x2 o = {cvtpk(acc[j][4 * g] * sc - ms * cs[0], acc[j][4 * g + 1] * sc - ms * cs[1]),
                           cvtpk(acc[j][4 * g + 2] * sc - ms * cs[2], acc[j][4 * g + 3] * sc - ms * cs[3])};
          st8(base + so[j * 4 + g], o);
          if (!isq) gmax = nanmax(gmax, dot2(o[1], o[1], dot2(o[0], o[0], 0.f)));
        }
      if (!isq) {
        gmax = wave_nanmax(gmax);
        if (lane == 0) *(float*)(ws + WS_KMX + (size_t)((((n0 - 640) >> 6) + nh) * 64 + tq) * 4) = gmax;
      }
    }
  }
  {
    const float sc = rs * __builtin_amdgcn_rsqf(var * rs * rs + LN_EPS), ms = mu * sc;
    const int tok = rb * 128 + mi * 32 + r;
    constexpr float GK1 = -2.0f * 1.4426950408889634f * 0.7978845608028654f, GK2 = GK1 * 0.044715f;
    char* dst = ws + WS_H + ((size_t)(tok >> 4) * 48 + (f0 >> 5) + nh * 2) * 1024 + (h * 16 + (tok & 15)) * 16;
#pragma unroll
    for (int j = 0; j < NHT; ++j)
#pragma unroll
      for (int u = 0; u < 2; ++u) {
        float v[8];
#pragma unroll
        for (int gg = 0; gg < 2; ++gg) {
          const int g = 2 * u + gg;
          const f32x4 cs = *(const LDS_AS f32x4*)(st_cs + 128 + nh * 64 + j * 32 + 8 * g + 4 * h);
          const f32x4 bb = *(const LDS_AS f32x4*)(st_b + nh * 64 + j * 32 + 8 * g + 4 * h);
#pragma unroll
          for (int e = 0; e < 4; ++e) {
            const float t = ach[j][4 * g + e] * sc + (bb[e] - ms * cs[e]);
            const float ex = __builtin_amdgcn_exp2f(t * (GK1 + GK2 * t * t));
            v[4 * gg + e] = t * __builtin_amdgcn_rcpf(1.0f + ex);
          }
        }
        st16(dst + j * 1024 + u * 512, u32x4{cvtpk(v[0], v[1]), cvtpk(v[2], v[3]), cvtpk(v[4], v[5]), cvtpk(v[6], v[7])});
      }
  }
  STAMP(0, 4);
  asm volatile("s_waitcnt vmcnt(0)" : "+v"(warm) :: "memory");
}

__global__ __launch_bounds__(512, 2) void ka_proj(const char* __restrict__ x, const char* __restrict__ wqkv,
                                                  const char* __restrict__ w1, const unsigned short* __restrict__ b1,
                                                  const char* __restrict__ w2, const char* __restrict__ wo,
                                                  char* __restrict__ ws) {
  extern __shared__ __attribute__((aligned(16))) char smem[];
  lds_ptr lds = (lds_ptr)smem;
  const int bid = blockIdx.x, xg = bid & 7, idx = bid >> 3;
  const int rb = (xg & 1) * 8 + (idx & 7), cb = (xg >> 1) * 4 + (idx >> 3);
  const int n0 = (cb < 15 ? cb : 14) * 128, f0 = cb * 96;
  if (bid == 0 && threadIdx.x < 32) *(unsigned*)(ws + WS_CNT + threadIdx.x * 256) = 0u;
  unsigned long long pc = 0;
  unsigned span = 0;
  asm volatile("s_getpc_b64 %0\n.Lka_pf_base:\n\ts_mov_b32 %1, kb_tail_label-.Lka_pf_base" : "=s"(pc), "=s"(span));
  const int nh = __builtin_amdgcn_readfirstlane(threadIdx.x >> 8);
  if (cb < 10) {
    if (nh) ka_body<0, 1>(lds, x, wqkv, w1, b1, w2, wo, n0, f0, rb, cb, ws, pc, span);
    else ka_body<0, 0>(lds, x, wqkv, w1, b1, w2, wo, n0, f0, rb, cb, ws, pc, span);
  } else {
    if (nh) ka_body<1, 1>(lds, x, wqkv, w1, b1, w2, wo, n0, f0, rb, cb, ws, pc, span);
    else ka_body<1, 0>(lds, x, wqkv, w1, b1, w2, wo, n0, f0, rb, cb, ws, pc, span);
  }
}

typedef __attribute__((address_space(1))) unsigned gu32;
DI void st8_dev(void* p, u32x2 v) { asm volatile("global_store_dwordx2 %0, %1, off sc1" :: "v"(p), "v"(v) : "memory"); }
DI u32x4 bload16_dev(srd_t r, unsigned voff, unsigned soff) { return __builtin_amdgcn_raw_buffer_load_b128(r, (int)voff, (int)soff, 16); }
DI void ld16_asm(bf16x8& d, unsigned voff, const void* sbase) {
  asm volatile("global_load_dwordx4 %0, %1, %2" : "=v"(d) : "v"(voff), "s"(sbase) : "memory");
}
DI void publish(char* ws, int cnt) {
  asm volatile("s_waitcnt vmcnt(0) lgkmcnt(0)\n\ts_barrier" ::: "memory");
  if (threadIdx.x == 0) __hip_atomic_fetch_add((gu32*)(ws + WS_CNT + cnt * 256), 1u, __ATOMIC_RELAXED, __HIP_MEMORY_SCOPE_AGENT);
}
DI void await(char* ws, int cnt, unsigned want, int wid) {
  if (wid == 0) {
    for (unsigned spins = 0; spins < (1u << 16); ++spins) {
      const unsigned v = __hip_atomic_load((gu32*)(ws + WS_CNT + cnt * 256), __ATOMIC_RELAXED, __HIP_MEMORY_SCOPE_AGENT);
      if ((unsigned)__builtin_amdgcn_readfirstlane(v) >= want) break;
      __builtin_amdgcn_s_sleep(1);
    }
  }
  asm volatile("s_waitcnt vmcnt(0)\n\ts_barrier" ::: "memory");
}

constexpr int AT_TILE = 3584;
constexpr int AT_SLOT = 28672;
constexpr int KB_WR = 2 * AT_SLOT;
constexpr int KB_GRP = 40960;
constexpr int KB_RED = KB_WR + 2 * KB_GRP;
constexpr int KB_LDS = KB_RED + 20480;

DI void attn_store(char* ws, const f32x16& o, const float inv, const int head, const int tok, const int h) {
  char* base = ws + WS_O + (size_t)(tok >> 4) * (20 * 1024) + (tok & 15) * 16;
#pragma unroll
  for (int g = 0; g < 3; ++g) {
    if (g < 2 || h == 0) {
      const int Gc = head * 5 + 2 * g + h;
      const int G8 = Gc & 7;
      const int off = (Gc >> 3) * 1024 + (((G8 >> 2) * 2 + (G8 & 1)) * 256) + ((G8 >> 1) & 1) * 8;
      u32x2 v = {cvtpk(o[4 * g] * inv, o[4 * g + 1] * inv), cvtpk(o[4 * g + 2] * inv, o[4 * g + 3] * inv)};
      st8_dev(base + off, v);
    }
  }
}

__global__ __launch_bounds__(512, 2) void kb_main(const char* __restrict__ x, const char* __restrict__ w2,
                                                  const unsigned short* __restrict__ b2, const char* __restrict__ wo,
                                                  char* __restrict__ ws, char* __restrict__ out) {
  extern __shared__ __attribute__((aligned(16))) char smem[];
  lds_ptr lds = (lds_ptr)smem;
  const int tid = threadIdx.x, lane = tid & 63;
  const int wid = __builtin_amdgcn_readfirstlane(tid >> 6);
  const int r = lane & 31, h = lane >> 5;
  const int xg = blockIdx.x & 7, idx = blockIdx.x >> 3;
  const int head = xg * 4 + (idx >> 3), qb = idx & 7;
  const int rbk = xg * 4 + (idx & 3), cbk = idx >> 2;
  const int mi = wid & 3, kh = wid >> 2, gq = lane >> 4;
  const unsigned lds0 = (unsigned)(uintptr_t)lds;
  const char* Kh = ws + WS_K + (size_t)head * (64 * 1280);
  const char* Vh = ws + WS_V + (size_t)head * (64 * 1280);
  const char* Qt = ws + WS_Q + (size_t)(head * 64 + qb * 8 + wid) * 1280;
  STAMP(1, 0);

  const unsigned vlin = (unsigned)lane * 16u;
  const unsigned vv = (unsigned)(h * 20 + r) * 16u;
  const unsigned vv2 = vv + (unsigned)(WS_V - WS_K) - 1280u - 1536u, vv3 = vv2 + 640u - 1024u;
  const unsigned long long mk16 = 0xffffull, mk20 = 0x000fffff000fffffull;
#define KV_M0(c) SET_M0(lds0 + (unsigned)(((c) & 1) * AT_SLOT + wid * AT_TILE))
#define KV_SRC(c) (Kh + (size_t)((c) * 8 + wid + 1) * 1280)
#define KV_P0(c) asm volatile("global_load_lds_dwordx4 %0, %1" :: "v"(vlin), "s"(KV_SRC(c)) : "memory")
#define KV_P1(c) asm volatile("s_mov_b64 exec, %2\n\tglobal_load_lds_dwordx4 %0, %1 offset:1024\n\ts_mov_b64 exec, -1" :: "v"(vlin), "s"(KV_SRC(c)), "s"(mk16) : "memory")
#define KV_P23(c) asm volatile("s_mov_b64 exec, %3\n\tglobal_load_lds_dwordx4 %0, %2 offset:1536\n\tglobal_load_lds_dwordx4 %1, %2 offset:2560\n\ts_mov_b64 exec, -1" :: "v"(vv2), "v"(vv3), "s"(KV_SRC(c)), "s"(mk20) : "memory")
  bf16x8 qa, ka0;
  u32x2 qbv, kb0;
  float kmax, kmax2;
  {
    const unsigned v8 = 1024u + (unsigned)r * 8u, v4 = (unsigned)lane * 4u;
    asm volatile("global_load_dwordx4 %0, %6, %9\n\tglobal_load_dwordx2 %1, %7, %9\n\t"
                 "global_load_dwordx4 %2, %6, %10\n\tglobal_load_dwordx2 %3, %7, %10\n\t"
                 "global_load_dword %4, %8, %11\n\tglobal_load_dword %5, %8, %12"
                 : "=&v"(qa), "=&v"(qbv), "=&v"(ka0), "=&v"(kb0), "=&v"(kmax), "=&v"(kmax2)
                 : "v"(vlin), "v"(v8), "v"(v4), "s"(Qt), "s"(Kh), "s"(ws + WS_KMX + ((head * 20) >> 6) * 256),
                   "s"(ws + WS_KMX + ((head * 20 + 19) >> 6) * 256) : "memory");
  }
  KV_M0(0); KV_P0(0); KV_P1(0); KV_P23(0);
#pragma unroll
  for (int sl = 0; sl < 2; ++sl) {
    lds_ptr tp = lds + sl * AT_SLOT + wid * AT_TILE;
    if (lane < 32) *(LDS_AS u32x2*)(tp + 1024 + 256 + lane * 8) = u32x2{0x00003f80u, 0u};
    if (lane < 48) {
      const int blk4 = (lane * 43) >> 9, dd = 20 + lane - blk4 * 12;
      const unsigned val = dd == 20 ? 0x3f803f80u : 0u;
      *(LDS_AS u32x4*)(tp + 1536 + blk4 * 512 + dd * 16) = u32x4{val, val, val, val};
    }
  }
  unsigned vo[5];
#pragma unroll
  for (int i = 0; i < 5; ++i) {
    const int p = i * 64 + lane, row = (p * 6554) >> 16;
    vo[i] = (unsigned)(row * 1280 + (p - row * 10) * 16 - (i & 3) * 1024);
  }
#define BABL_W 0
#define W_SRC(W, grp) ((W) + cbk * 160 + (size_t)((grp) * 256 + wid * 32) * 1280)
#define W_M0(reg, hi) SET_M0(lds0 + (unsigned)(KB_WR + (reg) * KB_GRP + wid * 5120 + (hi) * 4096))
#define W_P(W, i, grp) if (!(BABL_W)) asm volatile("global_load_lds_dwordx4 %0, %1 offset:%2" :: "v"(vo[i]), "s"(W_SRC(W, grp)), "i"(((i) & 3) * 1024) : "memory")
  const char* hsrc = ws + WS_H + ((size_t)(rbk * 4 + mi) * 48 + kh) * 1024 + 4096;
#define H_LOAD(D, j, grp) asm volatile("global_load_dwordx4 %0, %1, %2 offset:%3" : "=v"(D[j]) : "v"(vlin), "s"(hsrc + (grp) * 8192), "i"((j) * 2048 - 4096) : "memory")
  const unsigned wra = lds0 + (unsigned)(KB_WR + (kh * 32 + 4 * ((gq & 1) + 4 * (gq >> 1)) + ((lane & 15) >> 2)) * 160 + (lane & 3) * 8);
  const unsigned wrb = wra + KB_GRP;

  bf16x8 hA[4], hB[4];
  W_M0(0, 0); W_P(w2, 0, 0); W_P(w2, 1, 0); W_P(w2, 2, 0); W_P(w2, 3, 0); W_M0(0, 1); W_P(w2, 4, 0);
  KV_M0(1); KV_P0(1); KV_P1(1); KV_P23(1);
  H_LOAD(hA, 0, 0); H_LOAD(hA, 1, 0); H_LOAD(hA, 2, 0); H_LOAD(hA, 3, 0);
  asm volatile("s_waitcnt vmcnt(17)" : "+v"(qa), "+v"(qbv), "+v"(ka0), "+v"(kb0), "+v"(kmax), "+v"(kmax2) :: "memory");
  kmax = wave_nanmax(nanmax(kmax, kmax2));
  {
    const u32x4 a = __builtin_bit_cast(u32x4, qa);
    float n0 = dot2(a[0], a[0], 0.f), n1 = dot2(a[1], a[1], 0.f);
    n0 = dot2(a[2], a[2], n0); n1 = dot2(a[3], a[3], n1);
    if (h == 0) { n0 = dot2(qbv[0], qbv[0], n0); n1 = dot2(qbv[1], qbv[1], n1); }
    const float qn = x32_add(n0 + n1);
    const float bound = __builtin_amdgcn_sqrtf(qn * kmax * 5.0f) * 1.01f;
    if (h == 1) { qbv[0] = cvtpk(-bound, 0.f); qbv[1] = 0u; kb0[0] = 0x00003f80u; kb0[1] = 0u; }
  }
  const bf16x4 qb4 = __builtin_bit_cast(bf16x4, qbv);
  f32x16 o;
#pragma unroll
  for (int i = 0; i < 16; ++i) o[i] = 0.f;
  const f32x16 zero = o;
  f32x16 sA = MFMA32(ka0, qa, zero);
  sA = MFMA32K8(__builtin_bit_cast(bf16x4, kb0), qb4, sA);
  f32x16 sB;
  f32x4 acc[5];
#pragma unroll
  for (int j = 0; j < 5; ++j) acc[j] = f32x4{0.f, 0.f, 0.f, 0.f};
  WAIT_BAR(13);
  STAMP(1, 1);
  STAMPC(1, 14);
  STAMP2(2, 0);

  unsigned a16 = lds0 + vlin, a8 = lds0 + (unsigned)lane * 8u;
  int sdelta = AT_SLOT;
  bf16x8 kaA, v0A, v1A, kaB, v0B, v1B;
  bf16x4 kbA, kbB;
  bf16x4 wl[5], wu[5];
  u32x4 p0, p1 = {0u, 0u, 0u, 0u};
  v1B = __builtin_bit_cast(bf16x8, p1);
#define RD_KA(X, j) ka##X = *(const LDS_AS bf16x8*)(LP(a16) + (j) * AT_TILE)
#define RD_KB(X, j) kb##X = *(const LDS_AS bf16x4*)(LP(a8) + (j) * AT_TILE + 1024)
#define RD_V0(X, j) v0##X = *(const LDS_AS bf16x8*)(LP(a16) + (j) * AT_TILE + 1536)
#define RD_V1(X, j) v1##X = *(const LDS_AS bf16x8*)(LP(a16) + (j) * AT_TILE + 2560)
#define W_LO(WA, j, t) wl[t] = tr_read(LP(WA) + (j) * 10240 + (t) * 32)
#define W_HI(WA, j, t) wu[t] = tr_read(LP(WA) + (j) * 10240 + (t) * 32 + 1280)
#define BABL 0
#define G_MM(HS, j, t) do { if (BABL & 1) { PIN(wl[t]); PIN(wu[t]); PIN(HS[j]); } else { acc[t] = MFMA16(cat8(wl[t], wu[t]), HS[j], acc[t]); } } while (0)
#define EX2(S, i) do { S[i] = __builtin_amdgcn_exp2f(S[i]); S[i + 1] = __builtin_amdgcn_exp2f(S[i + 1]); } while (0)
#define MID() do { __builtin_amdgcn_sched_group_barrier(0x400, 2, 0); __builtin_amdgcn_sched_group_barrier(0x008, 1, 0); \
                    __builtin_amdgcn_sched_group_barrier(0x400, 2, 0); __builtin_amdgcn_sched_group_barrier(0x002, 2, 0); } while (0)
#define AT_STEP(SC, SN, X, Y, JN, PRE, WP, A0, A1, A2, A3, A4, A5, V0_, V1_, V2_, M1, M2, M3)            \
  do {                                                                                                   \
    PRE;                                                                                                 \
    asm volatile("" : "+v"(ka##X), "+v"(kb##X), "+v"(v0##X), "+v"(v1##X)); WP; SBAR();                   \
    EX2(SC, 0); PIN(SC); SBAR();                                                                         \
    o = MFMA32(v1##Y, __builtin_bit_cast(bf16x8, p1), o); SBAR();                                        \
    RD_KA(Y, JN); RD_KB(Y, JN); RD_V0(Y, JN); RD_V1(Y, JN); A0; A1; A2; A3; A4; A5; V0_; V1_; V2_; SBAR(); \
    M1; EX2(SC, 2); EX2(SC, 4); p0[0] = cvtpk(SC[0], SC[1]); p0[1] = cvtpk(SC[2], SC[3]); MID();         \
    asm volatile("" : "+v"(p0), "+v"(SC), "+v"(acc[0]), "+v"(acc[3])); SBAR();                           \
    SN = MFMA32(ka##X, qa, zero); SBAR();                                                                \
    M2; EX2(SC, 6); EX2(SC, 8); p0[2] = cvtpk(SC[4], SC[5]); p0[3] = cvtpk(SC[6], SC[7]); MID();         \
    asm volatile("" : "+v"(p0), "+v"(SC), "+v"(acc[1])); SBAR();                                         \
    SN = MFMA32K8(kb##X, qb4, SN); SBAR();                                                               \
    M3; EX2(SC, 10); EX2(SC, 12); p1[0] = cvtpk(SC[8], SC[9]); p1[1] = cvtpk(SC[10], SC[11]); MID();     \
    asm volatile("" : "+v"(p1), "+v"(SC), "+v"(acc[2]), "+v"(acc[4])); SBAR();                           \
    o = MFMA32(v0##X, __builtin_bit_cast(bf16x8, p0), o); SBAR();                                        \
    EX2(SC, 14); p1[2] = cvtpk(SC[12], SC[13]); p1[3] = cvtpk(SC[14], SC[15]); PIN(p1); SBAR();          \
  } while (0)
#define WP_E asm volatile("" : "+v"(wl[0]), "+v"(wu[0]), "+v"(wl[1]), "+v"(wu[1]), "+v"(wl[2]), "+v"(wu[2]))
#define WP_O asm volatile("" : "+v"(wl[3]), "+v"(wu[3]), "+v"(wl[4]), "+v"(wu[4]))
#define FLIP() do { a16 += (unsigned)sdelta; a8 += (unsigned)sdelta; sdelta = -sdelta; } while (0)
#define SYNC_H(HN) asm volatile("s_waitcnt vmcnt(3) lgkmcnt(0)\n\ts_barrier" : "+v"(HN[0]) :: "memory")
#define WAIT_H(HC, j, N) asm volatile("s_waitcnt vmcnt(" #N ")" : "+v"(HC[j]) :: "memory")
#define STEP_E(JN, PRE, WC, HC, j, X0, X1, V0_, V1_)                                                     \
  AT_STEP(sA, sB, A, B, JN, PRE, WP_E, W_LO(WC, j, 3), W_HI(WC, j, 3), W_LO(WC, j, 4), W_HI(WC, j, 4), X0, X1, V0_, V1_, NOP_, \
          G_MM(HC, j, 0), G_MM(HC, j, 1), G_MM(HC, j, 2))
#define STEP_O(JN, PRE, WA, jn, HC, j, X0, V0_, V1_)                                                     \
  AT_STEP(sB, sA, B, A, JN, PRE, WP_O, W_LO(WA, jn, 0), W_HI(WA, jn, 0), W_LO(WA, jn, 1), W_HI(WA, jn, 1), W_LO(WA, jn, 2), \
          W_HI(WA, jn, 2), X0, V0_, V1_, G_MM(HC, j, 3), NOP_, G_MM(HC, j, 4))
#define CHUNK_G(c, WC, HC, WN, HN, RN, NW, NG, HG)                                                       \
  do {                                                                                                   \
    STEP_E(1, NOP_, WC, HC, 0, W_M0(RN, 0), W_P(NW, 0, NG), W_P(NW, 1, NG), W_P(NW, 2, NG));             \
    STEP_O(2, NOP_, WC, 1, HC, 0, W_P(NW, 3, NG), W_M0(RN, 1), W_P(NW, 4, NG));                          \
    STEP_E(3, WAIT_H(HC, 1, 7), WC, HC, 1, KV_M0((c) + 1), KV_P0((c) + 1), KV_P1((c) + 1), KV_P23((c) + 1)); \
    STEP_O(4, NOP_, WC, 2, HC, 1, H_LOAD(HN, 0, HG), H_LOAD(HN, 1, HG), H_LOAD(HN, 2, HG));              \
    STEP_E(5, WAIT_H(HC, 2, 13), WC, HC, 2, H_LOAD(HN, 3, HG), NOP_, NOP_, NOP_);                        \
    STEP_O(6, NOP_, WC, 3, HC, 2, NOP_, NOP_, NOP_);                                                     \
    STEP_E(7, WAIT_H(HC, 3, 13), WC, HC, 3, NOP_, NOP_, NOP_, NOP_);                                     \
    STEP_O(0, SYNC_H(HN); STAMP2(2, 1 + (c)); FLIP(), WN, 0, HC, 3, NOP_, NOP_, NOP_);                   \
  } while (0)
#define PLAIN_E(JN, X0, X1, X2, X3) AT_STEP(sA, sB, A, B, JN, NOP_, NOP_, X0, X1, X2, X3, NOP_, NOP_, NOP_, NOP_, NOP_, NOP_, NOP_, NOP_)
#define PLAIN_O(JN, X0, X1, X2, X3) AT_STEP(sB, sA, B, A, JN, NOP_, NOP_, X0, X1, X2, X3, NOP_, NOP_, NOP_, NOP_, NOP_, NOP_, NOP_, NOP_)

  RD_KA(A, 0); RD_KB(A, 0); RD_V0(A, 0); RD_V1(A, 0);
  PLAIN_E(1, NOP_, NOP_, NOP_, NOP_);
  PLAIN_O(2, NOP_, NOP_, NOP_, NOP_);
  PLAIN_E(3, NOP_, NOP_, NOP_, NOP_);
  PLAIN_O(4, NOP_, NOP_, NOP_, NOP_);
  PLAIN_E(5, NOP_, NOP_, NOP_, NOP_);
  PLAIN_O(6, NOP_, NOP_, NOP_, NOP_);
  PLAIN_E(7, NOP_, NOP_, NOP_, NOP_);
  AT_STEP(sB, sA, B, A, 0, SYNC_H(hA); STAMP2(2, 1); FLIP(), NOP_, W_LO(wra, 0, 0), W_HI(wra, 0, 0), W_LO(wra, 0, 1), W_HI(wra, 0, 1),
          W_LO(wra, 0, 2), W_HI(wra, 0, 2), NOP_, NOP_, NOP_, NOP_, NOP_, NOP_);
#pragma unroll 1
  for (int c = 1; c < 7; c += 2) {
    CHUNK_G(c, wra, hA, wrb, hB, 1, w2, c, c);
    const char* wn = c < 5 ? w2 : wo;
    CHUNK_G(c + 1, wrb, hB, wra, hA, 0, wn, (c < 5 ? c + 1 : 0), (c < 5 ? c + 1 : 5));
  }
  const int tok = rbk * 64 + mi * 16 + (lane & 15);
  const size_t xo = (size_t)tok * 1280 + (cbk * 80 + 4 * gq) * 2;
  LDS_AS f32x4* red = (LDS_AS f32x4*)(lds + KB_RED) + (mi * 5) * 64 + lane;
  u32x2 gv[5], xv[5];
  PLAIN_E(1, NOP_,
          _Pragma("unroll") for (int j = 0; j < 5; ++j) gv[j] = *(const u32x2*)(b2 + cbk * 80 + j * 16 + 4 * gq),
          _Pragma("unroll") for (int j = 0; j < 5; ++j) xv[j] = *(const u32x2*)(x + xo + j * 32),
          if (kh == 1) { _Pragma("unroll") for (int j = 0; j < 5; ++j) red[j * 64] = acc[j]; });
  PLAIN_O(2, NOP_, NOP_, NOP_, NOP_);
  PLAIN_E(3, NOP_, NOP_, NOP_, NOP_);
  PLAIN_O(4, asm volatile("s_waitcnt vmcnt(0)" : "+v"(hA[1]), "+v"(hA[2]), "+v"(hA[3]) :: "memory");
          _Pragma("unroll") for (int j = 0; j < 5; ++j) { PIN(gv[j]); PIN(xv[j]); } W_M0(1, 0), W_P(wo, 0, 1), W_P(wo, 1, 1), W_P(wo, 2, 1));
  AT_STEP(sA, sB, A, B, 5, LBAR(0), NOP_, W_P(wo, 3, 1), W_M0(1, 1), W_P(wo, 4, 1), SET_M0(lds0 + (unsigned)(wid * 2560)),
          asm volatile("global_load_lds_dwordx4 %0, %3\n\tglobal_load_lds_dwordx4 %1, %3 offset:1024\n\t"
                       "s_mov_b32 exec_hi, 0\n\tglobal_load_lds_dwordx4 %2, %3 offset:2048\n\ts_mov_b32 exec_hi, -1"
                       :: "v"(vo[0]), "v"(vo[1]), "v"(vo[2]), "s"(wo + cbk * 160 + (size_t)(512 + wid * 16) * 1280) : "memory"),
          NOP_, NOP_, NOP_, NOP_, NOP_, NOP_, NOP_);
  if (kh == 0) {
#pragma unroll
    for (int j = 0; j < 5; ++j) {
      const f32x4 og = red[j * 64];
      const u32x2 bb = gv[j];
      float t[4] = {acc[j][0] + og[0] + bf_lo(bb[0]), acc[j][1] + og[1] + bf_hi(bb[0]), acc[j][2] + og[2] + bf_lo(bb[1]),
                    acc[j][3] + og[3] + bf_hi(bb[1])};
#pragma unroll
      for (int e = 0; e < 4; ++e)
        t[e] = t[e] * __builtin_amdgcn_rcpf(1.0f + __builtin_amdgcn_exp2f(t[e] * -1.4426950408889634f));
      gv[j] = u32x2{cvtpk(t[0], t[1]), cvtpk(t[2], t[3])};
      PIN(gv[j]);
    }
  }
  SBAR();
  PLAIN_O(6, NOP_, NOP_, NOP_, NOP_);
  PLAIN_E(7, NOP_, NOP_, NOP_, NOP_);
  PLAIN_O(7, NOP_, NOP_, NOP_, NOP_);
  o = MFMA32(v1B, __builtin_bit_cast(bf16x8, p1), o);

  STAMP(1, 3);
  STAMPC(1, 15);
  STAMP2(2, 8);
  const int qtok = qb * 256 + wid * 32 + r;
  float l = __uint_as_float(__builtin_amdgcn_permlane32_swap(__float_as_uint(o[8]), __float_as_uint(o[8]), false, false)[1]);
  if (__builtin_expect(__any(!(l >= 7.888609e-31f) || !(l < 3.0e38f)), 0)) {
    if (h == 1) { qbv[0] = 0u; }
    const bf16x4 qz = __builtin_bit_cast(bf16x4, qbv);
    float m = -3.0e38f;
#pragma unroll
    for (int i = 0; i < 16; ++i) o[i] = 0.f;
    const unsigned onev = r == 20 ? 0x3f803f80u : 0u;
#pragma unroll 1
    for (int t = 0; t < 64; ++t) {
      const bf16x8 ka = *(const bf16x8*)(Kh + t * 1280 + lane * 16);
      u32x2 kb = *(const u32x2*)(Kh + t * 1280 + 1024 + r * 8);
      u32x4 v0 = {onev, onev, onev, onev}, v1 = v0;
      if (r < 20) {
        v0 = *(const u32x4*)(Vh + t * 1280 + vv);
        v1 = *(const u32x4*)(Vh + t * 1280 + 640 + vv);
      }
      f32x16 s = MFMA32(ka, qa, zero);
      s = MFMA32K8(__builtin_bit_cast(bf16x4, kb), qz, s);
      float tm = s[0];
#pragma unroll
      for (int i = 1; i < 16; ++i) tm = fmaxf(tm, s[i]);
      tm = fmaxf(tm, xhalf(tm));
      const float mn = fmaxf(m, tm);
      const float al = __builtin_amdgcn_exp2f(m - mn);
      m = mn;
      u32x4 q0, q1;
#pragma unroll
      for (int i = 0; i < 16; ++i) { o[i] *= al; s[i] = __builtin_amdgcn_exp2f(s[i] - mn); }
#pragma unroll
      for (int e = 0; e < 4; ++e) { q0[e] = cvtpk(s[2 * e], s[2 * e + 1]); q1[e] = cvtpk(s[8 + 2 * e], s[9 + 2 * e]); }
      o = MFMA32(__builtin_bit_cast(bf16x8, v0), __builtin_bit_cast(bf16x8, q0), o);
      o = MFMA32(__builtin_bit_cast(bf16x8, v1), __builtin_bit_cast(bf16x8, q1), o);
    }
    l = __shfl(o[8], 32 + r);
  }
  attn_store(ws, o, __builtin_amdgcn_rcpf(l), head, qtok, h);
  publish(ws, qb);
  STAMP(1, 4);
  bf16x8 wfa[5][5];
#pragma unroll
  for (int c = 0; c < 5; ++c)
#pragma unroll
    for (int t = 0; t < 5; ++t) {
      wfa[c][t] = cat8(tr_read(LP(wra) + c * 10240 + t * 32), tr_read(LP(wra) + c * 10240 + t * 32 + 1280));
      PIN(wfa[c][t]);
    }
#pragma unroll
  for (int j = 0; j < 5; ++j) acc[j] = f32x4{0.f, 0.f, 0.f, 0.f};
  STAMP(1, 6);
  await(ws, rbk >> 2, 32u, wid);
  STAMP(1, 7);
  {
    const srd_t ro = make_srd(ws + WS_O, (unsigned)(WS_G - WS_O));
    const unsigned oa = (unsigned)(((rbk * 4 + mi) * 20 + kh) * 1024);
    u32x4 af[10];
#pragma unroll
    for (int c = 0; c < 10; ++c) af[c] = bload16_dev(ro, vlin, oa + c * 2048u);
    SBAR();
    bf16x8 wfb[5][5];
#pragma unroll
    for (int c = 5; c < 10; ++c) {
      const unsigned wa = c < 8 ? wrb + (unsigned)((c - 4) * 10240) : wra - (unsigned)KB_WR + (unsigned)((c - 8) * 10240);
      if (c < 8) {
#pragma unroll
        for (int t = 0; t < 5; ++t) wfb[c - 5][t] = cat8(tr_read(LP(wa) + t * 32), tr_read(LP(wa) + t * 32 + 1280));
      }
    }
    SBAR();
#pragma unroll
    for (int c = 0; c < 5; ++c)
#pragma unroll
      for (int t = 0; t < 5; ++t) acc[t] = MFMA16(wfa[c][t], __builtin_bit_cast(bf16x8, af[c]), acc[t]);
#pragma unroll
    for (int c = 8; c < 10; ++c)
#pragma unroll
      for (int t = 0; t < 5; ++t)
        wfb[c - 5][t] = cat8(tr_read(LP(wra - (unsigned)KB_WR) + (c - 8) * 10240 + t * 32), tr_read(LP(wra - (unsigned)KB_WR) + (c - 8) * 10240 + t * 32 + 1280));
#pragma unroll
    for (int c = 5; c < 10; ++c)
#pragma unroll
      for (int t = 0; t < 5; ++t) acc[t] = MFMA16(wfb[c - 5][t], __builtin_bit_cast(bf16x8, af[c]), acc[t]);
  }
  if (kh == 1) {
#pragma unroll
    for (int j = 0; j < 5; ++j) red[j * 64] = acc[j];
  }
  LBAR(0);
  if (kh == 0) {
#pragma unroll
    for (int j = 0; j < 5; ++j) {
      const f32x4 og = red[j * 64];
      const float r0 = bf_lo(xv[j][0]) + (acc[j][0] + og[0]) * bf_lo(gv[j][0]);
      const float r1 = bf_hi(xv[j][0]) + (acc[j][1] + og[1]) * bf_hi(gv[j][0]);
      const float r2 = bf_lo(xv[j][1]) + (acc[j][2] + og[2]) * bf_lo(gv[j][1]);
      const float r3 = bf_hi(xv[j][1]) + (acc[j][3] + og[3]) * bf_hi(gv[j][1]);
      *(u32x2*)(out + xo + j * 32) = u32x2{cvtpk(r0, r1), cvtpk(r2, r3)};
    }
  }
  STAMP(1, 8);
  asm volatile("kb_tail_label:");
}

extern "C" void kernel_launch(void* const* d_in, const int* in_sizes, int n_in, void* d_out, int out_size, void* d_ws,
                              size_t ws_size, hipStream_t stream) {
  (void)in_sizes; (void)n_in; (void)out_size; (void)ws_size;
  const char* x = (const char*)d_in[0];
  const char* wqkv = (const char*)d_in[1];
  const char* wo = (const char*)d_in[2];
  const char* w1 = (const char*)d_in[4];
  const unsigned short* b1 = (const unsigned short*)d_in[5];
  const char* w2 = (const char*)d_in[6];
  const unsigned short* b2 = (const unsigned short*)d_in[7];
  char* ws = (char*)d_ws;
  (void)hipFuncSetAttribute((const void*)ka_proj, hipFuncAttributeMaxDynamicSharedMemorySize, KA_LDS);
  (void)hipFuncSetAttribute((const void*)kb_main, hipFuncAttributeMaxDynamicSharedMemorySize, KB_LDS);
  ka_proj<<<256, 512, KA_LDS, stream>>>(x, wqkv, w1, b1, w2, wo, ws);
  kb_main<<<256, 512, KB_LDS, stream>>>(x, w2, b2, wo, ws, (char*)d_out);
}
```

```cpp
#include <hip/hip_runtime.h>
#include <hip/hip_bf16.h>
#include <stdint.h>

typedef __attribute__((ext_vector_type(8))) short bf16x8;
typedef __attribute__((ext_vector_type(4))) short bf16x4;
typedef __attribute__((ext_vector_type(16))) float f32x16;
typedef __attribute__((ext_vector_type(4))) float f32x4;
typedef __attribute__((ext_vector_type(2))) float f32x2;
typedef __attribute__((ext_vector_type(4))) unsigned u32x4;
typedef __attribute__((ext_vector_type(2))) unsigned u32x2;
typedef __attribute__((ext_vector_type(2))) __bf16 bf2;
typedef __attribute__((address_space(3))) char* lds_ptr;
#define LDS_AS __attribute__((address_space(3)))
#define DI __device__ __forceinline__

#define MFMA32(a, b, c) __builtin_amdgcn_mfma_f32_32x32x16_bf16((a), (b), (c), 0, 0, 0)
#define MFMA32K8(a, b, c) __builtin_amdgcn_mfma_f32_32x32x8bf16_1k((a), (b), (c), 0, 0, 0)
#define MFMA16(a, b, c) __builtin_amdgcn_mfma_f32_16x16x32_bf16((a), (b), (c), 0, 0, 0)
#define SBAR() __builtin_amdgcn_sched_barrier(0)
#define PIN(x) asm volatile("" : "+v"(x))
#define WAIT_BAR(N) asm volatile("s_waitcnt vmcnt(" #N ") lgkmcnt(0)\n\ts_barrier" ::: "memory")

constexpr int S_ = 2048, D_ = 640, NH = 32, F_ = 1536;
constexpr float LN_EPS = 1e-5f;
constexpr float QSCALE = 0.22360679774997896f * 1.4426950408889634f;

constexpr size_t WS_Q = 0;
constexpr size_t WS_K = WS_Q + (size_t)NH * 64 * 1280;
constexpr size_t WS_V = WS_K + (size_t)NH * 64 * 1280;
constexpr size_t WS_H = WS_V + (size_t)NH * 64 * 1280;
constexpr size_t WS_O = WS_H + (size_t)128 * 48 * 1024;
constexpr size_t WS_G = WS_O + (size_t)128 * 20 * 1024;
constexpr size_t WS_CNT = WS_G;
constexpr size_t WS_END = WS_CNT + 32 * 256;

#define STAMP(kern, i) do { } while (0)
#define STAMPC(kern, i) do { } while (0)
#define STAMP2(kern, i) do { } while (0)

#define STPOL 2
DI void st16(void* p, u32x4 v) {
  asm volatile("global_store_dwordx4 %0, %1, off sc1\n\ts_nop 1" :: "v"(p), "v"(v) : "memory");
}
DI void st8(void* p, u32x2 v) {
  asm volatile("global_store_dwordx2 %0, %1, off sc1" :: "v"(p), "v"(v) : "memory");
}
DI void st16s(const void* sb, unsigned off, u32x4 v) { asm volatile("global_store_dwordx4 %0, %1, %2 sc1\n\ts_nop 1" :: "v"(off), "v"(v), "s"(sb) : "memory"); }
DI void st8s(const void* sb, unsigned off, u32x2 v) { asm volatile("global_store_dwordx2 %0, %1, %2 sc1" :: "v"(off), "v"(v), "s"(sb) : "memory"); }
DI unsigned cvtpk(float lo, float hi) {
  f32x2 f = {lo, hi};
  return __builtin_bit_cast(unsigned, __builtin_convertvector(f, bf2));
}
DI float bf_lo(unsigned u) { return __uint_as_float(u << 16); }
DI float bf_hi(unsigned u) { return __uint_as_float(u & 0xffff0000u); }
DI float dot2(unsigned a, unsigned b, float c) {
  return __builtin_amdgcn_fdot2_f32_bf16(__builtin_bit_cast(bf2, a), __builtin_bit_cast(bf2, b), c, false);
}
DI float xhalf(float v) {
  return __shfl_xor(v, 32);
}
DI void glds16(unsigned voff, const void* sbase, unsigned ldsdst) {
  unsigned keep;
  asm volatile("s_mov_b32 %0, m0\n\ts_mov_b32 m0, %3\n\ts_nop 0\n\tglobal_load_lds_dwordx4 %1, %2\n\ts_mov_b32 m0, %0"
               : "=&s"(keep) : "v"(voff), "s"(sbase), "s"(ldsdst) : "memory");
}
DI void glds16_masked(unsigned voff, const void* sbase, unsigned ldsdst, unsigned mlo, unsigned mhi) {
  unsigned keep;
  asm volatile("s_mov_b32 %0, m0\n\ts_mov_b32 m0, %3\n\ts_mov_b32 exec_lo, %4\n\ts_mov_b32 exec_hi, %5\n\t"
               "global_load_lds_dwordx4 %1, %2\n\ts_mov_b64 exec, -1\n\ts_mov_b32 m0, %0"
               : "=&s"(keep) : "v"(voff), "s"(sbase), "s"(ldsdst), "s"(mlo), "s"(mhi) : "memory");
}
typedef short v4i16_t __attribute__((ext_vector_type(4)));
DI bf16x4 tr_read(lds_ptr p) {
  return __builtin_bit_cast(bf16x4, __builtin_amdgcn_ds_read_tr16_b64_v4i16((LDS_AS v4i16_t*)p));
}
DI bf16x8 cat8(bf16x4 a, bf16x4 b) { return __builtin_shufflevector(a, b, 0, 1, 2, 3, 4, 5, 6, 7); }

constexpr int KA_BUF = 45056;
constexpr int KA_STAT = 3 * KA_BUF;
constexpr int KA_SCS = KA_STAT + 2048, KA_SB1 = KA_SCS + 3584, KA_SMR = KA_SB1 + 384;
constexpr int KA_LDS = KA_SMR + 2048;
#define KABL 0
#define LBAR(N) asm volatile("s_waitcnt lgkmcnt(" #N ")\n\ts_barrier" ::: "memory")
#define LP(a) ((lds_ptr)(uintptr_t)(a))
#define SET_M0(v) asm volatile("s_mov_b32 m0, %0\n\ts_nop 0" :: "s"(v) : "memory")
#define NOP_ do { } while (0)
#define MFMA4(a, b, c) __builtin_amdgcn_mfma_f32_4x4x4bf16_1k((a), (b), (c), 0, 0, 0)

typedef __amdgpu_buffer_rsrc_t srd_t;
DI srd_t make_srd(const void* p, unsigned bytes) { return __builtin_amdgcn_make_buffer_rsrc((void*)p, (short)0, (int)bytes, 0x00020000); }
DI float x32_add(float v) {
  const auto rr = __builtin_amdgcn_permlane32_swap(__float_as_uint(v), __float_as_uint(v), false, false);
  return __uint_as_float(rr[0]) + __uint_as_float(rr[1]);
}
DI float nanmax(float a, float b) { return (b > a || b != b) ? b : a; }
template <int CTRL> DI float dpp_get(float v) { return __uint_as_float(__builtin_amdgcn_update_dpp(0u, __float_as_uint(v), CTRL, 0xf, 0xf, true)); }
DI float wave_nanmax(float v) {
  v = nanmax(v, dpp_get<0xB1>(v));
  v = nanmax(v, dpp_get<0x4E>(v));
  v = nanmax(v, dpp_get<0x141>(v));
  v = nanmax(v, dpp_get<0x140>(v));
  const auto a = __builtin_amdgcn_permlane16_swap(__float_as_uint(v), __float_as_uint(v), false, false);
  v = nanmax(__uint_as_float(a[0]), __uint_as_float(a[1]));
  const auto b = __builtin_amdgcn_permlane32_swap(__float_as_uint(v), __float_as_uint(v), false, false);
  return nanmax(__uint_as_float(b[0]), __uint_as_float(b[1]));
}
DI bf16x4 lo4(bf16x8 v) { return __builtin_shufflevector(v, v, 0, 1, 2, 3); }
DI bf16x4 hi4(bf16x8 v) { return __builtin_shufflevector(v, v, 4, 5, 6, 7); }

template <int TYPE, int nh>
DI void ka_body(lds_ptr lds, const char* __restrict__ x, const char* __restrict__ wqkv, const char* __restrict__ wh1,
                const unsigned short* __restrict__ b1, const char* __restrict__ w2, const char* __restrict__ wo,
                const int n0, const int f0, const int rb, const int cb, char* __restrict__ ws) {
  constexpr int NHT = nh ? 1 : 2;
  const int tid = threadIdx.x, lane = tid & 63;
  const int w = __builtin_amdgcn_readfirstlane(tid >> 6);
  const int mi = w & 3;
  const int r = lane & 31, h = lane >> 5;
  const unsigned lds0 = (unsigned)(uintptr_t)lds;
  STAMP(0, 0);
  const unsigned vx0 = (unsigned)((lane >> 3) * 1280 + (((lane & 7) ^ (lane >> 4)) << 4));
  const unsigned vx1 = (unsigned)((8 + (lane >> 3)) * 1280 + (((lane & 7) ^ (4 + (lane >> 4))) << 4) - 1024);
  const unsigned vw = (unsigned)((lane >> 3) * 3840 + ((((((lane >> 2) ^ (lane >> 4)) & 1) << 2) | (lane & 3)) << 4));
  const int p0 = w * 96 + lane, p1 = w * 96 + 64 + r;
  const int r0 = (p0 * 5462) >> 16, r1 = (p1 * 5462) >> 16;
  const unsigned vh0 = (unsigned)(r0 * 3072 + (p0 - r0 * 12) * 16), vh1 = (unsigned)(r1 * 3072 + (p1 - r1 * 12) * 16 - 1024);
  const char* xs = x + (size_t)(rb * 128 + 16 * w) * 1280;
  const char* wsrc = wqkv + ((size_t)(8 * w) * 1920 + n0) * 2;
  const char* hsrc = wh1 + f0 * 2;
  const unsigned mx = lds0 + (unsigned)w * 2048u, mw = lds0 + 16384u + (unsigned)w * 1024u, mh = lds0 + 32768u + (unsigned)w * 1536u;
#define KA_ROT 0
  const int rot = KA_ROT ? (rb * 2 + cb * 5) % 9 : 0;
#define KC(c) ((c) == 0 ? 0 : ((c) + rot >= 10 ? (c) + rot - 9 : (c) + rot))
#define KA_DX(c, buf) do { SET_M0(mx + (unsigned)(buf)); asm volatile("global_load_lds_dwordx4 %0, %2\n\tglobal_load_lds_dwordx4 %1, %2 offset:1024" :: "v"(vx0), "v"(vx1), "s"(xs + KC(c) * 128) : "memory"); } while (0)
#define KA_DW(c, buf, hf) do { SET_M0(mw + (unsigned)(buf) + (hf) * 8192u); asm volatile("global_load_lds_dwordx4 %0, %1" :: "v"(vw), "s"(wsrc + (size_t)KC(c) * (64 * 3840) + (hf) * 128) : "memory"); } while (0)
#define KA_DH(c, buf) do { SET_M0(mh + (unsigned)(buf)); asm volatile("global_load_lds_dwordx4 %0, %2\n\ts_mov_b32 exec_hi, 0\n\tglobal_load_lds_dwordx4 %1, %2 offset:1024\n\ts_mov_b32 exec_hi, -1" :: "v"(vh0), "v"(vh1), "s"(hsrc + (size_t)KC(c) * (64 * 3072)) : "memory"); } while (0)
#define KA_ISSUE(c, buf) do { KA_DX(c, buf); KA_DW(c, buf, 0); KA_DW(c, buf, 1); KA_DH(c, buf); } while (0)
  unsigned braw = 0, warm = 0;
  if (nh == 0 && tid < 96) braw = b1[f0 + tid];
  SBAR();
  KA_ISSUE(0, 0);
  KA_ISSUE(1, KA_BUF);

  unsigned xa[4], wa0[4], ha[4];
  {
    const unsigned wb = lds0 + (unsigned)(16384 + nh * 8192 + (8 * h + ((lane & 15) >> 2)) * 128 + 64 * ((lane >> 3) & 1) +
                                          (16 * ((lane >> 4) & 1) + 4 * (lane & 3)) * 2);
    const unsigned hb = lds0 + (unsigned)(32768 + (8 * h + ((lane & 15) >> 2)) * 192 + nh * 128 + (16 * ((lane >> 4) & 1) + 4 * (lane & 3)) * 2);
#pragma unroll
    for (int i = 0; i < 4; ++i) {
      const int ks = i ^ mi;
      xa[i] = lds0 + (unsigned)((mi * 32 + r) * 128 + (((2 * ks + h) ^ ((r >> 1) & 7)) << 4));
      wa0[i] = wb + (unsigned)(ks * 2048);
      ha[i] = hb + (unsigned)(ks * 3072);
    }
  }
  const int tq = rb * 4 + mi;
  const bool isq = n0 < 640;
  unsigned so[TYPE == 1 ? 2 : 8];
  if (TYPE == 1) {
#pragma unroll
    for (int j = 0; j < 2; ++j) {
      const int nn = n0 - 1280 + nh * 64 + j * 32 + r;
      const int head = (nn * 3277) >> 16;
      so[j] = (unsigned)(head * (64 * 1280) + (h * 20 + nn - head * 20) * 16);
      PIN(so[j]);
    }
  } else {
#pragma unroll
    for (int j = 0; j < 8; ++j) {
      const int G = (((isq ? n0 : n0 - 640) + nh * 64) >> 2) + h + 2 * j;
      const int head = (G * 205) >> 10;
      const int dg = G - head * 5;
      so[j] = (unsigned)(head * (64 * 1280) + (dg < 4 ? (dg >> 1) * 512 + r * 16 + (dg & 1) * 8 : 1024 + r * 8));
      PIN(so[j]);
    }
  }
  f32x16 acc[2], ach[NHT];
  f32x4 cq[2], chh[NHT], rsum = {0.f, 0.f, 0.f, 0.f}, rsq = rsum;
#pragma unroll
  for (int i = 0; i < 16; ++i) {
    acc[0][i] = 0.f; acc[1][i] = 0.f;
#pragma unroll
    for (int j = 0; j < NHT; ++j) ach[j][i] = 0.f;
  }
  cq[0] = rsum; cq[1] = rsum;
#pragma unroll
  for (int j = 0; j < NHT; ++j) chh[j] = rsum;
  bf16x8 sel;
  {
    const short sv = (lane & 15) == ((lane >> 4) & 1) ? (short)0x3f80 : (short)0;
    sel = bf16x8{sv, sv, sv, sv, sv, sv, sv, sv};
    PIN(sel);
  }
  bf16x8 xf[3], w0[3], w1[3], wh[3][NHT];
  unsigned wa1[4];
#pragma unroll
  for (int i = 0; i < 4; ++i) wa1[i] = wa0[i] ^ 64u;
#define KA_READ(B, i)                                                                                           \
  do {                                                                                                          \
    constexpr int s_ = ((B) + (i)) % 3;                                                                         \
    if ((KABL & 8) && started) break;                                                                           \
    xf[s_] = *(const LDS_AS bf16x8*)(LP(xa[i]) + BUFOFF(B));                                                    \
    if ((KABL & 16) && started) break;                                                                          \
    w0[s_] = cat8(tr_read(LP(wa0[i]) + BUFOFF(B)), tr_read(LP(wa0[i]) + BUFOFF(B) + 512));                      \
    w1[s_] = cat8(tr_read(LP(wa1[i]) + BUFOFF(B)), tr_read(LP(wa1[i]) + BUFOFF(B) + 512));                      \
    _Pragma("unroll") for (int j_ = 0; j_ < NHT; ++j_)                                                          \
      wh[s_][j_] = cat8(tr_read(LP(ha[i]) + BUFOFF(B) + j_ * 64), tr_read(LP(ha[i]) + BUFOFF(B) + j_ * 64 + 768)); \
  } while (0)
#define SGB(m, n) __builtin_amdgcn_sched_group_barrier((m), (n), 0)
#define KA_STEP(B, i, RD) KA_STEPX(B, i, RD, true)
#define KA_STEPX(B, i, RD, ROWS)                                                                                \
  do {                                                                                                          \
    constexpr int b_ = ((B) + (i)) % 3;                                                                         \
    if constexpr (NHT == 2) asm volatile("" : "+v"(xf[b_]), "+v"(w0[b_]), "+v"(w1[b_]), "+v"(wh[b_][0]), "+v"(wh[b_][NHT - 1])); \
    else asm volatile("" : "+v"(xf[b_]), "+v"(w0[b_]), "+v"(w1[b_]), "+v"(wh[b_][0]));                          \
    SBAR();                                                                                                     \
    RD;                                                                                                         \
    if (TYPE == 1) { acc[0] = MFMA32(xf[b_], w0[b_], acc[0]); acc[1] = MFMA32(xf[b_], w1[b_], acc[1]); }        \
    else { acc[0] = MFMA32(w0[b_], xf[b_], acc[0]); acc[1] = MFMA32(w1[b_], xf[b_], acc[1]); }                  \
    _Pragma("unroll") for (int j_ = 0; j_ < NHT; ++j_) ach[j_] = MFMA32(wh[b_][j_], xf[b_], ach[j_]);           \
    SGB(0x8, 1); SGB(0x100, 3); SGB(0x8, 1); SGB(0x100, 2); SGB(0x8, 1); SGB(0x100, 2);                         \
    if (NHT == 2) { SGB(0x8, 1); SGB(0x100, 2); }                                                               \
    if constexpr (NHT == 2) asm volatile("" : "+v"(acc[0]), "+v"(acc[1]), "+v"(ach[0]), "+v"(ach[NHT - 1]));    \
    else asm volatile("" : "+v"(acc[0]), "+v"(acc[1]), "+v"(ach[0]));                                           \
    SBAR();                                                                                                     \
    if ((i) == 0 && !(KABL & 1)) {                                                                              \
      cq[0] = MFMA16(w0[b_], sel, cq[0]); cq[1] = MFMA16(w1[b_], sel, cq[1]);                                   \
      _Pragma("unroll") for (int j_ = 0; j_ < NHT; ++j_) chh[j_] = MFMA16(wh[b_][j_], sel, chh[j_]);            \
      if constexpr (NHT == 2) asm volatile("" : "+v"(cq[0]), "+v"(cq[1]), "+v"(chh[0]), "+v"(chh[NHT - 1]));    \
      else asm volatile("" : "+v"(cq[0]), "+v"(cq[1]), "+v"(chh[0]));                                           \
    }                                                                                                           \
    if ((ROWS) && (nh ? (i) < 2 : (i) >= 2) && !(KABL & 2)) {                                                   \
      rsq = MFMA4(lo4(xf[b_]), lo4(xf[b_]), rsq); rsum = MFMA16(xf[b_], sel, rsum);                             \
      rsq2 = MFMA4(hi4(xf[b_]), hi4(xf[b_]), rsq2);                                                             \
      asm volatile("" : "+v"(rsum), "+v"(rsq), "+v"(rsq2));                                                     \
    }                                                                                                           \
    SBAR();                                                                                                     \
  } while (0)
#define BUFOFF(B) ((B) * KA_BUF)
#define KA_CHUNK(c, B, BN, WAITN, PREV, MORE)                                                                   \
  do {                                                                                                          \
    KA_STEP(B, 0, KA_READ(B, 2));                                                                               \
    if ((PREV) && !(KABL & 4)) { KA_DH((c) + 2, BUFOFF(((B) + 2) % 3)); SBAR(); }                               \
    KA_STEP(B, 1, KA_READ(B, 3));                                                                               \
    if (!(KABL & 32)) WAIT_BAR(WAITN);                                                                          \
    KA_STEP(B, 2, KA_READ(BN, 0));                                                                              \
    if ((MORE) && !(KABL & 4)) { KA_DX((c) + 3, BUFOFF(B)); SBAR(); }                                           \
    KA_STEP(B, 3, KA_READ(BN, 1));                                                                              \
    if ((MORE) && !(KABL & 4)) { KA_DW((c) + 3, BUFOFF(B), 0); KA_DW((c) + 3, BUFOFF(B), 1); SBAR(); }          \
  } while (0)
  f32x4 rsq2 = rsum;
  WAIT_BAR(6);
  STAMP(0, 1);
  STAMPC(0, 14);
  bool started = false;
  KA_READ(0, 0);
  KA_READ(0, 1);
  if (KABL & 24) { KA_READ(0, 2); started = true; }
  KA_STEP(0, 0, KA_READ(0, 2));
  KA_DX(2, BUFOFF(2)); KA_DW(2, BUFOFF(2), 0); KA_DW(2, BUFOFF(2), 1); SBAR();
  KA_STEP(0, 1, KA_READ(0, 3));
  KA_DH(2, BUFOFF(2)); SBAR();
  WAIT_BAR(6);
  KA_STEP(0, 2, KA_READ(1, 0));
  KA_DX(3, BUFOFF(0)); SBAR();
  KA_STEP(0, 3, KA_READ(1, 1));
  KA_DW(3, BUFOFF(0), 0); KA_DW(3, BUFOFF(0), 1); SBAR();
#pragma unroll 1
  for (int c = 1; c < 7; c += 3) {
    KA_CHUNK(c, 1, 2, 6, true, true);
    KA_CHUNK(c + 1, 2, 0, 6, true, true);
    KA_CHUNK(c + 2, 0, 1, 6, true, true);
  }
  KA_CHUNK(7, 1, 2, 6, true, false);
  KA_CHUNK(8, 2, 0, 0, false, false);
  KA_STEP(0, 0, KA_READ(0, 2));
  KA_STEP(0, 1, KA_READ(0, 3));
  if (nh == 0) {
    asm volatile("" : "+v"(xf[2]), "+v"(xf[0]));
    rsq = MFMA4(lo4(xf[2]), lo4(xf[2]), rsq); rsum = MFMA16(xf[2], sel, rsum); rsq2 = MFMA4(hi4(xf[2]), hi4(xf[2]), rsq2);
    rsum = MFMA16(xf[0], sel, rsum); rsq = MFMA4(lo4(xf[0]), lo4(xf[0]), rsq); rsq2 = MFMA4(hi4(xf[0]), hi4(xf[0]), rsq2);
  }
  asm volatile("s_nop 7" : "+v"(rsum), "+v"(rsq), "+v"(rsq2));
  rsq += rsq2;
  STAMP(0, 3);
  STAMPC(0, 15);
  LDS_AS float* st_r = (LDS_AS float*)(lds + KA_STAT);
  LDS_AS float* st_cs = (LDS_AS float*)(lds + KA_SCS);
  LDS_AS float* st_b = (LDS_AS float*)(lds + KA_SB1);
  {
    const int d = lane & 3;
    const float sq = x32_add(d == 0 ? rsq[0] : d == 1 ? rsq[1] : d == 2 ? rsq[2] : rsq[3]);
    if (h == 0) st_r[256 + nh * 128 + mi * 32 + r] = sq;
    if ((lane & 15) < 2) {
      const int q4 = (lane & 1) * 16 + (lane >> 4) * 4;
      *(LDS_AS f32x4*)(st_r + nh * 128 + mi * 32 + q4) = rsum;
      *(LDS_AS f32x4*)(st_cs + mi * 224 + nh * 64 + q4) = cq[0];
      *(LDS_AS f32x4*)(st_cs + mi * 224 + nh * 64 + 32 + q4) = cq[1];
#pragma unroll
      for (int j = 0; j < NHT; ++j) *(LDS_AS f32x4*)(st_cs + mi * 224 + 128 + nh * 64 + j * 32 + q4) = chh[j];
    }
    if (nh == 0 && tid < 96) st_b[tid] = __uint_as_float(braw << 16);
  }
  SBAR();
  KA_STEPX(0, 2, NOP_, false);
  LBAR(0);
  float cpart = 0.f;
  if (tid < 224) cpart = (st_cs[tid] + st_cs[224 + tid]) + (st_cs[448 + tid] + st_cs[672 + tid]);
  const float sx_ = st_r[mi * 32 + r] + st_r[128 + mi * 32 + r], sq_ = st_r[256 + mi * 32 + r] + st_r[384 + mi * 32 + r];
  SBAR();
  KA_STEPX(0, 3, NOP_, false);
#undef KA_READ
#undef KA_STEP
#undef KA_STEPX
#undef KA_CHUNK
#undef KA_ISSUE
  if (tid < 224) st_cs[tid] = cpart;
  if (nh == 1) {
    const int l0_ = blockIdx.x * 85 + (tid - 256 < 85 ? tid - 256 : 0);
    const char* p = l0_ < 15360 ? w2 + (size_t)l0_ * 128 : wo + (size_t)(l0_ - 15360) * 128;
    if (w < 6) asm volatile("global_load_dword %0, %1, off" : "=v"(warm) : "v"(p) : "memory");
  }
  const float mu = sx_ * (1.0f / D_);
  const float var = fmaxf(sq_ * (1.0f / D_) - mu * mu, 0.f);
  const float rs = __builtin_amdgcn_rsqf(var + LN_EPS);
  LDS_AS f32x2* st_w = (LDS_AS f32x2*)(lds + KA_SMR) + w * 32;
  if (TYPE == 1 && h == 0) st_w[r] = f32x2{mu * rs, rs};
  LBAR(0);
  STAMP(0, 5);
  if (cb < 15) {
    if (TYPE == 1) {
      char* dst = ws + WS_V + (size_t)tq * 1280;
      const float cs0 = st_cs[nh * 64 + r], cs1 = st_cs[nh * 64 + 32 + r];
#pragma unroll
      for (int s = 0; s < 2; ++s) {
        f32x2 mr[8];
#pragma unroll
        for (int e = 0; e < 8; ++e) mr[e] = st_w[16 * s + 8 * (e >> 2) + 4 * h + (e & 3)];
        u32x4 o0, o1;
#pragma unroll
        for (int e = 0; e < 4; ++e) {
          const int i = 8 * s + 2 * e;
          o0[e] = cvtpk(acc[0][i] * mr[2 * e][1] - mr[2 * e][0] * cs0, acc[0][i + 1] * mr[2 * e + 1][1] - mr[2 * e + 1][0] * cs0);
          o1[e] = cvtpk(acc[1][i] * mr[2 * e][1] - mr[2 * e][0] * cs1, acc[1][i + 1] * mr[2 * e + 1][1] - mr[2 * e + 1][0] * cs1);
        }
        st16s(dst + s * 640, so[0], o0);
        st16s(dst + s * 640, so[1], o1);
      }
    } else {
      const float sc = isq ? rs * QSCALE : rs, ms = mu * sc;
      char* base = ws + (isq ? WS_Q : WS_K) + (size_t)tq * 1280;
#pragma unroll
      for (int j = 0; j < 2; ++j)
#pragma unroll
        for (int g = 0; g < 4; ++g) {
          const f32x4 cs = *(const LDS_AS f32x4*)(st_cs + nh * 64 + j * 32 + 8 * g + 4 * h);
          const u32x2 o = {cvtpk(acc[j][4 * g] * sc - ms * cs[0], acc[j][4 * g + 1] * sc - ms * cs[1]),
                           cvtpk(acc[j][4 * g + 2] * sc - ms * cs[2], acc[j][4 * g + 3] * sc - ms * cs[3])};
          st8s(base, so[j * 4 + g], o);
        }
    }
  }
  {
    const float sc = rs * __builtin_amdgcn_rsqf(var * rs * rs + LN_EPS), ms = mu * sc;
    constexpr float GK1 = -2.0f * 1.4426950408889634f * 0.7978845608028654f, GK2 = GK1 * 0.044715f;
    const char* dst = ws + WS_H + ((size_t)(rb * 8 + mi * 2) * 48 + (f0 >> 5) + nh * 2) * 1024;
    const unsigned ho = (unsigned)((r >> 4) * (48 * 1024) + (h * 16 + (r & 15)) * 16);
    const f32x2 sc2 = {sc, sc}, ms2 = {ms, ms}, k1 = {GK1, GK1}, k2 = {GK2, GK2}, one2 = {1.0f, 1.0f};
#pragma unroll
    for (int j = 0; j < NHT; ++j)
#pragma unroll
      for (int u = 0; u < 2; ++u) {
        u32x4 o;
#pragma unroll
        for (int gg = 0; gg < 2; ++gg) {
          const int g = 2 * u + gg;
          const f32x4 cs = *(const LDS_AS f32x4*)(st_cs + 128 + nh * 64 + j * 32 + 8 * g + 4 * h);
          const f32x4 bb = *(const LDS_AS f32x4*)(st_b + nh * 64 + j * 32 + 8 * g + 4 * h);
#pragma unroll
          for (int e = 0; e < 2; ++e) {
            const f32x2 a2 = {ach[j][4 * g + 2 * e], ach[j][4 * g + 2 * e + 1]};
            const f32x2 c2 = {cs[2 * e], cs[2 * e + 1]}, b2_ = {bb[2 * e], bb[2 * e + 1]};
            const f32x2 t = a2 * sc2 + (b2_ - ms2 * c2);
            const f32x2 arg = t * ((t * t) * k2 + k1);
            const f32x2 den = f32x2{__builtin_amdgcn_exp2f(arg[0]), __builtin_amdgcn_exp2f(arg[1])} + one2;
            const f32x2 v = t * f32x2{__builtin_amdgcn_rcpf(den[0]), __builtin_amdgcn_rcpf(den[1])};
            o[2 * gg + e] = cvtpk(v[0], v[1]);
          }
        }
        st16s(dst + j * 1024 + u * 512, ho, o);
      }
  }
  STAMP(0, 4);
  asm volatile("s_waitcnt vmcnt(0)" : "+v"(warm) :: "memory");
}

__global__ __launch_bounds__(512, 2) void ka_proj(const char* __restrict__ x, const char* __restrict__ wqkv,
                                                  const char* __restrict__ w1, const unsigned short* __restrict__ b1,
                                                  const char* __restrict__ w2, const char* __restrict__ wo,
                                                  char* __restrict__ ws) {
  extern __shared__ __attribute__((aligned(16))) char smem[];
  lds_ptr lds = (lds_ptr)smem;
  const int bid = blockIdx.x, xg = bid & 7, idx = bid >> 3;
  const int rb = (xg & 1) * 8 + (idx & 7), cb = (xg >> 1) * 4 + (idx >> 3);
  const int n0 = (cb < 15 ? cb : 14) * 128, f0 = cb * 96;
  if (bid == 0 && threadIdx.x < 32) *(unsigned*)(ws + WS_CNT + threadIdx.x * 256) = 0u;
  const int nh = __builtin_amdgcn_readfirstlane(threadIdx.x >> 8);
  if (cb < 10) {
    if (nh) ka_body<0, 1>(lds, x, wqkv, w1, b1, w2, wo, n0, f0, rb, cb, ws);
    else ka_body<0, 0>(lds, x, wqkv, w1, b1, w2, wo, n0, f0, rb, cb, ws);
  } else {
    if (nh) ka_body<1, 1>(lds, x, wqkv, w1, b1, w2, wo, n0, f0, rb, cb, ws);
    else ka_body<1, 0>(lds, x, wqkv, w1, b1, w2, wo, n0, f0, rb, cb, ws);
  }
}

typedef __attribute__((address_space(1))) unsigned gu32;
DI void st8_dev(void* p, u32x2 v) { asm volatile("global_store_dwordx2 %0, %1, off sc1" :: "v"(p), "v"(v) : "memory"); }
DI u32x4 bload16_dev(srd_t r, unsigned voff, unsigned soff) { return __builtin_amdgcn_raw_buffer_load_b128(r, (int)voff, (int)soff, 16); }
DI void ld16_asm(bf16x8& d, unsigned voff, const void* sbase) {
  asm volatile("global_load_dwordx4 %0, %1, %2" : "=v"(d) : "v"(voff), "s"(sbase) : "memory");
}
DI void publish(char* ws, int cnt) {
  asm volatile("s_waitcnt vmcnt(0) lgkmcnt(0)\n\ts_barrier" ::: "memory");
  if (threadIdx.x == 0) __hip_atomic_fetch_add((gu32*)(ws + WS_CNT + cnt * 256), 1u, __ATOMIC_RELAXED, __HIP_MEMORY_SCOPE_AGENT);
}
DI void await(char* ws, int cnt, unsigned want, int wid) {
  if (wid == 0) {
    for (unsigned spins = 0; spins < (1u << 16); ++spins) {
      const unsigned v = __hip_atomic_load((gu32*)(ws + WS_CNT + cnt * 256), __ATOMIC_RELAXED, __HIP_MEMORY_SCOPE_AGENT);
      if ((unsigned)__builtin_amdgcn_readfirstlane(v) >= want) break;
      __builtin_amdgcn_s_sleep(1);
    }
  }
  asm volatile("s_waitcnt vmcnt(0)\n\ts_barrier" ::: "memory");
}

constexpr int AT_TILE = 3584;
constexpr int AT_SLOT = 28672;
constexpr int KB_WR = 2 * AT_SLOT;
constexpr int KB_GRP = 40960;
constexpr int KB_RED = KB_WR + 2 * KB_GRP;
constexpr int KB_LDS = KB_RED + 20480;

__global__ __launch_bounds__(512, 2) void kb_main(const char* __restrict__ x, const char* __restrict__ w2,
                                                  const unsigned short* __restrict__ b2, const char* __restrict__ wo,
                                                  char* __restrict__ ws, char* __restrict__ out) {
  extern __shared__ __attribute__((aligned(16))) char smem[];
  lds_ptr lds = (lds_ptr)smem;
  const int tid = threadIdx.x, lane = tid & 63;
  const int wid = __builtin_amdgcn_readfirstlane(tid >> 6);
  const int r = lane & 31, h = lane >> 5;
  const int xg = blockIdx.x & 7, idx = blockIdx.x >> 3;
  const int head = xg * 4 + (idx >> 3), qb = idx & 7;
  const int rbk = xg * 4 + (idx & 3), cbk = idx >> 2;
  const int mi = wid & 3, kh = wid >> 2, gq = lane >> 4;
  const unsigned lds0 = (unsigned)(uintptr_t)lds;
  const char* Kh = ws + WS_K + (size_t)head * (64 * 1280);
  const char* Vh = ws + WS_V + (size_t)head * (64 * 1280);
  const char* Qt = ws + WS_Q + (size_t)(head * 64 + qb * 8 + wid) * 1280;
  STAMP(1, 0);

  const unsigned vlin = (unsigned)lane * 16u;
  const unsigned vv = (unsigned)(h * 20 + r) * 16u;
  const unsigned vv2 = vv + (unsigned)(WS_V - WS_K) - 1280u - 1536u, vv3 = vv2 + 640u - 1024u;
  const unsigned long long mk16 = 0xffffull, mk20 = 0x000fffff000fffffull;
#define KV_M0(c) SET_M0(lds0 + (unsigned)(((c) & 1) * AT_SLOT + wid * AT_TILE))
#define KV_SRC(c) (Kh + (size_t)((c) * 8 + wid + 1) * 1280)
#define KV_P0(c) asm volatile("global_load_lds_dwordx4 %0, %1" :: "v"(vlin), "s"(KV_SRC(c)) : "memory")
#define KV_P1(c) asm volatile("s_mov_b64 exec, %2\n\tglobal_load_lds_dwordx4 %0, %1 offset:1024\n\ts_mov_b64 exec, -1" :: "v"(vlin), "s"(KV_SRC(c)), "s"(mk16) : "memory")
#define KV_P23(c) asm volatile("s_mov_b64 exec, %3\n\tglobal_load_lds_dwordx4 %0, %2 offset:1536\n\tglobal_load_lds_dwordx4 %1, %2 offset:2560\n\ts_mov_b64 exec, -1" :: "v"(vv2), "v"(vv3), "s"(KV_SRC(c)), "s"(mk20) : "memory")
  bf16x8 qa, ka0;
  u32x2 qbv, kb0;
  {
    const unsigned v8 = 1024u + (unsigned)r * 8u;
    asm volatile("global_load_dwordx4 %0, %4, %6\n\tglobal_load_dwordx2 %1, %5, %6\n\t"
                 "global_load_dwordx4 %2, %4, %7\n\tglobal_load_dwordx2 %3, %5, %7"
                 : "=&v"(qa), "=&v"(qbv), "=&v"(ka0), "=&v"(kb0) : "v"(vlin), "v"(v8), "s"(Qt), "s"(Kh) : "memory");
  }
  KV_M0(0); KV_P0(0); KV_P1(0); KV_P23(0);
#pragma unroll
  for (int sl = 0; sl < 2; ++sl) {
    lds_ptr tp = lds + sl * AT_SLOT + wid * AT_TILE;
    if (lane < 32) *(LDS_AS u32x2*)(tp + 1024 + 256 + lane * 8) = u32x2{0x00003f80u, 0u};
    if (lane < 48) {
      const int blk4 = (lane * 43) >> 9, dd = 20 + lane - blk4 * 12;
      const unsigned val = dd == 20 ? 0x3f803f80u : 0u;
      *(LDS_AS u32x4*)(tp + 1536 + blk4 * 512 + dd * 16) = u32x4{val, val, val, val};
    }
  }
  unsigned vo[5];
#pragma unroll
  for (int i = 0; i < 5; ++i) {
    const int p = i * 64 + lane, row = (p * 6554) >> 16;
    vo[i] = (unsigned)(row * 1280 + (p - row * 10) * 16 - (i & 3) * 1024);
  }
#define BABL_W 0
#define W_SRC(W, grp) ((W) + cbk * 160 + (size_t)((grp) * 256 + wid * 32) * 1280)
#define W_M0(reg, hi) SET_M0(lds0 + (unsigned)(KB_WR + (reg) * KB_GRP + wid * 5120 + (hi) * 4096))
#define W_P(W, i, grp) if (!(BABL_W)) asm volatile("global_load_lds_dwordx4 %0, %1 offset:%2" :: "v"(vo[i]), "s"(W_SRC(W, grp)), "i"(((i) & 3) * 1024) : "memory")
  const char* hsrc = ws + WS_H + ((size_t)(rbk * 4 + mi) * 48 + kh) * 1024 + 4096;
#define H_LOAD(D, j, grp) asm volatile("global_load_dwordx4 %0, %1, %2 offset:%3" : "=v"(D[j]) : "v"(vlin), "s"(hsrc + (grp) * 8192), "i"((j) * 2048 - 4096) : "memory")
  const unsigned wra = lds0 + (unsigned)(KB_WR + (kh * 32 + 4 * ((gq & 1) + 4 * (gq >> 1)) + ((lane & 15) >> 2)) * 160 + (lane & 3) * 8);
  const unsigned wrb = wra + KB_GRP;

  unsigned oo[3];
#pragma unroll
  for (int g = 0; g < 3; ++g) {
    const int Gc = head * 5 + 2 * g + h, G8 = Gc & 7;
    oo[g] = (unsigned)((r >> 4) * (20 * 1024) + (r & 15) * 16 + (Gc >> 3) * 1024 + ((G8 >> 2) * 2 + (G8 & 1)) * 256 + ((G8 >> 1) & 1) * 8);
    PIN(oo[g]);
  }
  bf16x8 hA[4], hB[4];
  asm volatile("s_waitcnt vmcnt(4)" : "+v"(qa), "+v"(qbv), "+v"(ka0), "+v"(kb0) :: "memory");
  f32x16 o;
#pragma unroll
  for (int i = 0; i < 16; ++i) o[i] = 0.f;
  const f32x16 zero = o;
  if (h == 1) { qbv[0] = 0u; qbv[1] = 0u; kb0[0] = 0x00003f80u; kb0[1] = 0u; }
  const f32x16 s16 = MFMA32(ka0, qa, zero);
  f32x16 sA = MFMA32K8(__builtin_bit_cast(bf16x4, kb0), __builtin_bit_cast(bf16x4, qbv), s16);
  {
    float m = fmaxf(fmaxf(sA[0], sA[1]), sA[2]);
#pragma unroll
    for (int i = 3; i < 15; i += 2) m = fmaxf(fmaxf(m, sA[i]), sA[i + 1]);
    m = fmaxf(m, sA[15]);
    const auto mm = __builtin_amdgcn_permlane32_swap(__float_as_uint(m), __float_as_uint(m), false, false);
    m = fmaxf(__uint_as_float(mm[0]), __uint_as_float(mm[1]));
    if (h == 1) qbv[0] = cvtpk(-m, 0.f);
  }
  const bf16x4 qb4 = __builtin_bit_cast(bf16x4, qbv);
  sA = MFMA32K8(__builtin_bit_cast(bf16x4, kb0), qb4, s16);
  f32x16 sB;
  f32x4 acc[5];
#pragma unroll
  for (int j = 0; j < 5; ++j) acc[j] = f32x4{0.f, 0.f, 0.f, 0.f};
  WAIT_BAR(0);
  STAMP(1, 1);
  STAMPC(1, 14);
  STAMP2(2, 0);

  unsigned a16 = lds0 + vlin, a8 = lds0 + (unsigned)lane * 8u;
  int sdelta = AT_SLOT;
  bf16x8 kaA, v0A, v1A, kaB, v0B, v1B;
  bf16x4 kbA, kbB;
  bf16x4 wl[5], wu[5];
  u32x4 p0, p1 = {0u, 0u, 0u, 0u};
  v1B = __builtin_bit_cast(bf16x8, p1);
#define RD_KA(X, j) ka##X = *(const LDS_AS bf16x8*)(LP(a16) + (j) * AT_TILE)
#define RD_KB(X, j) kb##X = *(const LDS_AS bf16x4*)(LP(a8) + (j) * AT_TILE + 1024)
#define RD_V0(X, j) v0##X = *(const LDS_AS bf16x8*)(LP(a16) + (j) * AT_TILE + 1536)
#define RD_V1(X, j) v1##X = *(const LDS_AS bf16x8*)(LP(a16) + (j) * AT_TILE + 2560)
#define W_LO(WA, j, t) wl[t] = tr_read(LP(WA) + (j) * 10240 + (t) * 32)
#define W_HI(WA, j, t) wu[t] = tr_read(LP(WA) + (j) * 10240 + (t) * 32 + 1280)
#define BABL 0
#define G_MM(HS, j, t) do { if (BABL & 1) { PIN(wl[t]); PIN(wu[t]); PIN(HS[j]); } else { acc[t] = MFMA16(cat8(wl[t], wu[t]), HS[j], acc[t]); } } while (0)
#define EX2(S, i) do { S[i] = __builtin_amdgcn_exp2f(S[i]); S[i + 1] = __builtin_amdgcn_exp2f(S[i + 1]); } while (0)
#define MID() do { __builtin_amdgcn_sched_group_barrier(0x400, 2, 0); __builtin_amdgcn_sched_group_barrier(0x008, 1, 0); \
                    __builtin_amdgcn_sched_group_barrier(0x400, 2, 0); __builtin_amdgcn_sched_group_barrier(0x002, 2, 0); } while (0)
#define AT_STEP(SC, SN, X, Y, JN, PRE, WP, A0, A1, A2, A3, A4, A5, V0_, V1_, V2_, M1, M2, M3)            \
  do {                                                                                                   \
    PRE;                                                                                                 \
    asm volatile("" : "+v"(ka##X), "+v"(kb##X), "+v"(v0##X), "+v"(v1##X)); WP; SBAR();                   \
    EX2(SC, 0); PIN(SC); SBAR();                                                                         \
    o = MFMA32(v1##Y, __builtin_bit_cast(bf16x8, p1), o); SBAR();                                        \
    RD_KA(Y, JN); RD_KB(Y, JN); RD_V0(Y, JN); RD_V1(Y, JN); A0; A1; A2; A3; A4; A5; V0_; V1_; SBAR(); \
    M1; EX2(SC, 2); EX2(SC, 4); p0[0] = cvtpk(SC[0], SC[1]); p0[1] = cvtpk(SC[2], SC[3]); MID();         \
    asm volatile("" : "+v"(p0), "+v"(SC), "+v"(acc[0]), "+v"(acc[3])); SBAR();                           \
    SN = MFMA32(ka##X, qa, zero); SBAR();                                                                \
    M2; EX2(SC, 6); EX2(SC, 8); p0[2] = cvtpk(SC[4], SC[5]); p0[3] = cvtpk(SC[6], SC[7]); MID();         \
    asm volatile("" : "+v"(p0), "+v"(SC), "+v"(acc[1])); SBAR();                                         \
    SN = MFMA32K8(kb##X, qb4, SN); SBAR();                                                               \
    V2_; SBAR();                                                                                         \
    M3; EX2(SC, 10); EX2(SC, 12); p1[0] = cvtpk(SC[8], SC[9]); p1[1] = cvtpk(SC[10], SC[11]); MID();     \
    asm volatile("" : "+v"(p1), "+v"(SC), "+v"(acc[2]), "+v"(acc[4])); SBAR();                           \
    o = MFMA32(v0##X, __builtin_bit_cast(bf16x8, p0), o); SBAR();                                        \
    EX2(SC, 14); p1[2] = cvtpk(SC[12], SC[13]); p1[3] = cvtpk(SC[14], SC[15]); PIN(p1); SBAR();          \
  } while (0)
#define WP_E asm volatile("" : "+v"(wl[0]), "+v"(wu[0]), "+v"(wl[1]), "+v"(wu[1]), "+v"(wl[2]), "+v"(wu[2]))
#define WP_O asm volatile("" : "+v"(wl[3]), "+v"(wu[3]), "+v"(wl[4]), "+v"(wu[4]))
#define FLIP() do { a16 += (unsigned)sdelta; a8 += (unsigned)sdelta; sdelta = -sdelta; } while (0)
#define SYNC_H(HN) asm volatile("s_waitcnt vmcnt(3) lgkmcnt(0)\n\ts_barrier" : "+v"(HN[0]) :: "memory")
#define WAIT_H(HC, j, N) asm volatile("s_waitcnt vmcnt(" #N ")" : "+v"(HC[j]) :: "memory")
#define STEP_E(JN, PRE, WC, HC, j, VA, VB)                                                               \
  AT_STEP(sA, sB, A, B, JN, PRE, WP_E, W_LO(WC, j, 3), W_HI(WC, j, 3), W_LO(WC, j, 4), W_HI(WC, j, 4), NOP_, NOP_, NOP_, VA, VB, \
          G_MM(HC, j, 0), G_MM(HC, j, 1), G_MM(HC, j, 2))
#define STEP_O(JN, PRE, WA, jn, HC, j, VA, VB)                                                           \
  AT_STEP(sB, sA, B, A, JN, PRE, WP_O, W_LO(WA, jn, 0), W_HI(WA, jn, 0), W_LO(WA, jn, 1), W_HI(WA, jn, 1), W_LO(WA, jn, 2), \
          W_HI(WA, jn, 2), NOP_, VA, VB, G_MM(HC, j, 3), NOP_, G_MM(HC, j, 4))
#define CHUNK_G(c, WC, HC, WN, HN, RN, NW, NG, HG)                                                       \
  do {                                                                                                   \
    STEP_E(1, NOP_, WC, HC, 0, W_M0(RN, 0); W_P(NW, 0, NG), W_P(NW, 1, NG));                             \
    STEP_O(2, NOP_, WC, 1, HC, 0, W_P(NW, 2, NG), W_P(NW, 3, NG));                                       \
    STEP_E(3, WAIT_H(HC, 1, 6), WC, HC, 1, W_M0(RN, 1); W_P(NW, 4, NG), KV_M0((c) + 1); KV_P0((c) + 1)); \
    STEP_O(4, NOP_, WC, 2, HC, 1, KV_P1((c) + 1), KV_P23((c) + 1));                                      \
    STEP_E(5, WAIT_H(HC, 2, 10), WC, HC, 2, H_LOAD(HN, 0, HG), H_LOAD(HN, 1, HG));                       \
    STEP_O(6, NOP_, WC, 3, HC, 2, H_LOAD(HN, 2, HG), H_LOAD(HN, 3, HG));                                 \
    STEP_E(7, WAIT_H(HC, 3, 13), WC, HC, 3, NOP_, NOP_);                                                 \
    STEP_O(0, SYNC_H(HN); STAMP2(2, 1 + (c)); FLIP(), WN, 0, HC, 3, NOP_, NOP_);                         \
  } while (0)
#define PLAIN2_E(JN, VA, VB) AT_STEP(sA, sB, A, B, JN, NOP_, NOP_, NOP_, NOP_, NOP_, NOP_, NOP_, NOP_, NOP_, VA, VB, NOP_, NOP_, NOP_)
#define PLAIN2_O(JN, VA, VB) AT_STEP(sB, sA, B, A, JN, NOP_, NOP_, NOP_, NOP_, NOP_, NOP_, NOP_, NOP_, NOP_, VA, VB, NOP_, NOP_, NOP_)
#define PLAIN_E(JN, X0, X1, X2, X3) AT_STEP(sA, sB, A, B, JN, NOP_, NOP_, X0, X1, X2, X3, NOP_, NOP_, NOP_, NOP_, NOP_, NOP_, NOP_, NOP_)
#define PLAIN_O(JN, X0, X1, X2, X3) AT_STEP(sB, sA, B, A, JN, NOP_, NOP_, X0, X1, X2, X3, NOP_, NOP_, NOP_, NOP_, NOP_, NOP_, NOP_, NOP_)

  RD_KA(A, 0); RD_KB(A, 0); RD_V0(A, 0); RD_V1(A, 0);
  PLAIN2_E(1, W_M0(0, 0); W_P(w2, 0, 0), W_P(w2, 1, 0));
  PLAIN2_O(2, W_P(w2, 2, 0), W_P(w2, 3, 0));
  PLAIN2_E(3, W_M0(0, 1); W_P(w2, 4, 0), KV_M0(1); KV_P0(1));
  PLAIN2_O(4, KV_P1(1), KV_P23(1));
  PLAIN2_E(5, H_LOAD(hA, 0, 0), H_LOAD(hA, 1, 0));
  PLAIN2_O(6, H_LOAD(hA, 2, 0), H_LOAD(hA, 3, 0));
  PLAIN_E(7, NOP_, NOP_, NOP_, NOP_);
  AT_STEP(sB, sA, B, A, 0, SYNC_H(hA); STAMP2(2, 1); FLIP(), NOP_, W_LO(wra, 0, 0), W_HI(wra, 0, 0), W_LO(wra, 0, 1), W_HI(wra, 0, 1),
          W_LO(wra, 0, 2), W_HI(wra, 0, 2), NOP_, NOP_, NOP_, NOP_, NOP_, NOP_);
#pragma unroll 1
  for (int c = 1; c < 7; c += 2) {
    CHUNK_G(c, wra, hA, wrb, hB, 1, w2, c, c);
    const char* wn = c < 5 ? w2 : wo;
    CHUNK_G(c + 1, wrb, hB, wra, hA, 0, wn, (c < 5 ? c + 1 : 0), (c < 5 ? c + 1 : 5));
  }
  const int tok = rbk * 64 + mi * 16 + (lane & 15);
  const size_t xo = (size_t)tok * 1280 + (cbk * 80 + 4 * gq) * 2;
  LDS_AS f32x4* red = (LDS_AS f32x4*)(lds + KB_RED) + (mi * 5) * 64 + lane;
  u32x2 gv[5], xv[5];
  PLAIN_E(1, NOP_,
          _Pragma("unroll") for (int j = 0; j < 5; ++j) gv[j] = *(const u32x2*)(b2 + cbk * 80 + j * 16 + 4 * gq),
          _Pragma("unroll") for (int j = 0; j < 5; ++j) xv[j] = *(const u32x2*)(x + xo + j * 32),
          if (kh == 1) { red[0] = acc[0]; red[64] = acc[1]; red[128] = acc[2]; } else { red[192] = acc[3]; red[256] = acc[4]; });
  PLAIN_O(2, NOP_, NOP_, NOP_, NOP_);
  PLAIN_E(3, NOP_, NOP_, NOP_, NOP_);
  PLAIN_O(4, asm volatile("s_waitcnt vmcnt(0)" : "+v"(hA[1]), "+v"(hA[2]), "+v"(hA[3]) :: "memory");
          _Pragma("unroll") for (int j = 0; j < 5; ++j) { PIN(gv[j]); PIN(xv[j]); } W_M0(1, 0), W_P(wo, 0, 1), W_P(wo, 1, 1), W_P(wo, 2, 1));
  AT_STEP(sA, sB, A, B, 5, LBAR(0), NOP_, W_P(wo, 3, 1), W_M0(1, 1), W_P(wo, 4, 1), SET_M0(lds0 + (unsigned)(wid * 2560)),
          asm volatile("global_load_lds_dwordx4 %0, %3\n\tglobal_load_lds_dwordx4 %1, %3 offset:1024\n\t"
                       "s_mov_b32 exec_hi, 0\n\tglobal_load_lds_dwordx4 %2, %3 offset:2048\n\ts_mov_b32 exec_hi, -1"
                       :: "v"(vo[0]), "v"(vo[1]), "v"(vo[2]), "s"(wo + cbk * 160 + (size_t)(512 + wid * 16) * 1280) : "memory"),
          NOP_, NOP_, NOP_, NOP_, NOP_, NOP_, NOP_);
#define G_FIN(j)                                                                                          \
  do {                                                                                                    \
    const f32x4 tt = acc[j] + og[j] + f32x4{bf_lo(gv[j][0]), bf_hi(gv[j][0]), bf_lo(gv[j][1]), bf_hi(gv[j][1])}; \
    float t[4];                                                                                           \
    _Pragma("unroll") for (int e = 0; e < 4; ++e)                                                         \
      t[e] = tt[e] * __builtin_amdgcn_rcpf(1.0f + __builtin_amdgcn_exp2f(tt[e] * -1.4426950408889634f));  \
    gv[j] = u32x2{cvtpk(t[0], t[1]), cvtpk(t[2], t[3])};                                                  \
    PIN(gv[j]);                                                                                           \
  } while (0)
  {
    f32x4 og[5];
    if (kh == 0) { og[0] = red[0]; og[1] = red[64]; og[2] = red[128]; G_FIN(0); G_FIN(1); G_FIN(2); }
    else { og[3] = red[192]; og[4] = red[256]; G_FIN(3); G_FIN(4); }
  }
#undef G_FIN
  SBAR();
  PLAIN_O(6, NOP_, NOP_, NOP_, NOP_);
  PLAIN_E(7, NOP_, NOP_, NOP_, NOP_);
  PLAIN_O(7, NOP_, NOP_, NOP_, NOP_);
  o = MFMA32(v1B, __builtin_bit_cast(bf16x8, p1), o);

  STAMP(1, 3);
  STAMPC(1, 15);
  STAMP2(2, 8);
  float l = __uint_as_float(__builtin_amdgcn_permlane32_swap(__float_as_uint(o[8]), __float_as_uint(o[8]), false, false)[1]);
  float big = fmaxf(fmaxf(__builtin_fabsf(o[0]), __builtin_fabsf(o[1])), l);
#pragma unroll
  for (int i = 2; i < 12; i += 2) big = fmaxf(fmaxf(big, __builtin_fabsf(o[i])), __builtin_fabsf(o[i + 1]));
  bool unsafe = !(big < 1.0e38f) || !(l > 0.25f);
#pragma unroll
  for (int i = 0; i < 12; ++i) unsafe |= o[i] != o[i];
  if (__builtin_expect(__any(unsafe), 0)) {
    if (h == 1) { qbv[0] = 0u; }
    const bf16x4 qz = __builtin_bit_cast(bf16x4, qbv);
    float m = -3.0e38f;
#pragma unroll
    for (int i = 0; i < 16; ++i) o[i] = 0.f;
    const unsigned onev = r == 20 ? 0x3f803f80u : 0u;
#pragma unroll 1
    for (int t = 0; t < 64; ++t) {
      const bf16x8 ka = *(const bf16x8*)(Kh + t * 1280 + lane * 16);
      u32x2 kb = *(const u32x2*)(Kh + t * 1280 + 1024 + r * 8);
      u32x4 v0 = {onev, onev, onev, onev}, v1 = v0;
      if (r < 20) {
        v0 = *(const u32x4*)(Vh + t * 1280 + vv);
        v1 = *(const u32x4*)(Vh + t * 1280 + 640 + vv);
      }
      f32x16 s = MFMA32(ka, qa, zero);
      s = MFMA32K8(__builtin_bit_cast(bf16x4, kb), qz, s);
      float tm = s[0];
#pragma unroll
      for (int i = 1; i < 16; ++i) tm = fmaxf(tm, s[i]);
      tm = fmaxf(tm, xhalf(tm));
      const float mn = fmaxf(m, tm);
      const float al = __builtin_amdgcn_exp2f(m - mn);
      m = mn;
      u32x4 q0, q1;
#pragma unroll
      for (int i = 0; i < 16; ++i) { o[i] *= al; s[i] = __builtin_amdgcn_exp2f(s[i] - mn); }
#pragma unroll
      for (int e = 0; e < 4; ++e) { q0[e] = cvtpk(s[2 * e], s[2 * e + 1]); q1[e] = cvtpk(s[8 + 2 * e], s[9 + 2 * e]); }
      o = MFMA32(__builtin_bit_cast(bf16x8, v0), __builtin_bit_cast(bf16x8, q0), o);
      o = MFMA32(__builtin_bit_cast(bf16x8, v1), __builtin_bit_cast(bf16x8, q1), o);
    }
    l = __shfl(o[8], 32 + r);
  }
  {
    const float inv = __builtin_amdgcn_rcpf(l);
    const char* ob = ws + WS_O + (size_t)(qb * 16 + wid * 2) * (20 * 1024);
#pragma unroll
    for (int g = 0; g < 3; ++g)
      if (g < 2 || h == 0)
        st8s(ob, oo[g], u32x2{cvtpk(o[4 * g] * inv, o[4 * g + 1] * inv), cvtpk(o[4 * g + 2] * inv, o[4 * g + 3] * inv)});
  }
  publish(ws, qb);
  STAMP(1, 4);
  bf16x8 wfa[5][5];
#pragma unroll
  for (int c = 0; c < 5; ++c)
#pragma unroll
    for (int t = 0; t < 5; ++t) {
      wfa[c][t] = cat8(tr_read(LP(wra) + c * 10240 + t * 32), tr_read(LP(wra) + c * 10240 + t * 32 + 1280));
    }
  SBAR();
#pragma unroll
  for (int j = 0; j < 5; ++j) acc[j] = f32x4{0.f, 0.f, 0.f, 0.f};
  STAMP(1, 6);
  await(ws, rbk >> 2, 32u, wid);
  STAMP(1, 7);
  {
    const srd_t ro = make_srd(ws + WS_O, (unsigned)(WS_G - WS_O));
    const unsigned oa = (unsigned)(((rbk * 4 + mi) * 20 + kh) * 1024);
    u32x4 af[10];
#pragma unroll
    for (int c = 0; c < 10; ++c) af[c] = bload16_dev(ro, vlin, oa + c * 2048u);
    SBAR();
    bf16x8 wfb[5][5];
#pragma unroll
    for (int c = 5; c < 10; ++c) {
      const unsigned wa = c < 8 ? wrb + (unsigned)((c - 4) * 10240) : wra - (unsigned)KB_WR + (unsigned)((c - 8) * 10240);
      if (c < 8) {
#pragma unroll
        for (int t = 0; t < 5; ++t) wfb[c - 5][t] = cat8(tr_read(LP(wa) + t * 32), tr_read(LP(wa) + t * 32 + 1280));
      }
    }
    SBAR();
#pragma unroll
    for (int c = 0; c < 5; ++c)
#pragma unroll
      for (int t = 0; t < 5; ++t) acc[t] = MFMA16(wfa[c][t], __builtin_bit_cast(bf16x8, af[c]), acc[t]);
#pragma unroll
    for (int c = 8; c < 10; ++c)
#pragma unroll
      for (int t = 0; t < 5; ++t)
        wfb[c - 5][t] = cat8(tr_read(LP(wra - (unsigned)KB_WR) + (c - 8) * 10240 + t * 32), tr_read(LP(wra - (unsigned)KB_WR) + (c - 8) * 10240 + t * 32 + 1280));
#pragma unroll
    for (int c = 5; c < 10; ++c)
#pragma unroll
      for (int t = 0; t < 5; ++t) acc[t] = MFMA16(wfb[c - 5][t], __builtin_bit_cast(bf16x8, af[c]), acc[t]);
  }
  if (kh == 1) { red[0] = acc[0]; red[64] = acc[1]; red[128] = acc[2]; } else { red[192] = acc[3]; red[256] = acc[4]; }
  LBAR(0);
#define O_FIN(j)                                                                                          \
  do {                                                                                                    \
    const f32x4 sv = acc[j] + red[(j) * 64];                                                              \
    const float r0 = bf_lo(xv[j][0]) + sv[0] * bf_lo(gv[j][0]), r1 = bf_hi(xv[j][0]) + sv[1] * bf_hi(gv[j][0]); \
    const float r2 = bf_lo(xv[j][1]) + sv[2] * bf_lo(gv[j][1]), r3 = bf_hi(xv[j][1]) + sv[3] * bf_hi(gv[j][1]); \
    *(u32x2*)(out + xo + (j) * 32) = u32x2{cvtpk(r0, r1), cvtpk(r2, r3)};                                 \
  } while (0)
  if (kh == 0) { O_FIN(0); O_FIN(1); O_FIN(2); } else { O_FIN(3); O_FIN(4); }
#undef O_FIN
  STAMP(1, 8);
}

extern "C" void kernel_launch(void* const* d_in, const int* in_sizes, int n_in, void* d_out, int out_size, void* d_ws,
                              size_t ws_size, hipStream_t stream) {
  (void)in_sizes; (void)n_in; (void)out_size; (void)ws_size;
  const char* x = (const char*)d_in[0];
  const char* wqkv = (const char*)d_in[1];
  const char* wo = (const char*)d_in[2];
  const char* w1 = (const char*)d_in[4];
  const unsigned short* b1 = (const unsigned short*)d_in[5];
  const char* w2 = (const char*)d_in[6];
  const unsigned short* b2 = (const unsigned short*)d_in[7];
  char* ws = (char*)d_ws;
  (void)hipFuncSetAttribute((const void*)ka_proj, hipFuncAttributeMaxDynamicSharedMemorySize, KA_LDS);
  (void)hipFuncSetAttribute((const void*)kb_main, hipFuncAttributeMaxDynamicSharedMemorySize, KB_LDS);
  ka_proj<<<256, 512, KA_LDS, stream>>>(x, wqkv, w1, b1, w2, wo, ws);
  kb_main<<<256, 512, KB_LDS, stream>>>(x, w2, b2, wo, ws, (char*)d_out);
}
```

```cpp
#include <hip/hip_runtime.h>
#include <hip/hip_bf16.h>
#include <stdint.h>

typedef __attribute__((ext_vector_type(8))) short bf16x8;
typedef __attribute__((ext_vector_type(4))) short bf16x4;
typedef __attribute__((ext_vector_type(16))) float f32x16;
typedef __attribute__((ext_vector_type(4))) float f32x4;
typedef __attribute__((ext_vector_type(2))) float f32x2;
typedef __attribute__((ext_vector_type(4))) unsigned u32x4;
typedef __attribute__((ext_vector_type(2))) unsigned u32x2;
typedef __attribute__((ext_vector_type(2))) __bf16 bf2;
typedef __attribute__((address_space(3))) char* lds_ptr;
#define LDS_AS __attribute__((address_space(3)))
#define DI __device__ __forceinline__

#define MFMA32(a, b, c) __builtin_amdgcn_mfma_f32_32x32x16_bf16((a), (b), (c), 0, 0, 0)
#define MFMA32K8(a, b, c) __builtin_amdgcn_mfma_f32_32x32x8bf16_1k((a), (b), (c), 0, 0, 0)
#define MFMA16(a, b, c) __builtin_amdgcn_mfma_f32_16x16x32_bf16((a), (b), (c), 0, 0, 0)
#define SBAR() __builtin_amdgcn_sched_barrier(0)
#define PIN(x) asm volatile("" : "+v"(x))
#define WAIT_BAR(N) asm volatile("s_waitcnt vmcnt(" #N ") lgkmcnt(0)\n\ts_barrier" ::: "memory")

constexpr int S_ = 2048, D_ = 640, NH = 32, F_ = 1536;
constexpr float LN_EPS = 1e-5f;
constexpr float QSCALE = 0.22360679774997896f * 1.4426950408889634f;

constexpr size_t WS_Q = 0;
constexpr size_t WS_K = WS_Q + (size_t)NH * 64 * 1280;
constexpr size_t WS_V = WS_K + (size_t)NH * 64 * 1280;
constexpr size_t WS_H = WS_V + (size_t)NH * 64 * 1280;
constexpr size_t WS_O = WS_H + (size_t)128 * 48 * 1024;
constexpr size_t WS_G = WS_O + (size_t)128 * 20 * 1024;
constexpr size_t WS_CNT = WS_G;
constexpr size_t WS_END = WS_CNT + 32 * 256;

#define STAMP(kern, i) do { } while (0)
#define STAMPC(kern, i) do { } while (0)
#define STAMP2(kern, i) do { } while (0)

#define STPOL 2
DI void st16(void* p, u32x4 v) {
  asm volatile("global_store_dwordx4 %0, %1, off sc1\n\ts_nop 1" :: "v"(p), "v"(v) : "memory");
}
DI void st8(void* p, u32x2 v) {
  asm volatile("global_store_dwordx2 %0, %1, off sc1" :: "v"(p), "v"(v) : "memory");
}
DI void st16s(const void* sb, unsigned off, u32x4 v) { asm volatile("global_store_dwordx4 %0, %1, %2 sc1\n\ts_nop 1" :: "v"(off), "v"(v), "s"(sb) : "memory"); }
DI void st8s(const void* sb, unsigned off, u32x2 v) { asm volatile("global_store_dwordx2 %0, %1, %2 sc1" :: "v"(off), "v"(v), "s"(sb) : "memory"); }
DI unsigned cvtpk(float lo, float hi) {
  f32x2 f = {lo, hi};
  return __builtin_bit_cast(unsigned, __builtin_convertvector(f, bf2));
}
DI float bf_lo(unsigned u) { return __uint_as_float(u << 16); }
DI float bf_hi(unsigned u) { return __uint_as_float(u & 0xffff0000u); }
DI float dot2(unsigned a, unsigned b, float c) {
  return __builtin_amdgcn_fdot2_f32_bf16(__builtin_bit_cast(bf2, a), __builtin_bit_cast(bf2, b), c, false);
}
DI float xhalf(float v) {
  return __shfl_xor(v, 32);
}
DI void glds16(unsigned voff, const void* sbase, unsigned ldsdst) {
  unsigned keep;
  asm volatile("s_mov_b32 %0, m0\n\ts_mov_b32 m0, %3\n\ts_nop 0\n\tglobal_load_lds_dwordx4 %1, %2\n\ts_mov_b32 m0, %0"
               : "=&s"(keep) : "v"(voff), "s"(sbase), "s"(ldsdst) : "memory");
}
DI void glds16_masked(unsigned voff, const void* sbase, unsigned ldsdst, unsigned mlo, unsigned mhi) {
  unsigned keep;
  asm volatile("s_mov_b32 %0, m0\n\ts_mov_b32 m0, %3\n\ts_mov_b32 exec_lo, %4\n\ts_mov_b32 exec_hi, %5\n\t"
               "global_load_lds_dwordx4 %1, %2\n\ts_mov_b64 exec, -1\n\ts_mov_b32 m0, %0"
               : "=&s"(keep) : "v"(voff), "s"(sbase), "s"(ldsdst), "s"(mlo), "s"(mhi) : "memory");
}
typedef short v4i16_t __attribute__((ext_vector_type(4)));
DI bf16x4 tr_read(lds_ptr p) {
  return __builtin_bit_cast(bf16x4, __builtin_amdgcn_ds_read_tr16_b64_v4i16((LDS_AS v4i16_t*)p));
}
DI bf16x8 cat8(bf16x4 a, bf16x4 b) { return __builtin_shufflevector(a, b, 0, 1, 2, 3, 4, 5, 6, 7); }

constexpr int KA_BUF = 45056;
constexpr int KA_STAT = 3 * KA_BUF;
constexpr int KA_SCS = KA_STAT + 2048, KA_SB1 = KA_SCS + 3584, KA_SMR = KA_SB1 + 384;
constexpr int KA_LDS = KA_SMR + 2048;
#define KABL 0
#define SENT_DELAY 8
#define LBAR(N) asm volatile("s_waitcnt lgkmcnt(" #N ")\n\ts_barrier" ::: "memory")
#define LP(a) ((lds_ptr)(uintptr_t)(a))
#define SET_M0(v) asm volatile("s_mov_b32 m0, %0\n\ts_nop 0" :: "s"(v) : "memory")
#define NOP_ do { } while (0)
#define MFMA4(a, b, c) __builtin_amdgcn_mfma_f32_4x4x4bf16_1k((a), (b), (c), 0, 0, 0)

typedef __amdgpu_buffer_rsrc_t srd_t;
DI srd_t make_srd(const void* p, unsigned bytes) { return __builtin_amdgcn_make_buffer_rsrc((void*)p, (short)0, (int)bytes, 0x00020000); }
DI float x32_add(float v) {
  const auto rr = __builtin_amdgcn_permlane32_swap(__float_as_uint(v), __float_as_uint(v), false, false);
  return __uint_as_float(rr[0]) + __uint_as_float(rr[1]);
}
DI float nanmax(float a, float b) { return (b > a || b != b) ? b : a; }
template <int CTRL> DI float dpp_get(float v) { return __uint_as_float(__builtin_amdgcn_update_dpp(0u, __float_as_uint(v), CTRL, 0xf, 0xf, true)); }
DI float wave_nanmax(float v) {
  v = nanmax(v, dpp_get<0xB1>(v));
  v = nanmax(v, dpp_get<0x4E>(v));
  v = nanmax(v, dpp_get<0x141>(v));
  v = nanmax(v, dpp_get<0x140>(v));
  const auto a = __builtin_amdgcn_permlane16_swap(__float_as_uint(v), __float_as_uint(v), false, false);
  v = nanmax(__uint_as_float(a[0]), __uint_as_float(a[1]));
  const auto b = __builtin_amdgcn_permlane32_swap(__float_as_uint(v), __float_as_uint(v), false, false);
  return nanmax(__uint_as_float(b[0]), __uint_as_float(b[1]));
}
DI bf16x4 lo4(bf16x8 v) { return __builtin_shufflevector(v, v, 0, 1, 2, 3); }
DI bf16x4 hi4(bf16x8 v) { return __builtin_shufflevector(v, v, 4, 5, 6, 7); }

template <int TYPE, int nh>
DI void ka_body(lds_ptr lds, const char* __restrict__ x, const char* __restrict__ wqkv, const char* __restrict__ wh1,
                const unsigned short* __restrict__ b1, const char* __restrict__ w2, const char* __restrict__ wo,
                const int n0, const int f0, const int rb, const int cb, char* __restrict__ ws) {
  constexpr int NHT = nh ? 1 : 2;
  const int tid = threadIdx.x, lane = tid & 63;
  const int w = __builtin_amdgcn_readfirstlane(tid >> 6);
  const int mi = w & 3;
  const int r = lane & 31, h = lane >> 5;
  const unsigned lds0 = (unsigned)(uintptr_t)lds;
  STAMP(0, 0);
  const unsigned vx0 = (unsigned)((lane >> 3) * 1280 + (((lane & 7) ^ (lane >> 4)) << 4));
  const unsigned vx1 = (unsigned)((8 + (lane >> 3)) * 1280 + (((lane & 7) ^ (4 + (lane >> 4))) << 4) - 1024);
  const unsigned vw = (unsigned)((lane >> 3) * 3840 + ((((((lane >> 2) ^ (lane >> 4)) & 1) << 2) | (lane & 3)) << 4));
  const int p0 = w * 96 + lane, p1 = w * 96 + 64 + r;
  const int r0 = (p0 * 5462) >> 16, r1 = (p1 * 5462) >> 16;
  const unsigned vh0 = (unsigned)(r0 * 3072 + (p0 - r0 * 12) * 16), vh1 = (unsigned)(r1 * 3072 + (p1 - r1 * 12) * 16 - 1024);
  const char* xs = x + (size_t)(rb * 128 + 16 * w) * 1280;
  const char* wsrc = wqkv + ((size_t)(8 * w) * 1920 + n0) * 2;
  const char* hsrc = wh1 + f0 * 2;
  const unsigned mx = lds0 + (unsigned)w * 2048u, mw = lds0 + 16384u + (unsigned)w * 1024u, mh = lds0 + 32768u + (unsigned)w * 1536u;
#define KA_ROT 0
  const int rot = KA_ROT ? (rb * 2 + cb * 5) % 9 : 0;
#define KC(c) ((c) == 0 ? 0 : ((c) + rot >= 10 ? (c) + rot - 9 : (c) + rot))
#define KA_DX(c, buf) do { SET_M0(mx + (unsigned)(buf)); asm volatile("global_load_lds_dwordx4 %0, %2\n\tglobal_load_lds_dwordx4 %1, %2 offset:1024" :: "v"(vx0), "v"(vx1), "s"(xs + KC(c) * 128) : "memory"); } while (0)
#define KA_DW(c, buf, hf) do { SET_M0(mw + (unsigned)(buf) + (hf) * 8192u); asm volatile("global_load_lds_dwordx4 %0, %1" :: "v"(vw), "s"(wsrc + (size_t)KC(c) * (64 * 3840) + (hf) * 128) : "memory"); } while (0)
#define KA_DH(c, buf) do { SET_M0(mh + (unsigned)(buf)); asm volatile("global_load_lds_dwordx4 %0, %2\n\ts_mov_b32 exec_hi, 0\n\tglobal_load_lds_dwordx4 %1, %2 offset:1024\n\ts_mov_b32 exec_hi, -1" :: "v"(vh0), "v"(vh1), "s"(hsrc + (size_t)KC(c) * (64 * 3072)) : "memory"); } while (0)
#define KA_ISSUE(c, buf) do { KA_DX(c, buf); KA_DW(c, buf, 0); KA_DW(c, buf, 1); KA_DH(c, buf); } while (0)
  unsigned braw = 0, warm = 0;
  if (nh == 0 && tid < 96) braw = b1[f0 + tid];
  SBAR();
  KA_ISSUE(0, 0);
  KA_ISSUE(1, KA_BUF);

  unsigned xa[4], wa0[4], ha[4];
  {
    const unsigned wb = lds0 + (unsigned)(16384 + nh * 8192 + (8 * h + ((lane & 15) >> 2)) * 128 + 64 * ((lane >> 3) & 1) +
                                          (16 * ((lane >> 4) & 1) + 4 * (lane & 3)) * 2);
    const unsigned hb = lds0 + (unsigned)(32768 + (8 * h + ((lane & 15) >> 2)) * 192 + nh * 128 + (16 * ((lane >> 4) & 1) + 4 * (lane & 3)) * 2);
#pragma unroll
    for (int i = 0; i < 4; ++i) {
      const int ks = i ^ mi;
      xa[i] = lds0 + (unsigned)((mi * 32 + r) * 128 + (((2 * ks + h) ^ ((r >> 1) & 7)) << 4));
      wa0[i] = wb + (unsigned)(ks * 2048);
      ha[i] = hb + (unsigned)(ks * 3072);
    }
  }
  const int tq = rb * 4 + mi;
  const bool isq = n0 < 640;
  unsigned so[TYPE == 1 ? 2 : 8];
  if (TYPE == 1) {
#pragma unroll
    for (int j = 0; j < 2; ++j) {
      const int nn = n0 - 1280 + nh * 64 + j * 32 + r;
      const int head = (nn * 3277) >> 16;
      so[j] = (unsigned)(head * (64 * 1280) + (h * 20 + nn - head * 20) * 16);
      PIN(so[j]);
    }
  } else {
#pragma unroll
    for (int j = 0; j < 8; ++j) {
      const int G = (((isq ? n0 : n0 - 640) + nh * 64) >> 2) + h + 2 * j;
      const int head = (G * 205) >> 10;
      const int dg = G - head * 5;
      so[j] = (unsigned)(head * (64 * 1280) + (dg < 4 ? (dg >> 1) * 512 + r * 16 + (dg & 1) * 8 : 1024 + r * 8));
      PIN(so[j]);
    }
  }
  f32x16 acc[2], ach[NHT];
  f32x4 cq[2], chh[NHT], rsum = {0.f, 0.f, 0.f, 0.f}, rsq = rsum;
#pragma unroll
  for (int i = 0; i < 16; ++i) {
    acc[0][i] = 0.f; acc[1][i] = 0.f;
#pragma unroll
    for (int j = 0; j < NHT; ++j) ach[j][i] = 0.f;
  }
  cq[0] = rsum; cq[1] = rsum;
#pragma unroll
  for (int j = 0; j < NHT; ++j) chh[j] = rsum;
  bf16x8 sel;
  {
    const short sv = (lane & 15) == ((lane >> 4) & 1) ? (short)0x3f80 : (short)0;
    sel = bf16x8{sv, sv, sv, sv, sv, sv, sv, sv};
    PIN(sel);
  }
  bf16x8 xf[3], w0[3], w1[3], wh[3][NHT];
  unsigned wa1[4];
#pragma unroll
  for (int i = 0; i < 4; ++i) wa1[i] = wa0[i] ^ 64u;
#define KA_READ(B, i)                                                                                           \
  do {                                                                                                          \
    constexpr int s_ = ((B) + (i)) % 3;                                                                         \
    if ((KABL & 8) && started) break;                                                                           \
    xf[s_] = *(const LDS_AS bf16x8*)(LP(xa[i]) + BUFOFF(B));                                                    \
    if ((KABL & 16) && started) break;                                                                          \
    w0[s_] = cat8(tr_read(LP(wa0[i]) + BUFOFF(B)), tr_read(LP(wa0[i]) + BUFOFF(B) + 512));                      \
    w1[s_] = cat8(tr_read(LP(wa1[i]) + BUFOFF(B)), tr_read(LP(wa1[i]) + BUFOFF(B) + 512));                      \
    _Pragma("unroll") for (int j_ = 0; j_ < NHT; ++j_)                                                          \
      wh[s_][j_] = cat8(tr_read(LP(ha[i]) + BUFOFF(B) + j_ * 64), tr_read(LP(ha[i]) + BUFOFF(B) + j_ * 64 + 768)); \
  } while (0)
#define SGB(m, n) __builtin_amdgcn_sched_group_barrier((m), (n), 0)
#define KA_STEP(B, i, RD) KA_STEPX(B, i, RD, true)
#define KA_STEPX(B, i, RD, ROWS)                                                                                \
  do {                                                                                                          \
    constexpr int b_ = ((B) + (i)) % 3;                                                                         \
    if constexpr (NHT == 2) asm volatile("" : "+v"(xf[b_]), "+v"(w0[b_]), "+v"(w1[b_]), "+v"(wh[b_][0]), "+v"(wh[b_][NHT - 1])); \
    else asm volatile("" : "+v"(xf[b_]), "+v"(w0[b_]), "+v"(w1[b_]), "+v"(wh[b_][0]));                          \
    SBAR();                                                                                                     \
    RD;                                                                                                         \
    if (TYPE == 1) { acc[0] = MFMA32(xf[b_], w0[b_], acc[0]); acc[1] = MFMA32(xf[b_], w1[b_], acc[1]); }        \
    else { acc[0] = MFMA32(w0[b_], xf[b_], acc[0]); acc[1] = MFMA32(w1[b_], xf[b_], acc[1]); }                  \
    _Pragma("unroll") for (int j_ = 0; j_ < NHT; ++j_) ach[j_] = MFMA32(wh[b_][j_], xf[b_], ach[j_]);           \
    SGB(0x8, 1); SGB(0x100, 3); SGB(0x8, 1); SGB(0x100, 2); SGB(0x8, 1); SGB(0x100, 2);                         \
    if (NHT == 2) { SGB(0x8, 1); SGB(0x100, 2); }                                                               \
    if constexpr (NHT == 2) asm volatile("" : "+v"(acc[0]), "+v"(acc[1]), "+v"(ach[0]), "+v"(ach[NHT - 1]));    \
    else asm volatile("" : "+v"(acc[0]), "+v"(acc[1]), "+v"(ach[0]));                                           \
    SBAR();                                                                                                     \
    if ((i) == 0 && !(KABL & 1)) {                                                                              \
      cq[0] = MFMA16(w0[b_], sel, cq[0]); cq[1] = MFMA16(w1[b_], sel, cq[1]);                                   \
      _Pragma("unroll") for (int j_ = 0; j_ < NHT; ++j_) chh[j_] = MFMA16(wh[b_][j_], sel, chh[j_]);            \
      if constexpr (NHT == 2) asm volatile("" : "+v"(cq[0]), "+v"(cq[1]), "+v"(chh[0]), "+v"(chh[NHT - 1]));    \
      else asm volatile("" : "+v"(cq[0]), "+v"(cq[1]), "+v"(chh[0]));                                           \
    }                                                                                                           \
    if ((ROWS) && (nh ? (i) < 2 : (i) >= 2) && !(KABL & 2)) {                                                   \
      rsq = MFMA4(lo4(xf[b_]), lo4(xf[b_]), rsq); rsum = MFMA16(xf[b_], sel, rsum);                             \
      rsq2 = MFMA4(hi4(xf[b_]), hi4(xf[b_]), rsq2);                                                             \
      asm volatile("" : "+v"(rsum), "+v"(rsq), "+v"(rsq2));                                                     \
    }                                                                                                           \
    SBAR();                                                                                                     \
  } while (0)
#define BUFOFF(B) ((B) * KA_BUF)
#define KA_CHUNK(c, B, BN, WAITN, PREV, MORE)                                                                   \
  do {                                                                                                          \
    KA_STEP(B, 0, KA_READ(B, 2));                                                                               \
    if ((PREV) && !(KABL & 4)) { KA_DH((c) + 2, BUFOFF(((B) + 2) % 3)); SBAR(); }                               \
    KA_STEP(B, 1, KA_READ(B, 3));                                                                               \
    if (!(KABL & 32)) WAIT_BAR(WAITN);                                                                          \
    KA_STEP(B, 2, KA_READ(BN, 0));                                                                              \
    if ((MORE) && !(KABL & 4)) { KA_DX((c) + 3, BUFOFF(B)); SBAR(); }                                           \
    KA_STEP(B, 3, KA_READ(BN, 1));                                                                              \
    if ((MORE) && !(KABL & 4)) { KA_DW((c) + 3, BUFOFF(B), 0); KA_DW((c) + 3, BUFOFF(B), 1); SBAR(); }          \
  } while (0)
  f32x4 rsq2 = rsum;
  WAIT_BAR(6);
  STAMP(0, 1);
  STAMPC(0, 14);
  bool started = false;
  KA_READ(0, 0);
  KA_READ(0, 1);
  if (KABL & 24) { KA_READ(0, 2); started = true; }
  KA_STEP(0, 0, KA_READ(0, 2));
  KA_DX(2, BUFOFF(2)); KA_DW(2, BUFOFF(2), 0); KA_DW(2, BUFOFF(2), 1); SBAR();
  KA_STEP(0, 1, KA_READ(0, 3));
  KA_DH(2, BUFOFF(2)); SBAR();
  WAIT_BAR(6);
  KA_STEP(0, 2, KA_READ(1, 0));
  KA_DX(3, BUFOFF(0)); SBAR();
  KA_STEP(0, 3, KA_READ(1, 1));
  KA_DW(3, BUFOFF(0), 0); KA_DW(3, BUFOFF(0), 1); SBAR();
#pragma unroll 1
  for (int c = 1; c < 7; c += 3) {
    KA_CHUNK(c, 1, 2, 6, true, true);
    KA_CHUNK(c + 1, 2, 0, 6, true, true);
    KA_CHUNK(c + 2, 0, 1, 6, true, true);
  }
  KA_CHUNK(7, 1, 2, 6, true, false);
  KA_CHUNK(8, 2, 0, 0, false, false);
  KA_STEP(0, 0, KA_READ(0, 2));
  KA_STEP(0, 1, KA_READ(0, 3));
  if (nh == 0) {
    asm volatile("" : "+v"(xf[2]), "+v"(xf[0]));
    rsq = MFMA4(lo4(xf[2]), lo4(xf[2]), rsq); rsum = MFMA16(xf[2], sel, rsum); rsq2 = MFMA4(hi4(xf[2]), hi4(xf[2]), rsq2);
    rsum = MFMA16(xf[0], sel, rsum); rsq = MFMA4(lo4(xf[0]), lo4(xf[0]), rsq); rsq2 = MFMA4(hi4(xf[0]), hi4(xf[0]), rsq2);
  }
  asm volatile("s_nop 7" : "+v"(rsum), "+v"(rsq), "+v"(rsq2));
  rsq += rsq2;
  STAMP(0, 3);
  STAMPC(0, 15);
  LDS_AS float* st_r = (LDS_AS float*)(lds + KA_STAT);
  LDS_AS float* st_cs = (LDS_AS float*)(lds + KA_SCS);
  LDS_AS float* st_b = (LDS_AS float*)(lds + KA_SB1);
  {
    const int d = lane & 3;
    const float sq = x32_add(d == 0 ? rsq[0] : d == 1 ? rsq[1] : d == 2 ? rsq[2] : rsq[3]);
    if (h == 0) st_r[256 + nh * 128 + mi * 32 + r] = sq;
    if ((lane & 15) < 2) {
      const int q4 = (lane & 1) * 16 + (lane >> 4) * 4;
      *(LDS_AS f32x4*)(st_r + nh * 128 + mi * 32 + q4) = rsum;
      *(LDS_AS f32x4*)(st_cs + mi * 224 + nh * 64 + q4) = cq[0];
      *(LDS_AS f32x4*)(st_cs + mi * 224 + nh * 64 + 32 + q4) = cq[1];
#pragma unroll
      for (int j = 0; j < NHT; ++j) *(LDS_AS f32x4*)(st_cs + mi * 224 + 128 + nh * 64 + j * 32 + q4) = chh[j];
    }
    if (nh == 0 && tid < 96) st_b[tid] = __uint_as_float(braw << 16);
  }
  SBAR();
  KA_STEPX(0, 2, NOP_, false);
  LBAR(0);
  float cpart = 0.f;
  if (tid < 224) cpart = (st_cs[tid] + st_cs[224 + tid]) + (st_cs[448 + tid] + st_cs[672 + tid]);
  const float sx_ = st_r[mi * 32 + r] + st_r[128 + mi * 32 + r], sq_ = st_r[256 + mi * 32 + r] + st_r[384 + mi * 32 + r];
  SBAR();
  KA_STEPX(0, 3, NOP_, false);
#undef KA_READ
#undef KA_STEP
#undef KA_STEPX
#undef KA_CHUNK
#undef KA_ISSUE
  if (tid < 224) st_cs[tid] = cpart;
  {
    const char* ob = ws + WS_O + (size_t)blockIdx.x * 10240;
    const u32x4 ff = {0xffffffffu, 0xffffffffu, 0xffffffffu, 0xffffffffu};
    st16s(ob, (unsigned)tid * 16u, ff);
    if (w < 2) st16s(ob + 8192, (unsigned)tid * 16u, ff);
  }
  if (nh == 1) {
    const int l0_ = blockIdx.x * 85 + (tid - 256 < 85 ? tid - 256 : 0);
    const char* p = l0_ < 15360 ? w2 + (size_t)l0_ * 128 : wo + (size_t)(l0_ - 15360) * 128;
    if (w < 6) asm volatile("global_load_dword %0, %1, off" : "=v"(warm) : "v"(p) : "memory");
  }
  const float mu = sx_ * (1.0f / D_);
  const float var = fmaxf(sq_ * (1.0f / D_) - mu * mu, 0.f);
  const float rs = __builtin_amdgcn_rsqf(var + LN_EPS);
  LDS_AS f32x2* st_w = (LDS_AS f32x2*)(lds + KA_SMR) + w * 32;
  if (TYPE == 1 && h == 0) st_w[r] = f32x2{mu * rs, rs};
  LBAR(0);
  STAMP(0, 5);
  if (cb < 15) {
    if (TYPE == 1) {
      char* dst = ws + WS_V + (size_t)tq * 1280;
      const float cs0 = st_cs[nh * 64 + r], cs1 = st_cs[nh * 64 + 32 + r];
#pragma unroll
      for (int s = 0; s < 2; ++s) {
        f32x2 mr[8];
#pragma unroll
        for (int e = 0; e < 8; ++e) mr[e] = st_w[16 * s + 8 * (e >> 2) + 4 * h + (e & 3)];
        u32x4 o0, o1;
#pragma unroll
        for (int e = 0; e < 4; ++e) {
          const int i = 8 * s + 2 * e;
          o0[e] = cvtpk(acc[0][i] * mr[2 * e][1] - mr[2 * e][0] * cs0, acc[0][i + 1] * mr[2 * e + 1][1] - mr[2 * e + 1][0] * cs0);
          o1[e] = cvtpk(acc[1][i] * mr[2 * e][1] - mr[2 * e][0] * cs1, acc[1][i + 1] * mr[2 * e + 1][1] - mr[2 * e + 1][0] * cs1);
        }
        st16s(dst + s * 640, so[0], o0);
        st16s(dst + s * 640, so[1], o1);
      }
    } else {
      const float sc = isq ? rs * QSCALE : rs, ms = mu * sc;
      char* base = ws + (isq ? WS_Q : WS_K) + (size_t)tq * 1280;
#pragma unroll
      for (int j = 0; j < 2; ++j)
#pragma unroll
        for (int g = 0; g < 4; ++g) {
          const f32x4 cs = *(const LDS_AS f32x4*)(st_cs + nh * 64 + j * 32 + 8 * g + 4 * h);
          const u32x2 o = {cvtpk(acc[j][4 * g] * sc - ms * cs[0], acc[j][4 * g + 1] * sc - ms * cs[1]),
                           cvtpk(acc[j][4 * g + 2] * sc - ms * cs[2], acc[j][4 * g + 3] * sc - ms * cs[3])};
          st8s(base, so[j * 4 + g], o);
        }
    }
  }
  {
    const float sc = rs * __builtin_amdgcn_rsqf(var * rs * rs + LN_EPS), ms = mu * sc;
    constexpr float GK1 = -2.0f * 1.4426950408889634f * 0.7978845608028654f, GK2 = GK1 * 0.044715f;
    const char* dst = ws + WS_H + ((size_t)(rb * 8 + mi * 2) * 48 + (f0 >> 5) + nh * 2) * 1024;
    const unsigned ho = (unsigned)((r >> 4) * (48 * 1024) + (h * 16 + (r & 15)) * 16);
    const f32x2 sc2 = {sc, sc}, ms2 = {ms, ms}, k1 = {GK1, GK1}, k2 = {GK2, GK2}, one2 = {1.0f, 1.0f};
#pragma unroll
    for (int j = 0; j < NHT; ++j)
#pragma unroll
      for (int u = 0; u < 2; ++u) {
        u32x4 o;
#pragma unroll
        for (int gg = 0; gg < 2; ++gg) {
          const int g = 2 * u + gg;
          const f32x4 cs = *(const LDS_AS f32x4*)(st_cs + 128 + nh * 64 + j * 32 + 8 * g + 4 * h);
          const f32x4 bb = *(const LDS_AS f32x4*)(st_b + nh * 64 + j * 32 + 8 * g + 4 * h);
#pragma unroll
          for (int e = 0; e < 2; ++e) {
            const f32x2 a2 = {ach[j][4 * g + 2 * e], ach[j][4 * g + 2 * e + 1]};
            const f32x2 c2 = {cs[2 * e], cs[2 * e + 1]}, b2_ = {bb[2 * e], bb[2 * e + 1]};
            const f32x2 t = a2 * sc2 + (b2_ - ms2 * c2);
            const f32x2 arg = t * ((t * t) * k2 + k1);
            const f32x2 den = f32x2{__builtin_amdgcn_exp2f(arg[0]), __builtin_amdgcn_exp2f(arg[1])} + one2;
            const f32x2 v = t * f32x2{__builtin_amdgcn_rcpf(den[0]), __builtin_amdgcn_rcpf(den[1])};
            o[2 * gg + e] = cvtpk(v[0], v[1]);
          }
        }
        st16s(dst + j * 1024 + u * 512, ho, o);
      }
  }
  STAMP(0, 4);
  asm volatile("s_waitcnt vmcnt(0)" : "+v"(warm) :: "memory");
}

__global__ __launch_bounds__(512, 2) void ka_proj(const char* __restrict__ x, const char* __restrict__ wqkv,
                                                  const char* __restrict__ w1, const unsigned short* __restrict__ b1,
                                                  const char* __restrict__ w2, const char* __restrict__ wo,
                                                  char* __restrict__ ws) {
  extern __shared__ __attribute__((aligned(16))) char smem[];
  lds_ptr lds = (lds_ptr)smem;
  const int bid = blockIdx.x, xg = bid & 7, idx = bid >> 3;
  const int rb = (xg & 1) * 8 + (idx & 7), cb = (xg >> 1) * 4 + (idx >> 3);
  const int n0 = (cb < 15 ? cb : 14) * 128, f0 = cb * 96;
  if (bid == 0 && threadIdx.x < 32) *(unsigned*)(ws + WS_CNT + threadIdx.x * 256) = 0u;
  const int nh = __builtin_amdgcn_readfirstlane(threadIdx.x >> 8);
  if (cb < 10) {
    if (nh) ka_body<0, 1>(lds, x, wqkv, w1, b1, w2, wo, n0, f0, rb, cb, ws);
    else ka_body<0, 0>(lds, x, wqkv, w1, b1, w2, wo, n0, f0, rb, cb, ws);
  } else {
    if (nh) ka_body<1, 1>(lds, x, wqkv, w1, b1, w2, wo, n0, f0, rb, cb, ws);
    else ka_body<1, 0>(lds, x, wqkv, w1, b1, w2, wo, n0, f0, rb, cb, ws);
  }
}

typedef __attribute__((address_space(1))) unsigned gu32;
DI void st8_dev(void* p, u32x2 v) { asm volatile("global_store_dwordx2 %0, %1, off sc1" :: "v"(p), "v"(v) : "memory"); }
DI u32x4 bload16_dev(srd_t r, unsigned voff, unsigned soff) { return __builtin_amdgcn_raw_buffer_load_b128(r, (int)voff, (int)soff, 16); }
DI void ld16_asm(bf16x8& d, unsigned voff, const void* sbase) {
  asm volatile("global_load_dwordx4 %0, %1, %2" : "=v"(d) : "v"(voff), "s"(sbase) : "memory");
}
DI void publish(char* ws, int cnt) {
  asm volatile("s_waitcnt vmcnt(0) lgkmcnt(0)\n\ts_barrier" ::: "memory");
  if (threadIdx.x == 0) __hip_atomic_fetch_add((gu32*)(ws + WS_CNT + cnt * 256), 1u, __ATOMIC_RELAXED, __HIP_MEMORY_SCOPE_AGENT);
}
DI void await(char* ws, int cnt, unsigned want, int wid) {
  if (wid == 0) {
    for (unsigned spins = 0; spins < (1u << 16); ++spins) {
      const unsigned v = __hip_atomic_load((gu32*)(ws + WS_CNT + cnt * 256), __ATOMIC_RELAXED, __HIP_MEMORY_SCOPE_AGENT);
      if ((unsigned)__builtin_amdgcn_readfirstlane(v) >= want) break;
      __builtin_amdgcn_s_sleep(1);
    }
  }
  asm volatile("s_waitcnt vmcnt(0)\n\ts_barrier" ::: "memory");
}

constexpr int AT_TILE = 3584;
constexpr int AT_SLOT = 28672;
constexpr int KB_WR = 2 * AT_SLOT;
constexpr int KB_GRP = 40960;
constexpr int KB_RED = KB_WR + 2 * KB_GRP;
constexpr int KB_LDS = KB_RED + 20480;

__global__ __launch_bounds__(512, 2) void kb_main(const char* __restrict__ x, const char* __restrict__ w2,
                                                  const unsigned short* __restrict__ b2, const char* __restrict__ wo,
                                                  char* __restrict__ ws, char* __restrict__ out) {
  extern __shared__ __attribute__((aligned(16))) char smem[];
  lds_ptr lds = (lds_ptr)smem;
  const int tid = threadIdx.x, lane = tid & 63;
  const int wid = __builtin_amdgcn_readfirstlane(tid >> 6);
  const int r = lane & 31, h = lane >> 5;
  const int xg = blockIdx.x & 7, idx = blockIdx.x >> 3;
  const int head = xg * 4 + (idx >> 3), qb = idx & 7;
  const int rbk = xg * 4 + (idx & 3), cbk = idx >> 2;
  const int mi = wid & 3, kh = wid >> 2, gq = lane >> 4;
  const unsigned lds0 = (unsigned)(uintptr_t)lds;
  const char* Kh = ws + WS_K + (size_t)head * (64 * 1280);
  const char* Vh = ws + WS_V + (size_t)head * (64 * 1280);
  const char* Qt = ws + WS_Q + (size_t)(head * 64 + qb * 8 + wid) * 1280;
  STAMP(1, 0);

  const unsigned vlin = (unsigned)lane * 16u;
  const unsigned vv = (unsigned)(h * 20 + r) * 16u;
  const unsigned vv2 = vv + (unsigned)(WS_V - WS_K) - 1280u - 1536u, vv3 = vv2 + 640u - 1024u;
  const unsigned long long mk16 = 0xffffull, mk20 = 0x000fffff000fffffull;
#define KV_M0(c) SET_M0(lds0 + (unsigned)(((c) & 1) * AT_SLOT + wid * AT_TILE))
#define KV_SRC(c) (Kh + (size_t)((c) * 8 + wid + 1) * 1280)
#define KV_P0(c) asm volatile("global_load_lds_dwordx4 %0, %1" :: "v"(vlin), "s"(KV_SRC(c)) : "memory")
#define KV_P1(c) asm volatile("s_mov_b64 exec, %2\n\tglobal_load_lds_dwordx4 %0, %1 offset:1024\n\ts_mov_b64 exec, -1" :: "v"(vlin), "s"(KV_SRC(c)), "s"(mk16) : "memory")
#define KV_P23(c) asm volatile("s_mov_b64 exec, %3\n\tglobal_load_lds_dwordx4 %0, %2 offset:1536\n\tglobal_load_lds_dwordx4 %1, %2 offset:2560\n\ts_mov_b64 exec, -1" :: "v"(vv2), "v"(vv3), "s"(KV_SRC(c)), "s"(mk20) : "memory")
  bf16x8 qa, ka0;
  u32x2 qbv, kb0;
  {
    const unsigned v8 = 1024u + (unsigned)r * 8u;
    asm volatile("global_load_dwordx4 %0, %4, %6\n\tglobal_load_dwordx2 %1, %5, %6\n\t"
                 "global_load_dwordx4 %2, %4, %7\n\tglobal_load_dwordx2 %3, %5, %7"
                 : "=&v"(qa), "=&v"(qbv), "=&v"(ka0), "=&v"(kb0) : "v"(vlin), "v"(v8), "s"(Qt), "s"(Kh) : "memory");
  }
  KV_M0(0); KV_P0(0); KV_P1(0); KV_P23(0);
#pragma unroll
  for (int sl = 0; sl < 2; ++sl) {
    lds_ptr tp = lds + sl * AT_SLOT + wid * AT_TILE;
    if (lane < 32) *(LDS_AS u32x2*)(tp + 1024 + 256 + lane * 8) = u32x2{0x00003f80u, 0u};
    if (lane < 48) {
      const int blk4 = (lane * 43) >> 9, dd = 20 + lane - blk4 * 12;
      const unsigned val = dd == 20 ? 0x3f803f80u : 0u;
      *(LDS_AS u32x4*)(tp + 1536 + blk4 * 512 + dd * 16) = u32x4{val, val, val, val};
    }
  }
  unsigned vo[5];
#pragma unroll
  for (int i = 0; i < 5; ++i) {
    const int p = i * 64 + lane, row = (p * 6554) >> 16;
    vo[i] = (unsigned)(row * 1280 + (p - row * 10) * 16 - (i & 3) * 1024);
  }
#define BABL_W 0
#define W_SRC(W, grp) ((W) + cbk * 160 + (size_t)((grp) * 256 + wid * 32) * 1280)
#define W_M0(reg, hi) SET_M0(lds0 + (unsigned)(KB_WR + (reg) * KB_GRP + wid * 5120 + (hi) * 4096))
#define W_P(W, i, grp) if (!(BABL_W)) asm volatile("global_load_lds_dwordx4 %0, %1 offset:%2" :: "v"(vo[i]), "s"(W_SRC(W, grp)), "i"(((i) & 3) * 1024) : "memory")
  const char* hsrc = ws + WS_H + ((size_t)(rbk * 4 + mi) * 48 + kh) * 1024 + 4096;
#define H_LOAD(D, j, grp) asm volatile("global_load_dwordx4 %0, %1, %2 offset:%3" : "=v"(D[j]) : "v"(vlin), "s"(hsrc + (grp) * 8192), "i"((j) * 2048 - 4096) : "memory")
  const unsigned wra = lds0 + (unsigned)(KB_WR + (kh * 32 + 4 * ((gq & 1) + 4 * (gq >> 1)) + ((lane & 15) >> 2)) * 160 + (lane & 3) * 8);
  const unsigned wrb = wra + KB_GRP;

  unsigned oo[3];
#pragma unroll
  for (int g = 0; g < 3; ++g) {
    const int Gc = head * 5 + 2 * g + h, G8 = Gc & 7;
    oo[g] = (unsigned)((r >> 4) * (20 * 1024) + (r & 15) * 16 + (Gc >> 3) * 1024 + ((G8 >> 2) * 2 + (G8 & 1)) * 256 + ((G8 >> 1) & 1) * 8);
    PIN(oo[g]);
  }
  bf16x8 hA[4], hB[4];
  asm volatile("s_waitcnt vmcnt(4)" : "+v"(qa), "+v"(qbv), "+v"(ka0), "+v"(kb0) :: "memory");
  f32x16 o;
#pragma unroll
  for (int i = 0; i < 16; ++i) o[i] = 0.f;
  const f32x16 zero = o;
  if (h == 1) { qbv[0] = 0u; qbv[1] = 0u; kb0[0] = 0x00003f80u; kb0[1] = 0u; }
  const f32x16 s16 = MFMA32(ka0, qa, zero);
  f32x16 sA = MFMA32K8(__builtin_bit_cast(bf16x4, kb0), __builtin_bit_cast(bf16x4, qbv), s16);
  {
    float m = fmaxf(fmaxf(sA[0], sA[1]), sA[2]);
#pragma unroll
    for (int i = 3; i < 15; i += 2) m = fmaxf(fmaxf(m, sA[i]), sA[i + 1]);
    m = fmaxf(m, sA[15]);
    const auto mm = __builtin_amdgcn_permlane32_swap(__float_as_uint(m), __float_as_uint(m), false, false);
    m = fmaxf(__uint_as_float(mm[0]), __uint_as_float(mm[1]));
    if (h == 1) qbv[0] = cvtpk(-m, 0.f);
  }
  const bf16x4 qb4 = __builtin_bit_cast(bf16x4, qbv);
  sA = MFMA32K8(__builtin_bit_cast(bf16x4, kb0), qb4, s16);
  f32x16 sB;
  f32x4 acc[5];
#pragma unroll
  for (int j = 0; j < 5; ++j) acc[j] = f32x4{0.f, 0.f, 0.f, 0.f};
  WAIT_BAR(0);
  STAMP(1, 1);
  STAMPC(1, 14);
  STAMP2(2, 0);

  unsigned a16 = lds0 + vlin, a8 = lds0 + (unsigned)lane * 8u;
  int sdelta = AT_SLOT;
  bf16x8 kaA, v0A, v1A, kaB, v0B, v1B;
  bf16x4 kbA, kbB;
  bf16x4 wl[5], wu[5];
  u32x4 p0, p1 = {0u, 0u, 0u, 0u};
  v1B = __builtin_bit_cast(bf16x8, p1);
#define RD_KA(X, j) ka##X = *(const LDS_AS bf16x8*)(LP(a16) + (j) * AT_TILE)
#define RD_KB(X, j) kb##X = *(const LDS_AS bf16x4*)(LP(a8) + (j) * AT_TILE + 1024)
#define RD_V0(X, j) v0##X = *(const LDS_AS bf16x8*)(LP(a16) + (j) * AT_TILE + 1536)
#define RD_V1(X, j) v1##X = *(const LDS_AS bf16x8*)(LP(a16) + (j) * AT_TILE + 2560)
#define W_LO(WA, j, t) wl[t] = tr_read(LP(WA) + (j) * 10240 + (t) * 32)
#define W_HI(WA, j, t) wu[t] = tr_read(LP(WA) + (j) * 10240 + (t) * 32 + 1280)
#define BABL 0
#define G_MM(HS, j, t) do { if (BABL & 1) { PIN(wl[t]); PIN(wu[t]); PIN(HS[j]); } else { acc[t] = MFMA16(cat8(wl[t], wu[t]), HS[j], acc[t]); } } while (0)
#define EX2(S, i) do { S[i] = __builtin_amdgcn_exp2f(S[i]); S[i + 1] = __builtin_amdgcn_exp2f(S[i + 1]); } while (0)
#define MID() do { __builtin_amdgcn_sched_group_barrier(0x400, 2, 0); __builtin_amdgcn_sched_group_barrier(0x008, 1, 0); \
                    __builtin_amdgcn_sched_group_barrier(0x400, 2, 0); __builtin_amdgcn_sched_group_barrier(0x002, 2, 0); } while (0)
#define AT_STEP(SC, SN, X, Y, JN, PRE, WP, A0, A1, A2, A3, A4, A5, V0_, V1_, V2_, M1, M2, M3)            \
  do {                                                                                                   \
    PRE;                                                                                                 \
    asm volatile("" : "+v"(ka##X), "+v"(kb##X), "+v"(v0##X), "+v"(v1##X)); WP; SBAR();                   \
    EX2(SC, 0); PIN(SC); SBAR();                                                                         \
    o = MFMA32(v1##Y, __builtin_bit_cast(bf16x8, p1), o); SBAR();                                        \
    RD_KA(Y, JN); RD_KB(Y, JN); RD_V0(Y, JN); RD_V1(Y, JN); A0; A1; A2; A3; A4; A5; V0_; V1_; SBAR(); \
    M1; EX2(SC, 2); EX2(SC, 4); p0[0] = cvtpk(SC[0], SC[1]); p0[1] = cvtpk(SC[2], SC[3]); MID();         \
    asm volatile("" : "+v"(p0), "+v"(SC), "+v"(acc[0]), "+v"(acc[3])); SBAR();                           \
    SN = MFMA32(ka##X, qa, zero); SBAR();                                                                \
    M2; EX2(SC, 6); EX2(SC, 8); p0[2] = cvtpk(SC[4], SC[5]); p0[3] = cvtpk(SC[6], SC[7]); MID();         \
    asm volatile("" : "+v"(p0), "+v"(SC), "+v"(acc[1])); SBAR();                                         \
    SN = MFMA32K8(kb##X, qb4, SN); SBAR();                                                               \
    V2_; SBAR();                                                                                         \
    M3; EX2(SC, 10); EX2(SC, 12); p1[0] = cvtpk(SC[8], SC[9]); p1[1] = cvtpk(SC[10], SC[11]); MID();     \
    asm volatile("" : "+v"(p1), "+v"(SC), "+v"(acc[2]), "+v"(acc[4])); SBAR();                           \
    o = MFMA32(v0##X, __builtin_bit_cast(bf16x8, p0), o); SBAR();                                        \
    EX2(SC, 14); p1[2] = cvtpk(SC[12], SC[13]); p1[3] = cvtpk(SC[14], SC[15]); PIN(p1); SBAR();          \
  } while (0)
#define WP_E asm volatile("" : "+v"(wl[0]), "+v"(wu[0]), "+v"(wl[1]), "+v"(wu[1]), "+v"(wl[2]), "+v"(wu[2]))
#define WP_O asm volatile("" : "+v"(wl[3]), "+v"(wu[3]), "+v"(wl[4]), "+v"(wu[4]))
#define FLIP() do { a16 += (unsigned)sdelta; a8 += (unsigned)sdelta; sdelta = -sdelta; } while (0)
#define SYNC_H(HN) asm volatile("s_waitcnt vmcnt(3) lgkmcnt(0)\n\ts_barrier" : "+v"(HN[0]) :: "memory")
#define WAIT_H(HC, j, N) asm volatile("s_waitcnt vmcnt(" #N ")" : "+v"(HC[j]) :: "memory")
#define STEP_E(JN, PRE, WC, HC, j, VA, VB)                                                               \
  AT_STEP(sA, sB, A, B, JN, PRE, WP_E, W_LO(WC, j, 3), W_HI(WC, j, 3), W_LO(WC, j, 4), W_HI(WC, j, 4), NOP_, NOP_, NOP_, VA, VB, \
          G_MM(HC, j, 0), G_MM(HC, j, 1), G_MM(HC, j, 2))
#define STEP_O(JN, PRE, WA, jn, HC, j, VA, VB)                                                           \
  AT_STEP(sB, sA, B, A, JN, PRE, WP_O, W_LO(WA, jn, 0), W_HI(WA, jn, 0), W_LO(WA, jn, 1), W_HI(WA, jn, 1), W_LO(WA, jn, 2), \
          W_HI(WA, jn, 2), NOP_, VA, VB, G_MM(HC, j, 3), NOP_, G_MM(HC, j, 4))
#define CHUNK_G(c, WC, HC, WN, HN, RN, NW, NG, HG)                                                       \
  do {                                                                                                   \
    STEP_E(1, NOP_, WC, HC, 0, W_M0(RN, 0); W_P(NW, 0, NG), W_P(NW, 1, NG));                             \
    STEP_O(2, NOP_, WC, 1, HC, 0, W_P(NW, 2, NG), W_P(NW, 3, NG));                                       \
    STEP_E(3, WAIT_H(HC, 1, 6), WC, HC, 1, W_M0(RN, 1); W_P(NW, 4, NG), KV_M0((c) + 1); KV_P0((c) + 1)); \
    STEP_O(4, NOP_, WC, 2, HC, 1, KV_P1((c) + 1), KV_P23((c) + 1));                                      \
    STEP_E(5, WAIT_H(HC, 2, 10), WC, HC, 2, H_LOAD(HN, 0, HG), H_LOAD(HN, 1, HG));                       \
    STEP_O(6, NOP_, WC, 3, HC, 2, H_LOAD(HN, 2, HG), H_LOAD(HN, 3, HG));                                 \
    STEP_E(7, WAIT_H(HC, 3, 13), WC, HC, 3, NOP_, NOP_);                                                 \
    STEP_O(0, SYNC_H(HN); STAMP2(2, 1 + (c)); FLIP(), WN, 0, HC, 3, NOP_, NOP_);                         \
  } while (0)
#define PLAIN2_E(JN, VA, VB) AT_STEP(sA, sB, A, B, JN, NOP_, NOP_, NOP_, NOP_, NOP_, NOP_, NOP_, NOP_, NOP_, VA, VB, NOP_, NOP_, NOP_)
#define PLAIN2_O(JN, VA, VB) AT_STEP(sB, sA, B, A, JN, NOP_, NOP_, NOP_, NOP_, NOP_, NOP_, NOP_, NOP_, NOP_, VA, VB, NOP_, NOP_, NOP_)
#define PLAIN_E(JN, X0, X1, X2, X3) AT_STEP(sA, sB, A, B, JN, NOP_, NOP_, X0, X1, X2, X3, NOP_, NOP_, NOP_, NOP_, NOP_, NOP_, NOP_, NOP_)
#define PLAIN_O(JN, X0, X1, X2, X3) AT_STEP(sB, sA, B, A, JN, NOP_, NOP_, X0, X1, X2, X3, NOP_, NOP_, NOP_, NOP_, NOP_, NOP_, NOP_, NOP_)

  RD_KA(A, 0); RD_KB(A, 0); RD_V0(A, 0); RD_V1(A, 0);
  PLAIN2_E(1, W_M0(0, 0); W_P(w2, 0, 0), W_P(w2, 1, 0));
  PLAIN2_O(2, W_P(w2, 2, 0), W_P(w2, 3, 0));
  PLAIN2_E(3, W_M0(0, 1); W_P(w2, 4, 0), KV_M0(1); KV_P0(1));
  PLAIN2_O(4, KV_P1(1), KV_P23(1));
  PLAIN2_E(5, H_LOAD(hA, 0, 0), H_LOAD(hA, 1, 0));
  PLAIN2_O(6, H_LOAD(hA, 2, 0), H_LOAD(hA, 3, 0));
  PLAIN_E(7, NOP_, NOP_, NOP_, NOP_);
  AT_STEP(sB, sA, B, A, 0, SYNC_H(hA); STAMP2(2, 1); FLIP(), NOP_, W_LO(wra, 0, 0), W_HI(wra, 0, 0), W_LO(wra, 0, 1), W_HI(wra, 0, 1),
          W_LO(wra, 0, 2), W_HI(wra, 0, 2), NOP_, NOP_, NOP_, NOP_, NOP_, NOP_);
#pragma unroll 1
  for (int c = 1; c < 7; c += 2) {
    CHUNK_G(c, wra, hA, wrb, hB, 1, w2, c, c);
    const char* wn = c < 5 ? w2 : wo;
    CHUNK_G(c + 1, wrb, hB, wra, hA, 0, wn, (c < 5 ? c + 1 : 0), (c < 5 ? c + 1 : 5));
  }
  const int tok = rbk * 64 + mi * 16 + (lane & 15);
  const size_t xo = (size_t)tok * 1280 + (cbk * 80 + 4 * gq) * 2;
  LDS_AS f32x4* red = (LDS_AS f32x4*)(lds + KB_RED) + (mi * 5) * 64 + lane;
  u32x2 gv[5], xv[5];
  PLAIN_E(1, NOP_,
          _Pragma("unroll") for (int j = 0; j < 5; ++j) gv[j] = *(const u32x2*)(b2 + cbk * 80 + j * 16 + 4 * gq),
          _Pragma("unroll") for (int j = 0; j < 5; ++j) xv[j] = *(const u32x2*)(x + xo + j * 32),
          if (kh == 1) { red[0] = acc[0]; red[64] = acc[1]; red[128] = acc[2]; } else { red[192] = acc[3]; red[256] = acc[4]; });
  PLAIN_O(2, NOP_, NOP_, NOP_, NOP_);
  PLAIN_E(3, NOP_, NOP_, NOP_, NOP_);
  PLAIN_O(4, asm volatile("s_waitcnt vmcnt(0)" : "+v"(hA[1]), "+v"(hA[2]), "+v"(hA[3]) :: "memory");
          _Pragma("unroll") for (int j = 0; j < 5; ++j) { PIN(gv[j]); PIN(xv[j]); } W_M0(1, 0), W_P(wo, 0, 1), W_P(wo, 1, 1), W_P(wo, 2, 1));
  AT_STEP(sA, sB, A, B, 5, LBAR(0), NOP_, W_P(wo, 3, 1), W_M0(1, 1), W_P(wo, 4, 1), SET_M0(lds0 + (unsigned)(wid * 2560)),
          asm volatile("global_load_lds_dwordx4 %0, %3\n\tglobal_load_lds_dwordx4 %1, %3 offset:1024\n\t"
                       "s_mov_b32 exec_hi, 0\n\tglobal_load_lds_dwordx4 %2, %3 offset:2048\n\ts_mov_b32 exec_hi, -1"
                       :: "v"(vo[0]), "v"(vo[1]), "v"(vo[2]), "s"(wo + cbk * 160 + (size_t)(512 + wid * 16) * 1280) : "memory"),
          NOP_, NOP_, NOP_, NOP_, NOP_, NOP_, NOP_);
#define G_FIN(j)                                                                                          \
  do {                                                                                                    \
    const f32x4 tt = acc[j] + og[j] + f32x4{bf_lo(gv[j][0]), bf_hi(gv[j][0]), bf_lo(gv[j][1]), bf_hi(gv[j][1])}; \
    float t[4];                                                                                           \
    _Pragma("unroll") for (int e = 0; e < 4; ++e)                                                         \
      t[e] = tt[e] * __builtin_amdgcn_rcpf(1.0f + __builtin_amdgcn_exp2f(tt[e] * -1.4426950408889634f));  \
    gv[j] = u32x2{cvtpk(t[0], t[1]), cvtpk(t[2], t[3])};                                                  \
    PIN(gv[j]);                                                                                           \
  } while (0)
  {
    f32x4 og[5];
    if (kh == 0) { og[0] = red[0]; og[1] = red[64]; og[2] = red[128]; G_FIN(0); G_FIN(1); G_FIN(2); }
    else { og[3] = red[192]; og[4] = red[256]; G_FIN(3); G_FIN(4); }
  }
#undef G_FIN
  SBAR();
  PLAIN_O(6, NOP_, NOP_, NOP_, NOP_);
  PLAIN_E(7, NOP_, NOP_, NOP_, NOP_);
  PLAIN_O(7, NOP_, NOP_, NOP_, NOP_);
  o = MFMA32(v1B, __builtin_bit_cast(bf16x8, p1), o);

  STAMP(1, 3);
  STAMPC(1, 15);
  STAMP2(2, 8);
  float l = __uint_as_float(__builtin_amdgcn_permlane32_swap(__float_as_uint(o[8]), __float_as_uint(o[8]), false, false)[1]);
  float big = fmaxf(fmaxf(__builtin_fabsf(o[0]), __builtin_fabsf(o[1])), l);
#pragma unroll
  for (int i = 2; i < 12; i += 2) big = fmaxf(fmaxf(big, __builtin_fabsf(o[i])), __builtin_fabsf(o[i + 1]));
  bool unsafe = !(big < 1.0e38f) || !(l > 0.25f);
#pragma unroll
  for (int i = 0; i < 12; ++i) unsafe |= o[i] != o[i];
  if (__builtin_expect(__any(unsafe), 0)) {
    if (h == 1) { qbv[0] = 0u; }
    const bf16x4 qz = __builtin_bit_cast(bf16x4, qbv);
    float m = -3.0e38f;
#pragma unroll
    for (int i = 0; i < 16; ++i) o[i] = 0.f;
    const unsigned onev = r == 20 ? 0x3f803f80u : 0u;
#pragma unroll 1
    for (int t = 0; t < 64; ++t) {
      const bf16x8 ka = *(const bf16x8*)(Kh + t * 1280 + lane * 16);
      u32x2 kb = *(const u32x2*)(Kh + t * 1280 + 1024 + r * 8);
      u32x4 v0 = {onev, onev, onev, onev}, v1 = v0;
      if (r < 20) {
        v0 = *(const u32x4*)(Vh + t * 1280 + vv);
        v1 = *(const u32x4*)(Vh + t * 1280 + 640 + vv);
      }
      f32x16 s = MFMA32(ka, qa, zero);
      s = MFMA32K8(__builtin_bit_cast(bf16x4, kb), qz, s);
      float tm = s[0];
#pragma unroll
      for (int i = 1; i < 16; ++i) tm = fmaxf(tm, s[i]);
      tm = fmaxf(tm, xhalf(tm));
      const float mn = fmaxf(m, tm);
      const float al = __builtin_amdgcn_exp2f(m - mn);
      m = mn;
      u32x4 q0, q1;
#pragma unroll
      for (int i = 0; i < 16; ++i) { o[i] *= al; s[i] = __builtin_amdgcn_exp2f(s[i] - mn); }
#pragma unroll
      for (int e = 0; e < 4; ++e) { q0[e] = cvtpk(s[2 * e], s[2 * e + 1]); q1[e] = cvtpk(s[8 + 2 * e], s[9 + 2 * e]); }
      o = MFMA32(__builtin_bit_cast(bf16x8, v0), __builtin_bit_cast(bf16x8, q0), o);
      o = MFMA32(__builtin_bit_cast(bf16x8, v1), __builtin_bit_cast(bf16x8, q1), o);
    }
    l = __shfl(o[8], 32 + r);
  }
  {
    const float inv = __builtin_amdgcn_rcpf(l);
    const char* ob = ws + WS_O + (size_t)(qb * 16 + wid * 2) * (20 * 1024);
#pragma unroll
    for (int g = 0; g < 3; ++g)
      if (g < 2 || h == 0)
        st8s(ob, oo[g], u32x2{cvtpk(o[4 * g] * inv, o[4 * g + 1] * inv), cvtpk(o[4 * g + 2] * inv, o[4 * g + 3] * inv)});
  }
  asm volatile("s_waitcnt vmcnt(3) lgkmcnt(0)\n\ts_barrier" ::: "memory");
  {
    const srd_t ro = make_srd(ws + WS_O, (unsigned)(WS_G - WS_O));
    const unsigned oa = (unsigned)(((rbk * 4 + mi) * 20 + kh) * 1024);
    bf16x8 wfa[5][5];
#pragma unroll
    for (int c = 0; c < 5; ++c)
#pragma unroll
      for (int t = 0; t < 5; ++t)
        wfa[c][t] = cat8(tr_read(LP(wra) + c * 10240 + t * 32), tr_read(LP(wra) + c * 10240 + t * 32 + 1280));
    SBAR();
#pragma unroll
    for (int j = 0; j < 5; ++j) acc[j] = f32x4{0.f, 0.f, 0.f, 0.f};
    __builtin_amdgcn_s_sleep(SENT_DELAY);
    u32x4 af[10];
    for (unsigned spins = 0; spins < (1u << 15); ++spins) {
      asm volatile("" ::: "memory");
#pragma unroll
      for (int c = 0; c < 10; ++c) af[c] = bload16_dev(ro, vlin, oa + c * 2048u);
      unsigned mx = 0u;
#pragma unroll
      for (int c = 0; c < 10; ++c) mx = max(max(max(mx, af[c][0]), af[c][1]), max(af[c][2], af[c][3]));
      if (!__any(mx == 0xffffffffu)) break;
      __builtin_amdgcn_s_sleep(4);
    }
    SBAR();
    bf16x8 wfb[5][5];
#pragma unroll
    for (int c = 5; c < 10; ++c) {
      const unsigned wa = c < 8 ? wrb + (unsigned)((c - 4) * 10240) : wra - (unsigned)KB_WR + (unsigned)((c - 8) * 10240);
      if (c < 8) {
#pragma unroll
        for (int t = 0; t < 5; ++t) wfb[c - 5][t] = cat8(tr_read(LP(wa) + t * 32), tr_read(LP(wa) + t * 32 + 1280));
      }
    }
    SBAR();
#pragma unroll
    for (int c = 0; c < 5; ++c)
#pragma unroll
      for (int t = 0; t < 5; ++t) acc[t] = MFMA16(wfa[c][t], __builtin_bit_cast(bf16x8, af[c]), acc[t]);
#pragma unroll
    for (int c = 8; c < 10; ++c)
#pragma unroll
      for (int t = 0; t < 5; ++t)
        wfb[c - 5][t] = cat8(tr_read(LP(wra - (unsigned)KB_WR) + (c - 8) * 10240 + t * 32), tr_read(LP(wra - (unsigned)KB_WR) + (c - 8) * 10240 + t * 32 + 1280));
#pragma unroll
    for (int c = 5; c < 10; ++c)
#pragma unroll
      for (int t = 0; t < 5; ++t) acc[t] = MFMA16(wfb[c - 5][t], __builtin_bit_cast(bf16x8, af[c]), acc[t]);
  }
  if (kh == 1) { red[0] = acc[0]; red[64] = acc[1]; red[128] = acc[2]; } else { red[192] = acc[3]; red[256] = acc[4]; }
  LBAR(0);
#define O_FIN(j)                                                                                          \
  do {                                                                                                    \
    const f32x4 sv = acc[j] + red[(j) * 64];                                                              \
    const float r0 = bf_lo(xv[j][0]) + sv[0] * bf_lo(gv[j][0]), r1 = bf_hi(xv[j][0]) + sv[1] * bf_hi(gv[j][0]); \
    const float r2 = bf_lo(xv[j][1]) + sv[2] * bf_lo(gv[j][1]), r3 = bf_hi(xv[j][1]) + sv[3] * bf_hi(gv[j][1]); \
    *(u32x2*)(out + xo + (j) * 32) = u32x2{cvtpk(r0, r1), cvtpk(r2, r3)};                                 \
  } while (0)
  if (kh == 0) { O_FIN(0); O_FIN(1); O_FIN(2); } else { O_FIN(3); O_FIN(4); }
#undef O_FIN
  STAMP(1, 8);
}

extern "C" void kernel_launch(void* const* d_in, const int* in_sizes, int n_in, void* d_out, int out_size, void* d_ws,
                              size_t ws_size, hipStream_t stream) {
  (void)in_sizes; (void)n_in; (void)out_size; (void)ws_size;
  const char* x = (const char*)d_in[0];
  const char* wqkv = (const char*)d_in[1];
  const char* wo = (const char*)d_in[2];
  const char* w1 = (const char*)d_in[4];
  const unsigned short* b1 = (const unsigned short*)d_in[5];
  const char* w2 = (const char*)d_in[6];
  const unsigned short* b2 = (const unsigned short*)d_in[7];
  char* ws = (char*)d_ws;
  (void)hipFuncSetAttribute((const void*)ka_proj, hipFuncAttributeMaxDynamicSharedMemorySize, KA_LDS);
  (void)hipFuncSetAttribute((const void*)kb_main, hipFuncAttributeMaxDynamicSharedMemorySize, KB_LDS);
  ka_proj<<<256, 512, KA_LDS, stream>>>(x, wqkv, w1, b1, w2, wo, ws);
  kb_main<<<256, 512, KB_LDS, stream>>>(x, w2, b2, wo, ws, (char*)d_out);
}
```

```cpp
#include <hip/hip_runtime.h>
#include <hip/hip_bf16.h>
#include <stdint.h>

typedef __attribute__((ext_vector_type(8))) short bf16x8;
typedef __attribute__((ext_vector_type(4))) short bf16x4;
typedef __attribute__((ext_vector_type(16))) float f32x16;
typedef __attribute__((ext_vector_type(4))) float f32x4;
typedef __attribute__((ext_vector_type(2))) float f32x2;
typedef __attribute__((ext_vector_type(4))) unsigned u32x4;
typedef __attribute__((ext_vector_type(2))) unsigned u32x2;
typedef __attribute__((ext_vector_type(2))) __bf16 bf2;
typedef __attribute__((address_space(3))) char* lds_ptr;
#define LDS_AS __attribute__((address_space(3)))
#define DI __device__ __forceinline__

#define MFMA32(a, b, c) __builtin_amdgcn_mfma_f32_32x32x16_bf16((a), (b), (c), 0, 0, 0)
#define MFMA32K8(a, b, c) __builtin_amdgcn_mfma_f32_32x32x8bf16_1k((a), (b), (c), 0, 0, 0)
#define MFMA16(a, b, c) __builtin_amdgcn_mfma_f32_16x16x32_bf16((a), (b), (c), 0, 0, 0)
#define SBAR() __builtin_amdgcn_sched_barrier(0)
#define PIN(x) asm volatile("" : "+v"(x))
#define WAIT_BAR(N) asm volatile("s_waitcnt vmcnt(" #N ") lgkmcnt(0)\n\ts_barrier" ::: "memory")

constexpr int D_ = 640, NH = 32;
constexpr float LN_EPS = 1e-5f;
constexpr float QSCALE = 0.22360679774997896f * 1.4426950408889634f;

constexpr size_t WS_Q = 0;
constexpr size_t WS_K = WS_Q + (size_t)NH * 64 * 1280;
constexpr size_t WS_V = WS_K + (size_t)NH * 64 * 1280;
constexpr size_t WS_H = WS_V + (size_t)NH * 64 * 1280;
constexpr size_t WS_O = WS_H + (size_t)128 * 48 * 1024;
constexpr size_t WS_G = WS_O + (size_t)128 * 20 * 1024;

#define STAMP(kern, i) do { } while (0)
#define STAMPC(kern, i) do { } while (0)
#define STAMP2(kern, i) do { } while (0)

DI void st16s(const void* sb, unsigned off, u32x4 v) { asm volatile("global_store_dwordx4 %0, %1, %2 sc1\n\ts_nop 1" :: "v"(off), "v"(v), "s"(sb) : "memory"); }
DI void st8s(const void* sb, unsigned off, u32x2 v) { asm volatile("global_store_dwordx2 %0, %1, %2 sc1" :: "v"(off), "v"(v), "s"(sb) : "memory"); }
DI unsigned cvtpk(float lo, float hi) {
  f32x2 f = {lo, hi};
  return __builtin_bit_cast(unsigned, __builtin_convertvector(f, bf2));
}
DI float bf_lo(unsigned u) { return __uint_as_float(u << 16); }
DI float bf_hi(unsigned u) { return __uint_as_float(u & 0xffff0000u); }
typedef short v4i16_t __attribute__((ext_vector_type(4)));
DI bf16x4 tr_read(lds_ptr p) {
  return __builtin_bit_cast(bf16x4, __builtin_amdgcn_ds_read_tr16_b64_v4i16((LDS_AS v4i16_t*)p));
}
DI bf16x8 cat8(bf16x4 a, bf16x4 b) { return __builtin_shufflevector(a, b, 0, 1, 2, 3, 4, 5, 6, 7); }

constexpr int KA_BUF = 45056;
constexpr int KA_STAT = 3 * KA_BUF;
constexpr int KA_SCS = KA_STAT + 2048, KA_SB1 = KA_SCS + 3584, KA_SMR = KA_SB1 + 384;
constexpr int KA_LDS = KA_SMR + 2048;
#define LBAR(N) asm volatile("s_waitcnt lgkmcnt(" #N ")\n\ts_barrier" ::: "memory")
#define LP(a) ((lds_ptr)(uintptr_t)(a))
#define SET_M0(v) asm volatile("s_mov_b32 m0, %0\n\ts_nop 0" :: "s"(v) : "memory")
#define NOP_ do { } while (0)
#define MFMA4(a, b, c) __builtin_amdgcn_mfma_f32_4x4x4bf16_1k((a), (b), (c), 0, 0, 0)

typedef __amdgpu_buffer_rsrc_t srd_t;
DI srd_t make_srd(const void* p, unsigned bytes) { return __builtin_amdgcn_make_buffer_rsrc((void*)p, (short)0, (int)bytes, 0x00020000); }
DI float x32_add(float v) {
  const auto rr = __builtin_amdgcn_permlane32_swap(__float_as_uint(v), __float_as_uint(v), false, false);
  return __uint_as_float(rr[0]) + __uint_as_float(rr[1]);
}
DI bf16x4 lo4(bf16x8 v) { return __builtin_shufflevector(v, v, 0, 1, 2, 3); }
DI bf16x4 hi4(bf16x8 v) { return __builtin_shufflevector(v, v, 4, 5, 6, 7); }

template <int TYPE, int nh>
DI void ka_body(lds_ptr lds, const char* __restrict__ x, const char* __restrict__ wqkv, const char* __restrict__ wh1,
                const unsigned short* __restrict__ b1, const char* __restrict__ w2, const char* __restrict__ wo,
                const int n0, const int f0, const int rb, const int cb, char* __restrict__ ws) {
  constexpr int NHT = nh ? 1 : 2;
  const int tid = threadIdx.x, lane = tid & 63;
  const int w = __builtin_amdgcn_readfirstlane(tid >> 6);
  const int mi = w & 3;
  const int r = lane & 31, h = lane >> 5;
  const unsigned lds0 = (unsigned)(uintptr_t)lds;
  STAMP(0, 0);
  const unsigned vx0 = (unsigned)((lane >> 3) * 1280 + (((lane & 7) ^ (lane >> 4)) << 4));
  const unsigned vx1 = (unsigned)((8 + (lane >> 3)) * 1280 + (((lane & 7) ^ (4 + (lane >> 4))) << 4) - 1024);
  const unsigned vw = (unsigned)((lane >> 3) * 3840 + ((((((lane >> 2) ^ (lane >> 4)) & 1) << 2) | (lane & 3)) << 4));
  const int p0 = w * 96 + lane, p1 = w * 96 + 64 + r;
  const int r0 = (p0 * 5462) >> 16, r1 = (p1 * 5462) >> 16;
  const unsigned vh0 = (unsigned)(r0 * 3072 + (p0 - r0 * 12) * 16), vh1 = (unsigned)(r1 * 3072 + (p1 - r1 * 12) * 16 - 1024);
  const char* xs = x + (size_t)(rb * 128 + 16 * w) * 1280;
  const char* wsrc = wqkv + ((size_t)(8 * w) * 1920 + n0) * 2;
  const char* hsrc = wh1 + f0 * 2;
  const unsigned mx = lds0 + (unsigned)w * 2048u, mw = lds0 + 16384u + (unsigned)w * 1024u, mh = lds0 + 32768u + (unsigned)w * 1536u;
#define KA_DX(c, buf) do { SET_M0(mx + (unsigned)(buf)); asm volatile("global_load_lds_dwordx4 %0, %2\n\tglobal_load_lds_dwordx4 %1, %2 offset:1024" :: "v"(vx0), "v"(vx1), "s"(xs + (c) * 128) : "memory"); } while (0)
#define KA_DW(c, buf, hf) do { SET_M0(mw + (unsigned)(buf) + (hf) * 8192u); asm volatile("global_load_lds_dwordx4 %0, %1" :: "v"(vw), "s"(wsrc + (size_t)(c) * (64 * 3840) + (hf) * 128) : "memory"); } while (0)
#define KA_DH(c, buf) do { SET_M0(mh + (unsigned)(buf)); asm volatile("global_load_lds_dwordx4 %0, %2\n\ts_mov_b32 exec_hi, 0\n\tglobal_load_lds_dwordx4 %1, %2 offset:1024\n\ts_mov_b32 exec_hi, -1" :: "v"(vh0), "v"(vh1), "s"(hsrc + (size_t)(c) * (64 * 3072)) : "memory"); } while (0)
#define KA_ISSUE(c, buf) do { KA_DX(c, buf); KA_DW(c, buf, 0); KA_DW(c, buf, 1); KA_DH(c, buf); } while (0)
  unsigned braw = 0, warm = 0;
  if (nh == 0 && tid < 96) braw = b1[f0 + tid];
  SBAR();
  KA_ISSUE(0, 0);
  KA_ISSUE(1, KA_BUF);

  unsigned xa[4], wa0[4], ha[4];
  {
    const unsigned wb = lds0 + (unsigned)(16384 + nh * 8192 + (8 * h + ((lane & 15) >> 2)) * 128 + 64 * ((lane >> 3) & 1) +
                                          (16 * ((lane >> 4) & 1) + 4 * (lane & 3)) * 2);
    const unsigned hb = lds0 + (unsigned)(32768 + (8 * h + ((lane & 15) >> 2)) * 192 + nh * 128 + (16 * ((lane >> 4) & 1) + 4 * (lane & 3)) * 2);
#pragma unroll
    for (int i = 0; i < 4; ++i) {
      const int ks = i ^ mi;
      xa[i] = lds0 + (unsigned)((mi * 32 + r) * 128 + (((2 * ks + h) ^ ((r >> 1) & 7)) << 4));
      wa0[i] = wb + (unsigned)(ks * 2048);
      ha[i] = hb + (unsigned)(ks * 3072);
    }
  }
  const int tq = rb * 4 + mi;
  const bool isq = n0 < 640;
  unsigned so[TYPE == 1 ? 2 : 8];
  if (TYPE == 1) {
#pragma unroll
    for (int j = 0; j < 2; ++j) {
      const int nn = n0 - 1280 + nh * 64 + j * 32 + r;
      const int head = (nn * 3277) >> 16;
      so[j] = (unsigned)(head * (64 * 1280) + (h * 20 + nn - head * 20) * 16);
      PIN(so[j]);
    }
  } else {
#pragma unroll
    for (int j = 0; j < 8; ++j) {
      const int G = (((isq ? n0 : n0 - 640) + nh * 64) >> 2) + h + 2 * j;
      const int head = (G * 205) >> 10;
      const int dg = G - head * 5;
      so[j] = (unsigned)(head * (64 * 1280) + (dg < 4 ? (dg >> 1) * 512 + r * 16 + (dg & 1) * 8 : 1024 + r * 8));
      PIN(so[j]);
    }
  }
  f32x16 acc[2], ach[NHT];
  f32x4 cq[2], chh[NHT], rsum = {0.f, 0.f, 0.f, 0.f}, rsq = rsum;
#pragma unroll
  for (int i = 0; i < 16; ++i) {
    acc[0][i] = 0.f; acc[1][i] = 0.f;
#pragma unroll
    for (int j = 0; j < NHT; ++j) ach[j][i] = 0.f;
  }
  cq[0] = rsum; cq[1] = rsum;
#pragma unroll
  for (int j = 0; j < NHT; ++j) chh[j] = rsum;
  bf16x8 sel;
  {
    const short sv = (lane & 15) == ((lane >> 4) & 1) ? (short)0x3f80 : (short)0;
    sel = bf16x8{sv, sv, sv, sv, sv, sv, sv, sv};
    PIN(sel);
  }
  bf16x8 xf[3], w0[3], w1[3], wh[3][NHT];
  unsigned wa1[4];
#pragma unroll
  for (int i = 0; i < 4; ++i) wa1[i] = wa0[i] ^ 64u;
#define KA_READ(B, i)                                                                                           \
  do {                                                                                                          \
    constexpr int s_ = ((B) + (i)) % 3;                                                                         \
    xf[s_] = *(const LDS_AS bf16x8*)(LP(xa[i]) + BUFOFF(B));                                                    \
    w0[s_] = cat8(tr_read(LP(wa0[i]) + BUFOFF(B)), tr_read(LP(wa0[i]) + BUFOFF(B) + 512));                      \
    w1[s_] = cat8(tr_read(LP(wa1[i]) + BUFOFF(B)), tr_read(LP(wa1[i]) + BUFOFF(B) + 512));                      \
    _Pragma("unroll") for (int j_ = 0; j_ < NHT; ++j_)                                                          \
      wh[s_][j_] = cat8(tr_read(LP(ha[i]) + BUFOFF(B) + j_ * 64), tr_read(LP(ha[i]) + BUFOFF(B) + j_ * 64 + 768)); \
  } while (0)
#define SGB(m, n) __builtin_amdgcn_sched_group_barrier((m), (n), 0)
#define KA_STEP(B, i, RD) KA_STEPX(B, i, RD, true)
#define KA_STEPX(B, i, RD, ROWS)                                                                                \
  do {                                                                                                          \
    constexpr int b_ = ((B) + (i)) % 3;                                                                         \
    if constexpr (NHT == 2) asm volatile("" : "+v"(xf[b_]), "+v"(w0[b_]), "+v"(w1[b_]), "+v"(wh[b_][0]), "+v"(wh[b_][NHT - 1])); \
    else asm volatile("" : "+v"(xf[b_]), "+v"(w0[b_]), "+v"(w1[b_]), "+v"(wh[b_][0]));                          \
    SBAR();                                                                                                     \
    RD;                                                                                                         \
    if (TYPE == 1) { acc[0] = MFMA32(xf[b_], w0[b_], acc[0]); acc[1] = MFMA32(xf[b_], w1[b_], acc[1]); }        \
    else { acc[0] = MFMA32(w0[b_], xf[b_], acc[0]); acc[1] = MFMA32(w1[b_], xf[b_], acc[1]); }                  \
    _Pragma("unroll") for (int j_ = 0; j_ < NHT; ++j_) ach[j_] = MFMA32(wh[b_][j_], xf[b_], ach[j_]);           \
    SGB(0x8, 1); SGB(0x100, 3); SGB(0x8, 1); SGB(0x100, 2); SGB(0x8, 1); SGB(0x100, 2);                         \
    if (NHT == 2) { SGB(0x8, 1); SGB(0x100, 2); }                                                               \
    if constexpr (NHT == 2) asm volatile("" : "+v"(acc[0]), "+v"(acc[1]), "+v"(ach[0]), "+v"(ach[NHT - 1]));    \
    else asm volatile("" : "+v"(acc[0]), "+v"(acc[1]), "+v"(ach[0]));                                           \
    SBAR();                                                                                                     \
    if ((i) == 0) {                                                                              \
      cq[0] = MFMA16(w0[b_], sel, cq[0]); cq[1] = MFMA16(w1[b_], sel, cq[1]);                                   \
      _Pragma("unroll") for (int j_ = 0; j_ < NHT; ++j_) chh[j_] = MFMA16(wh[b_][j_], sel, chh[j_]);            \
      if constexpr (NHT == 2) asm volatile("" : "+v"(cq[0]), "+v"(cq[1]), "+v"(chh[0]), "+v"(chh[NHT - 1]));    \
      else asm volatile("" : "+v"(cq[0]), "+v"(cq[1]), "+v"(chh[0]));                                           \
    }                                                                                                           \
    if ((ROWS) && (nh ? (i) < 2 : (i) >= 2)) {                                                   \
      rsq = MFMA4(lo4(xf[b_]), lo4(xf[b_]), rsq); rsum = MFMA16(xf[b_], sel, rsum);                             \
      rsq2 = MFMA4(hi4(xf[b_]), hi4(xf[b_]), rsq2);                                                             \
      asm volatile("" : "+v"(rsum), "+v"(rsq), "+v"(rsq2));                                                     \
    }                                                                                                           \
    SBAR();                                                                                                     \
  } while (0)
#define BUFOFF(B) ((B) * KA_BUF)
#define KA_CHUNK(c, B, BN, WAITN, PREV, MORE)                                                                   \
  do {                                                                                                          \
    KA_STEP(B, 0, KA_READ(B, 2));                                                                               \
    if ((PREV)) { KA_DH((c) + 2, BUFOFF(((B) + 2) % 3)); SBAR(); }                               \
    KA_STEP(B, 1, KA_READ(B, 3));                                                                               \
    WAIT_BAR(WAITN);                                                                                            \
    KA_STEP(B, 2, KA_READ(BN, 0));                                                                              \
    if ((MORE)) { KA_DX((c) + 3, BUFOFF(B)); SBAR(); }                                           \
    KA_STEP(B, 3, KA_READ(BN, 1));                                                                              \
    if ((MORE)) { KA_DW((c) + 3, BUFOFF(B), 0); KA_DW((c) + 3, BUFOFF(B), 1); SBAR(); }          \
  } while (0)
  f32x4 rsq2 = rsum;
  WAIT_BAR(6);
  STAMP(0, 1);
  STAMPC(0, 14);
  KA_READ(0, 0);
  KA_READ(0, 1);
  KA_STEP(0, 0, KA_READ(0, 2));
  KA_DX(2, BUFOFF(2)); KA_DW(2, BUFOFF(2), 0); KA_DW(2, BUFOFF(2), 1); SBAR();
  KA_STEP(0, 1, KA_READ(0, 3));
  KA_DH(2, BUFOFF(2)); SBAR();
  WAIT_BAR(6);
  KA_STEP(0, 2, KA_READ(1, 0));
  KA_DX(3, BUFOFF(0)); SBAR();
  KA_STEP(0, 3, KA_READ(1, 1));
  KA_DW(3, BUFOFF(0), 0); KA_DW(3, BUFOFF(0), 1); SBAR();
#pragma unroll 1
  for (int c = 1; c < 7; c += 3) {
    KA_CHUNK(c, 1, 2, 6, true, true);
    KA_CHUNK(c + 1, 2, 0, 6, true, true);
    KA_CHUNK(c + 2, 0, 1, 6, true, true);
  }
  KA_CHUNK(7, 1, 2, 6, true, false);
  KA_CHUNK(8, 2, 0, 0, false, false);
  KA_STEP(0, 0, KA_READ(0, 2));
  KA_STEP(0, 1, KA_READ(0, 3));
  if (nh == 0) {
    asm volatile("" : "+v"(xf[2]), "+v"(xf[0]));
    rsq = MFMA4(lo4(xf[2]), lo4(xf[2]), rsq); rsum = MFMA16(xf[2], sel, rsum); rsq2 = MFMA4(hi4(xf[2]), hi4(xf[2]), rsq2);
    rsum = MFMA16(xf[0], sel, rsum); rsq = MFMA4(lo4(xf[0]), lo4(xf[0]), rsq); rsq2 = MFMA4(hi4(xf[0]), hi4(xf[0]), rsq2);
  }
  asm volatile("s_nop 7" : "+v"(rsum), "+v"(rsq), "+v"(rsq2));
  rsq += rsq2;
  STAMP(0, 3);
  STAMPC(0, 15);
  LDS_AS float* st_r = (LDS_AS float*)(lds + KA_STAT);
  LDS_AS float* st_cs = (LDS_AS float*)(lds + KA_SCS);
  LDS_AS float* st_b = (LDS_AS float*)(lds + KA_SB1);
  {
    const int d = lane & 3;
    const float sq = x32_add(d == 0 ? rsq[0] : d == 1 ? rsq[1] : d == 2 ? rsq[2] : rsq[3]);
    if (h == 0) st_r[256 + nh * 128 + mi * 32 + r] = sq;
    if ((lane & 15) < 2) {
      const int q4 = (lane & 1) * 16 + (lane >> 4) * 4;
      *(LDS_AS f32x4*)(st_r + nh * 128 + mi * 32 + q4) = rsum;
      *(LDS_AS f32x4*)(st_cs + mi * 224 + nh * 64 + q4) = cq[0];
      *(LDS_AS f32x4*)(st_cs + mi * 224 + nh * 64 + 32 + q4) = cq[1];
#pragma unroll
      for (int j = 0; j < NHT; ++j) *(LDS_AS f32x4*)(st_cs + mi * 224 + 128 + nh * 64 + j * 32 + q4) = chh[j];
    }
    if (nh == 0 && tid < 96) st_b[tid] = __uint_as_float(braw << 16);
  }
  SBAR();
  KA_STEPX(0, 2, NOP_, false);
  LBAR(0);
  float cpart = 0.f;
  if (tid < 224) cpart = (st_cs[tid] + st_cs[224 + tid]) + (st_cs[448 + tid] + st_cs[672 + tid]);
  const float sx_ = st_r[mi * 32 + r] + st_r[128 + mi * 32 + r], sq_ = st_r[256 + mi * 32 + r] + st_r[384 + mi * 32 + r];
  SBAR();
  KA_STEPX(0, 3, NOP_, false);
#undef KA_READ
#undef KA_STEP
#undef KA_STEPX
#undef KA_CHUNK
#undef KA_ISSUE
  if (tid < 224) st_cs[tid] = cpart;
  {
    const char* ob = ws + WS_O + (size_t)blockIdx.x * 10240;
    const u32x4 ff = {0xffffffffu, 0xffffffffu, 0xffffffffu, 0xffffffffu};
    st16s(ob, (unsigned)tid * 16u, ff);
    if (w < 2) st16s(ob + 8192, (unsigned)tid * 16u, ff);
  }
  if (nh == 1) {
    const int l0_ = blockIdx.x * 85 + (tid - 256 < 85 ? tid - 256 : 0);
    const char* p = l0_ < 15360 ? w2 + (size_t)l0_ * 128 : wo + (size_t)(l0_ - 15360) * 128;
    if (w < 6) asm volatile("global_load_dword %0, %1, off" : "=v"(warm) : "v"(p) : "memory");
  }
  const float mu = sx_ * (1.0f / D_);
  const float var = fmaxf(sq_ * (1.0f / D_) - mu * mu, 0.f);
  const float rs = __builtin_amdgcn_rsqf(var + LN_EPS);
  LDS_AS f32x2* st_w = (LDS_AS f32x2*)(lds + KA_SMR) + w * 32;
  if (TYPE == 1 && h == 0) st_w[r] = f32x2{mu * rs, rs};
  LBAR(0);
  STAMP(0, 5);
  if (cb < 15) {
    if (TYPE == 1) {
      char* dst = ws + WS_V + (size_t)tq * 1280;
      const float cs0 = st_cs[nh * 64 + r], cs1 = st_cs[nh * 64 + 32 + r];
#pragma unroll
      for (int s = 0; s < 2; ++s) {
        f32x2 mr[8];
#pragma unroll
        for (int e = 0; e < 8; ++e) mr[e] = st_w[16 * s + 8 * (e >> 2) + 4 * h + (e & 3)];
        u32x4 o0, o1;
#pragma unroll
        for (int e = 0; e < 4; ++e) {
          const int i = 8 * s + 2 * e;
          o0[e] = cvtpk(acc[0][i] * mr[2 * e][1] - mr[2 * e][0] * cs0, acc[0][i + 1] * mr[2 * e + 1][1] - mr[2 * e + 1][0] * cs0);
          o1[e] = cvtpk(acc[1][i] * mr[2 * e][1] - mr[2 * e][0] * cs1, acc[1][i + 1] * mr[2 * e + 1][1] - mr[2 * e + 1][0] * cs1);
        }
        st16s(dst + s * 640, so[0], o0);
        st16s(dst + s * 640, so[1], o1);
      }
    } else {
      const float sc = isq ? rs * QSCALE : rs, ms = mu * sc;
      char* base = ws + (isq ? WS_Q : WS_K) + (size_t)tq * 1280;
#pragma unroll
      for (int j = 0; j < 2; ++j)
#pragma unroll
        for (int g = 0; g < 4; ++g) {
          const f32x4 cs = *(const LDS_AS f32x4*)(st_cs + nh * 64 + j * 32 + 8 * g + 4 * h);
          const u32x2 o = {cvtpk(acc[j][4 * g] * sc - ms * cs[0], acc[j][4 * g + 1] * sc - ms * cs[1]),
                           cvtpk(acc[j][4 * g + 2] * sc - ms * cs[2], acc[j][4 * g + 3] * sc - ms * cs[3])};
          st8s(base, so[j * 4 + g], o);
        }
    }
  }
  {
    const float sc = rs * __builtin_amdgcn_rsqf(var * rs * rs + LN_EPS), ms = mu * sc;
    constexpr float GK1 = -2.0f * 1.4426950408889634f * 0.7978845608028654f, GK2 = GK1 * 0.044715f;
    const char* dst = ws + WS_H + ((size_t)(rb * 8 + mi * 2) * 48 + (f0 >> 5) + nh * 2) * 1024;
    const unsigned ho = (unsigned)((r >> 4) * (48 * 1024) + (h * 16 + (r & 15)) * 16);
    const f32x2 sc2 = {sc, sc}, ms2 = {ms, ms}, k1 = {GK1, GK1}, k2 = {GK2, GK2}, one2 = {1.0f, 1.0f};
#pragma unroll
    for (int j = 0; j < NHT; ++j)
#pragma unroll
      for (int u = 0; u < 2; ++u) {
        u32x4 o;
#pragma unroll
        for (int gg = 0; gg < 2; ++gg) {
          const int g = 2 * u + gg;
          const f32x4 cs = *(const LDS_AS f32x4*)(st_cs + 128 + nh * 64 + j * 32 + 8 * g + 4 * h);
          const f32x4 bb = *(const LDS_AS f32x4*)(st_b + nh * 64 + j * 32 + 8 * g + 4 * h);
#pragma unroll
          for (int e = 0; e < 2; ++e) {
            const f32x2 a2 = {ach[j][4 * g + 2 * e], ach[j][4 * g + 2 * e + 1]};
            const f32x2 c2 = {cs[2 * e], cs[2 * e + 1]}, b2_ = {bb[2 * e], bb[2 * e + 1]};
            const f32x2 t = a2 * sc2 + (b2_ - ms2 * c2);
            const f32x2 arg = t * ((t * t) * k2 + k1);
            const f32x2 den = f32x2{__builtin_amdgcn_exp2f(arg[0]), __builtin_amdgcn_exp2f(arg[1])} + one2;
            const f32x2 v = t * f32x2{__builtin_amdgcn_rcpf(den[0]), __builtin_amdgcn_rcpf(den[1])};
            o[2 * gg + e] = cvtpk(v[0], v[1]);
          }
        }
        st16s(dst + j * 1024 + u * 512, ho, o);
      }
  }
  STAMP(0, 4);
  asm volatile("s_waitcnt vmcnt(0)" : "+v"(warm) :: "memory");
}

__global__ __launch_bounds__(512, 2) void ka_proj(const char* __restrict__ x, const char* __restrict__ wqkv,
                                                  const char* __restrict__ w1, const unsigned short* __restrict__ b1,
                                                  const char* __restrict__ w2, const char* __restrict__ wo,
                                                  char* __restrict__ ws) {
  extern __shared__ __attribute__((aligned(16))) char smem[];
  lds_ptr lds = (lds_ptr)smem;
  const int bid = blockIdx.x, xg = bid & 7, idx = bid >> 3;
  const int rb = (xg & 1) * 8 + (idx & 7), cb = (xg >> 1) * 4 + (idx >> 3);
  const int n0 = (cb < 15 ? cb : 14) * 128, f0 = cb * 96;
  const int nh = __builtin_amdgcn_readfirstlane(threadIdx.x >> 8);
  if (cb < 10) {
    if (nh) ka_body<0, 1>(lds, x, wqkv, w1, b1, w2, wo, n0, f0, rb, cb, ws);
    else ka_body<0, 0>(lds, x, wqkv, w1, b1, w2, wo, n0, f0, rb, cb, ws);
  } else {
    if (nh) ka_body<1, 1>(lds, x, wqkv, w1, b1, w2, wo, n0, f0, rb, cb, ws);
    else ka_body<1, 0>(lds, x, wqkv, w1, b1, w2, wo, n0, f0, rb, cb, ws);
  }
}

DI u32x4 bload16_dev(srd_t r, unsigned voff, unsigned soff) { return __builtin_amdgcn_raw_buffer_load_b128(r, (int)voff, (int)soff, 16); }

constexpr int AT_TILE = 3584;
constexpr int AT_SLOT = 28672;
constexpr int KB_WR = 2 * AT_SLOT;
constexpr int KB_GRP = 40960;
constexpr int KB_RED = KB_WR + 2 * KB_GRP;
constexpr int KB_LDS = KB_RED + 20480;

__global__ __launch_bounds__(512, 2) void kb_main(const char* __restrict__ x, const char* __restrict__ w2,
                                                  const unsigned short* __restrict__ b2, const char* __restrict__ wo,
                                                  char* __restrict__ ws, char* __restrict__ out) {
  extern __shared__ __attribute__((aligned(16))) char smem[];
  lds_ptr lds = (lds_ptr)smem;
  const int tid = threadIdx.x, lane = tid & 63;
  const int wid = __builtin_amdgcn_readfirstlane(tid >> 6);
  const int r = lane & 31, h = lane >> 5;
  const int xg = blockIdx.x & 7, idx = blockIdx.x >> 3;
  const int head = xg * 4 + (idx >> 3), qb = idx & 7;
  const int rbk = xg * 4 + (idx & 3), cbk = idx >> 2;
  const int mi = wid & 3, kh = wid >> 2, gq = lane >> 4;
  const unsigned lds0 = (unsigned)(uintptr_t)lds;
  const char* Kh = ws + WS_K + (size_t)head * (64 * 1280);
  const char* Vh = ws + WS_V + (size_t)head * (64 * 1280);
  const char* Qt = ws + WS_Q + (size_t)(head * 64 + qb * 8 + wid) * 1280;
  STAMP(1, 0);

  const unsigned vlin = (unsigned)lane * 16u;
  const unsigned vv = (unsigned)(h * 20 + r) * 16u;
  const unsigned vv2 = vv + (unsigned)(WS_V - WS_K) - 1280u - 1536u, vv3 = vv2 + 640u - 1024u;
  const unsigned long long mk16 = 0xffffull, mk20 = 0x000fffff000fffffull;
#define KV_M0(c) SET_M0(lds0 + (unsigned)(((c) & 1) * AT_SLOT + wid * AT_TILE))
#define KV_SRC(c) (Kh + (size_t)((c) * 8 + wid + 1) * 1280)
#define KV_P0(c) asm volatile("global_load_lds_dwordx4 %0, %1" :: "v"(vlin), "s"(KV_SRC(c)) : "memory")
#define KV_P1(c) asm volatile("s_mov_b64 exec, %2\n\tglobal_load_lds_dwordx4 %0, %1 offset:1024\n\ts_mov_b64 exec, -1" :: "v"(vlin), "s"(KV_SRC(c)), "s"(mk16) : "memory")
#define KV_P23(c) asm volatile("s_mov_b64 exec, %3\n\tglobal_load_lds_dwordx4 %0, %2 offset:1536\n\tglobal_load_lds_dwordx4 %1, %2 offset:2560\n\ts_mov_b64 exec, -1" :: "v"(vv2), "v"(vv3), "s"(KV_SRC(c)), "s"(mk20) : "memory")
  bf16x8 qa, ka0;
  u32x2 qbv, kb0;
  {
    const unsigned v8 = 1024u + (unsigned)r * 8u;
    asm volatile("global_load_dwordx4 %0, %4, %6\n\tglobal_load_dwordx2 %1, %5, %6\n\t"
                 "global_load_dwordx4 %2, %4, %7\n\tglobal_load_dwordx2 %3, %5, %7"
                 : "=&v"(qa), "=&v"(qbv), "=&v"(ka0), "=&v"(kb0) : "v"(vlin), "v"(v8), "s"(Qt), "s"(Kh) : "memory");
  }
  KV_M0(0); KV_P0(0); KV_P1(0); KV_P23(0);
#pragma unroll
  for (int sl = 0; sl < 2; ++sl) {
    lds_ptr tp = lds + sl * AT_SLOT + wid * AT_TILE;
    if (lane < 32) *(LDS_AS u32x2*)(tp + 1024 + 256 + lane * 8) = u32x2{0x00003f80u, 0u};
    if (lane < 48) {
      const int blk4 = (lane * 43) >> 9, dd = 20 + lane - blk4 * 12;
      const unsigned val = dd == 20 ? 0x3f803f80u : 0u;
      *(LDS_AS u32x4*)(tp + 1536 + blk4 * 512 + dd * 16) = u32x4{val, val, val, val};
    }
  }
  unsigned vo[5];
#pragma unroll
  for (int i = 0; i < 5; ++i) {
    const int p = i * 64 + lane, row = (p * 6554) >> 16;
    vo[i] = (unsigned)(row * 1280 + (p - row * 10) * 16 - (i & 3) * 1024);
  }
#define W_SRC(W, grp) ((W) + cbk * 160 + (size_t)((grp) * 256 + wid * 32) * 1280)
#define W_M0(reg, hi) SET_M0(lds0 + (unsigned)(KB_WR + (reg) * KB_GRP + wid * 5120 + (hi) * 4096))
#define W_P(W, i, grp) asm volatile("global_load_lds_dwordx4 %0, %1 offset:%2" :: "v"(vo[i]), "s"(W_SRC(W, grp)), "i"(((i) & 3) * 1024) : "memory")
  const char* hsrc = ws + WS_H + ((size_t)(rbk * 4 + mi) * 48 + kh) * 1024 + 4096;
#define H_LOAD(D, j, grp) asm volatile("global_load_dwordx4 %0, %1, %2 offset:%3" : "=v"(D[j]) : "v"(vlin), "s"(hsrc + (grp) * 8192), "i"((j) * 2048 - 4096) : "memory")
  const unsigned wra = lds0 + (unsigned)(KB_WR + (kh * 32 + 4 * ((gq & 1) + 4 * (gq >> 1)) + ((lane & 15) >> 2)) * 160 + (lane & 3) * 8);
  const unsigned wrb = wra + KB_GRP;

  unsigned oo[3];
#pragma unroll
  for (int g = 0; g < 3; ++g) {
    const int Gc = head * 5 + 2 * g + h, G8 = Gc & 7;
    oo[g] = (unsigned)((r >> 4) * (20 * 1024) + (r & 15) * 16 + (Gc >> 3) * 1024 + ((G8 >> 2) * 2 + (G8 & 1)) * 256 + ((G8 >> 1) & 1) * 8);
    PIN(oo[g]);
  }
  bf16x8 hA[4], hB[4];
  asm volatile("s_waitcnt vmcnt(4)" : "+v"(qa), "+v"(qbv), "+v"(ka0), "+v"(kb0) :: "memory");
  f32x16 o;
#pragma unroll
  for (int i = 0; i < 16; ++i) o[i] = 0.f;
  const f32x16 zero = o;
  if (h == 1) { qbv[0] = 0u; qbv[1] = 0u; kb0[0] = 0x00003f80u; kb0[1] = 0u; }
  const f32x16 s16 = MFMA32(ka0, qa, zero);
  f32x16 sA = MFMA32K8(__builtin_bit_cast(bf16x4, kb0), __builtin_bit_cast(bf16x4, qbv), s16);
  {
    float m = fmaxf(fmaxf(sA[0], sA[1]), sA[2]);
#pragma unroll
    for (int i = 3; i < 15; i += 2) m = fmaxf(fmaxf(m, sA[i]), sA[i + 1]);
    m = fmaxf(m, sA[15]);
    const auto mm = __builtin_amdgcn_permlane32_swap(__float_as_uint(m), __float_as_uint(m), false, false);
    m = fmaxf(__uint_as_float(mm[0]), __uint_as_float(mm[1]));
    if (h == 1) qbv[0] = cvtpk(-m, 0.f);
  }
  const bf16x4 qb4 = __builtin_bit_cast(bf16x4, qbv);
  sA = MFMA32K8(__builtin_bit_cast(bf16x4, kb0), qb4, s16);
  f32x16 sB;
  f32x4 acc[5];
#pragma unroll
  for (int j = 0; j < 5; ++j) acc[j] = f32x4{0.f, 0.f, 0.f, 0.f};
  WAIT_BAR(0);
  STAMP(1, 1);
  STAMPC(1, 14);
  STAMP2(2, 0);

  unsigned a16 = lds0 + vlin, a8 = lds0 + (unsigned)lane * 8u;
  int sdelta = AT_SLOT;
  bf16x8 kaA, v0A, v1A, kaB, v0B, v1B;
  bf16x4 kbA, kbB;
  bf16x4 wl[5], wu[5];
  u32x4 p0, p1 = {0u, 0u, 0u, 0u};
  v1B = __builtin_bit_cast(bf16x8, p1);
#define RD_KA(X, j) ka##X = *(const LDS_AS bf16x8*)(LP(a16) + (j) * AT_TILE)
#define RD_KB(X, j) kb##X = *(const LDS_AS bf16x4*)(LP(a8) + (j) * AT_TILE + 1024)
#define RD_V0(X, j) v0##X = *(const LDS_AS bf16x8*)(LP(a16) + (j) * AT_TILE + 1536)
#define RD_V1(X, j) v1##X = *(const LDS_AS bf16x8*)(LP(a16) + (j) * AT_TILE + 2560)
#define W_LO(WA, j, t) wl[t] = tr_read(LP(WA) + (j) * 10240 + (t) * 32)
#define W_HI(WA, j, t) wu[t] = tr_read(LP(WA) + (j) * 10240 + (t) * 32 + 1280)
#define G_MM(HS, j, t) acc[t] = MFMA16(cat8(wl[t], wu[t]), HS[j], acc[t])
#define EX2(S, i) do { S[i] = __builtin_amdgcn_exp2f(S[i]); S[i + 1] = __builtin_amdgcn_exp2f(S[i + 1]); } while (0)
#define MID() do { __builtin_amdgcn_sched_group_barrier(0x400, 2, 0); __builtin_amdgcn_sched_group_barrier(0x008, 1, 0); \
                    __builtin_amdgcn_sched_group_barrier(0x400, 2, 0); __builtin_amdgcn_sched_group_barrier(0x002, 2, 0); } while (0)
#define AT_STEP(SC, SN, X, Y, JN, PRE, WP, A0, A1, A2, A3, A4, A5, V0_, V1_, V2_, M1, M2, M3)            \
  do {                                                                                                   \
    PRE;                                                                                                 \
    asm volatile("" : "+v"(ka##X), "+v"(kb##X), "+v"(v0##X), "+v"(v1##X)); WP; SBAR();                   \
    EX2(SC, 0); PIN(SC); SBAR();                                                                         \
    o = MFMA32(v1##Y, __builtin_bit_cast(bf16x8, p1), o); SBAR();                                        \
    RD_KA(Y, JN); RD_KB(Y, JN); RD_V0(Y, JN); RD_V1(Y, JN); A0; A1; A2; A3; A4; A5; V0_; V1_; SBAR(); \
    M1; EX2(SC, 2); EX2(SC, 4); p0[0] = cvtpk(SC[0], SC[1]); p0[1] = cvtpk(SC[2], SC[3]); MID();         \
    asm volatile("" : "+v"(p0), "+v"(SC), "+v"(acc[0]), "+v"(acc[3])); SBAR();                           \
    SN = MFMA32(ka##X, qa, zero); SBAR();                                                                \
    M2; EX2(SC, 6); EX2(SC, 8); p0[2] = cvtpk(SC[4], SC[5]); p0[3] = cvtpk(SC[6], SC[7]); MID();         \
    asm volatile("" : "+v"(p0), "+v"(SC), "+v"(acc[1])); SBAR();                                         \
    SN = MFMA32K8(kb##X, qb4, SN); SBAR();                                                               \
    V2_; SBAR();                                                                                         \
    M3; EX2(SC, 10); EX2(SC, 12); p1[0] = cvtpk(SC[8], SC[9]); p1[1] = cvtpk(SC[10], SC[11]); MID();     \
    asm volatile("" : "+v"(p1), "+v"(SC), "+v"(acc[2]), "+v"(acc[4])); SBAR();                           \
    o = MFMA32(v0##X, __builtin_bit_cast(bf16x8, p0), o); SBAR();                                        \
    EX2(SC, 14); p1[2] = cvtpk(SC[12], SC[13]); p1[3] = cvtpk(SC[14], SC[15]); PIN(p1); SBAR();          \
  } while (0)
#define WP_E asm volatile("" : "+v"(wl[0]), "+v"(wu[0]), "+v"(wl[1]), "+v"(wu[1]), "+v"(wl[2]), "+v"(wu[2]))
#define WP_O asm volatile("" : "+v"(wl[3]), "+v"(wu[3]), "+v"(wl[4]), "+v"(wu[4]))
#define FLIP() do { a16 += (unsigned)sdelta; a8 += (unsigned)sdelta; sdelta = -sdelta; } while (0)
#define SYNC_H(HN) asm volatile("s_waitcnt vmcnt(3) lgkmcnt(0)\n\ts_barrier" : "+v"(HN[0]) :: "memory")
#define WAIT_H(HC, j, N) asm volatile("s_waitcnt vmcnt(" #N ")" : "+v"(HC[j]) :: "memory")
#define STEP_E(JN, PRE, WC, HC, j, VA, VB)                                                               \
  AT_STEP(sA, sB, A, B, JN, PRE, WP_E, W_LO(WC, j, 3), W_HI(WC, j, 3), W_LO(WC, j, 4), W_HI(WC, j, 4), NOP_, NOP_, NOP_, VA, VB, \
          G_MM(HC, j, 0), G_MM(HC, j, 1), G_MM(HC, j, 2))
#define STEP_O(JN, PRE, WA, jn, HC, j, VA, VB)                                                           \
  AT_STEP(sB, sA, B, A, JN, PRE, WP_O, W_LO(WA, jn, 0), W_HI(WA, jn, 0), W_LO(WA, jn, 1), W_HI(WA, jn, 1), W_LO(WA, jn, 2), \
          W_HI(WA, jn, 2), NOP_, VA, VB, G_MM(HC, j, 3), NOP_, G_MM(HC, j, 4))
#define CHUNK_G(c, WC, HC, WN, HN, RN, NW, NG, HG)                                                       \
  do {                                                                                                   \
    STEP_E(1, NOP_, WC, HC, 0, W_M0(RN, 0); W_P(NW, 0, NG), W_P(NW, 1, NG));                             \
    STEP_O(2, NOP_, WC, 1, HC, 0, W_P(NW, 2, NG), W_P(NW, 3, NG));                                       \
    STEP_E(3, WAIT_H(HC, 1, 6), WC, HC, 1, W_M0(RN, 1); W_P(NW, 4, NG), KV_M0((c) + 1); KV_P0((c) + 1)); \
    STEP_O(4, NOP_, WC, 2, HC, 1, KV_P1((c) + 1), KV_P23((c) + 1));                                      \
    STEP_E(5, WAIT_H(HC, 2, 10), WC, HC, 2, H_LOAD(HN, 0, HG), H_LOAD(HN, 1, HG));                       \
    STEP_O(6, NOP_, WC, 3, HC, 2, H_LOAD(HN, 2, HG), H_LOAD(HN, 3, HG));                                 \
    STEP_E(7, WAIT_H(HC, 3, 13), WC, HC, 3, NOP_, NOP_);                                                 \
    STEP_O(0, SYNC_H(HN); STAMP2(2, 1 + (c)); FLIP(), WN, 0, HC, 3, NOP_, NOP_);                         \
  } while (0)
#define PLAIN2_E(JN, VA, VB) AT_STEP(sA, sB, A, B, JN, NOP_, NOP_, NOP_, NOP_, NOP_, NOP_, NOP_, NOP_, NOP_, VA, VB, NOP_, NOP_, NOP_)
#define PLAIN2_O(JN, VA, VB) AT_STEP(sB, sA, B, A, JN, NOP_, NOP_, NOP_, NOP_, NOP_, NOP_, NOP_, NOP_, NOP_, VA, VB, NOP_, NOP_, NOP_)
#define PLAIN_E(JN, X0, X1, X2, X3) AT_STEP(sA, sB, A, B, JN, NOP_, NOP_, X0, X1, X2, X3, NOP_, NOP_, NOP_, NOP_, NOP_, NOP_, NOP_, NOP_)
#define PLAIN_O(JN, X0, X1, X2, X3) AT_STEP(sB, sA, B, A, JN, NOP_, NOP_, X0, X1, X2, X3, NOP_, NOP_, NOP_, NOP_, NOP_, NOP_, NOP_, NOP_)

  RD_KA(A, 0); RD_KB(A, 0); RD_V0(A, 0); RD_V1(A, 0);
  PLAIN2_E(1, W_M0(0, 0); W_P(w2, 0, 0), W_P(w2, 1, 0));
  PLAIN2_O(2, W_P(w2, 2, 0), W_P(w2, 3, 0));
  PLAIN2_E(3, W_M0(0, 1); W_P(w2, 4, 0), KV_M0(1); KV_P0(1));
  PLAIN2_O(4, KV_P1(1), KV_P23(1));
  PLAIN2_E(5, H_LOAD(hA, 0, 0), H_LOAD(hA, 1, 0));
  PLAIN2_O(6, H_LOAD(hA, 2, 0), H_LOAD(hA, 3, 0));
  PLAIN_E(7, NOP_, NOP_, NOP_, NOP_);
  AT_STEP(sB, sA, B, A, 0, SYNC_H(hA); STAMP2(2, 1); FLIP(), NOP_, W_LO(wra, 0, 0), W_HI(wra, 0, 0), W_LO(wra, 0, 1), W_HI(wra, 0, 1),
          W_LO(wra, 0, 2), W_HI(wra, 0, 2), NOP_, NOP_, NOP_, NOP_, NOP_, NOP_);
#pragma unroll 1
  for (int c = 1; c < 7; c += 2) {
    CHUNK_G(c, wra, hA, wrb, hB, 1, w2, c, c);
    const char* wn = c < 5 ? w2 : wo;
    CHUNK_G(c + 1, wrb, hB, wra, hA, 0, wn, (c < 5 ? c + 1 : 0), (c < 5 ? c + 1 : 5));
  }
  const int tok = rbk * 64 + mi * 16 + (lane & 15);
  const size_t xo = (size_t)tok * 1280 + (cbk * 80 + 4 * gq) * 2;
  LDS_AS f32x4* red = (LDS_AS f32x4*)(lds + KB_RED) + (mi * 5) * 64 + lane;
  u32x2 gv[5], xv[5];
  PLAIN_E(1, NOP_,
          _Pragma("unroll") for (int j = 0; j < 5; ++j) gv[j] = *(const u32x2*)(b2 + cbk * 80 + j * 16 + 4 * gq),
          _Pragma("unroll") for (int j = 0; j < 5; ++j) xv[j] = *(const u32x2*)(x + xo + j * 32),
          if (kh == 1) { red[0] = acc[0]; red[64] = acc[1]; red[128] = acc[2]; } else { red[192] = acc[3]; red[256] = acc[4]; });
  PLAIN_O(2, NOP_, NOP_, NOP_, NOP_);
  PLAIN_E(3, NOP_, NOP_, NOP_, NOP_);
  PLAIN_O(4, asm volatile("s_waitcnt vmcnt(0)" : "+v"(hA[1]), "+v"(hA[2]), "+v"(hA[3]) :: "memory");
          _Pragma("unroll") for (int j = 0; j < 5; ++j) { PIN(gv[j]); PIN(xv[j]); } W_M0(1, 0), W_P(wo, 0, 1), W_P(wo, 1, 1), W_P(wo, 2, 1));
  AT_STEP(sA, sB, A, B, 5, LBAR(0), NOP_, W_P(wo, 3, 1), W_M0(1, 1), W_P(wo, 4, 1), SET_M0(lds0 + (unsigned)(wid * 2560)),
          asm volatile("global_load_lds_dwordx4 %0, %3\n\tglobal_load_lds_dwordx4 %1, %3 offset:1024\n\t"
                       "s_mov_b32 exec_hi, 0\n\tglobal_load_lds_dwordx4 %2, %3 offset:2048\n\ts_mov_b32 exec_hi, -1"
                       :: "v"(vo[0]), "v"(vo[1]), "v"(vo[2]), "s"(wo + cbk * 160 + (size_t)(512 + wid * 16) * 1280) : "memory"),
          NOP_, NOP_, NOP_, NOP_, NOP_, NOP_, NOP_);
#define G_FIN(j)                                                                                          \
  do {                                                                                                    \
    const f32x4 tt = acc[j] + og[j] + f32x4{bf_lo(gv[j][0]), bf_hi(gv[j][0]), bf_lo(gv[j][1]), bf_hi(gv[j][1])}; \
    float t[4];                                                                                           \
    _Pragma("unroll") for (int e = 0; e < 4; ++e)                                                         \
      t[e] = tt[e] * __builtin_amdgcn_rcpf(1.0f + __builtin_amdgcn_exp2f(tt[e] * -1.4426950408889634f));  \
    gv[j] = u32x2{cvtpk(t[0], t[1]), cvtpk(t[2], t[3])};                                                  \
    PIN(gv[j]);                                                                                           \
  } while (0)
  {
    f32x4 og[5];
    if (kh == 0) { og[0] = red[0]; og[1] = red[64]; og[2] = red[128]; G_FIN(0); G_FIN(1); G_FIN(2); }
    else { og[3] = red[192]; og[4] = red[256]; G_FIN(3); G_FIN(4); }
  }
#undef G_FIN
  SBAR();
  PLAIN_O(6, NOP_, NOP_, NOP_, NOP_);
  PLAIN_E(7, NOP_, NOP_, NOP_, NOP_);
  PLAIN_O(7, NOP_, NOP_, NOP_, NOP_);
  o = MFMA32(v1B, __builtin_bit_cast(bf16x8, p1), o);

  STAMP(1, 3);
  STAMPC(1, 15);
  STAMP2(2, 8);
  float l = __uint_as_float(__builtin_amdgcn_permlane32_swap(__float_as_uint(o[8]), __float_as_uint(o[8]), false, false)[1]);
  float big = fmaxf(fmaxf(__builtin_fabsf(o[0]), __builtin_fabsf(o[1])), l);
#pragma unroll
  for (int i = 2; i < 12; i += 2) big = fmaxf(fmaxf(big, __builtin_fabsf(o[i])), __builtin_fabsf(o[i + 1]));
  bool unsafe = !(big < 1.0e38f) || !(l > 0.25f);
#pragma unroll
  for (int i = 0; i < 12; ++i) unsafe |= o[i] != o[i];
  if (__builtin_expect(__any(unsafe), 0)) {
    if (h == 1) { qbv[0] = 0u; }
    const bf16x4 qz = __builtin_bit_cast(bf16x4, qbv);
    float m = -3.0e38f;
#pragma unroll
    for (int i = 0; i < 16; ++i) o[i] = 0.f;
    const unsigned onev = r == 20 ? 0x3f803f80u : 0u;
#pragma unroll 1
    for (int t = 0; t < 64; ++t) {
      const bf16x8 ka = *(const bf16x8*)(Kh + t * 1280 + lane * 16);
      u32x2 kb = *(const u32x2*)(Kh + t * 1280 + 1024 + r * 8);
      u32x4 v0 = {onev, onev, onev, onev}, v1 = v0;
      if (r < 20) {
        v0 = *(const u32x4*)(Vh + t * 1280 + vv);
        v1 = *(const u32x4*)(Vh + t * 1280 + 640 + vv);
      }
      f32x16 s = MFMA32(ka, qa, zero);
      s = MFMA32K8(__builtin_bit_cast(bf16x4, kb), qz, s);
      float tm = s[0];
#pragma unroll
      for (int i = 1; i < 16; ++i) tm = fmaxf(tm, s[i]);
      tm = fmaxf(tm, __shfl_xor(tm, 32));
      const float mn = fmaxf(m, tm);
      const float al = __builtin_amdgcn_exp2f(m - mn);
      m = mn;
      u32x4 q0, q1;
#pragma unroll
      for (int i = 0; i < 16; ++i) { o[i] *= al; s[i] = __builtin_amdgcn_exp2f(s[i] - mn); }
#pragma unroll
      for (int e = 0; e < 4; ++e) { q0[e] = cvtpk(s[2 * e], s[2 * e + 1]); q1[e] = cvtpk(s[8 + 2 * e], s[9 + 2 * e]); }
      o = MFMA32(__builtin_bit_cast(bf16x8, v0), __builtin_bit_cast(bf16x8, q0), o);
      o = MFMA32(__builtin_bit_cast(bf16x8, v1), __builtin_bit_cast(bf16x8, q1), o);
    }
    l = __shfl(o[8], 32 + r);
  }
  {
    const float inv = __builtin_amdgcn_rcpf(l);
    const char* ob = ws + WS_O + (size_t)(qb * 16 + wid * 2) * (20 * 1024);
#pragma unroll
    for (int g = 0; g < 3; ++g)
      if (g < 2 || h == 0)
        st8s(ob, oo[g], u32x2{min(cvtpk(o[4 * g] * inv, o[4 * g + 1] * inv), 0xfffffffeu),
                              min(cvtpk(o[4 * g + 2] * inv, o[4 * g + 3] * inv), 0xfffffffeu)});
  }
  asm volatile("s_waitcnt vmcnt(3) lgkmcnt(0)\n\ts_barrier" ::: "memory");
  STAMP(1, 4);
  {
    const srd_t ro = make_srd(ws + WS_O, (unsigned)(WS_G - WS_O));
    const unsigned oa = (unsigned)(((rbk * 4 + mi) * 20 + kh) * 1024);
    bf16x8 wfa[5][5];
#pragma unroll
    for (int c = 0; c < 5; ++c)
#pragma unroll
      for (int t = 0; t < 5; ++t)
        wfa[c][t] = cat8(tr_read(LP(wra) + c * 10240 + t * 32), tr_read(LP(wra) + c * 10240 + t * 32 + 1280));
    SBAR();
#pragma unroll
    for (int j = 0; j < 5; ++j) acc[j] = f32x4{0.f, 0.f, 0.f, 0.f};
    STAMP(1, 6);
    __builtin_amdgcn_s_sleep(8);
    u32x4 af[10];
    for (unsigned spins = 0; spins < (1u << 15); ++spins) {
      asm volatile("" ::: "memory");
#pragma unroll
      for (int c = 0; c < 10; ++c) af[c] = bload16_dev(ro, vlin, oa + c * 2048u);
      unsigned mx = 0u;
#pragma unroll
      for (int c = 0; c < 10; ++c) mx = max(max(max(mx, af[c][0]), af[c][1]), max(af[c][2], af[c][3]));
      if (!__any(mx == 0xffffffffu)) break;
      __builtin_amdgcn_s_sleep(4);
    }
    STAMP(1, 7);
    SBAR();
    bf16x8 wfb[5][5];
#pragma unroll
    for (int c = 5; c < 10; ++c) {
      const unsigned wa = c < 8 ? wrb + (unsigned)((c - 4) * 10240) : wra - (unsigned)KB_WR + (unsigned)((c - 8) * 10240);
      if (c < 8) {
#pragma unroll
        for (int t = 0; t < 5; ++t) wfb[c - 5][t] = cat8(tr_read(LP(wa) + t * 32), tr_read(LP(wa) + t * 32 + 1280));
      }
    }
    SBAR();
#pragma unroll
    for (int c = 0; c < 5; ++c)
#pragma unroll
      for (int t = 0; t < 5; ++t) acc[t] = MFMA16(wfa[c][t], __builtin_bit_cast(bf16x8, af[c]), acc[t]);
#pragma unroll
    for (int c = 8; c < 10; ++c)
#pragma unroll
      for (int t = 0; t < 5; ++t)
        wfb[c - 5][t] = cat8(tr_read(LP(wra - (unsigned)KB_WR) + (c - 8) * 10240 + t * 32), tr_read(LP(wra - (unsigned)KB_WR) + (c - 8) * 10240 + t * 32 + 1280));
#pragma unroll
    for (int c = 5; c < 10; ++c)
#pragma unroll
      for (int t = 0; t < 5; ++t) acc[t] = MFMA16(wfb[c - 5][t], __builtin_bit_cast(bf16x8, af[c]), acc[t]);
  }
  if (kh == 1) { red[0] = acc[0]; red[64] = acc[1]; red[128] = acc[2]; } else { red[192] = acc[3]; red[256] = acc[4]; }
  LBAR(0);
#define O_FIN(j)                                                                                          \
  do {                                                                                                    \
    const f32x4 sv = acc[j] + red[(j) * 64];                                                              \
    const float r0 = bf_lo(xv[j][0]) + sv[0] * bf_lo(gv[j][0]), r1 = bf_hi(xv[j][0]) + sv[1] * bf_hi(gv[j][0]); \
    const float r2 = bf_lo(xv[j][1]) + sv[2] * bf_lo(gv[j][1]), r3 = bf_hi(xv[j][1]) + sv[3] * bf_hi(gv[j][1]); \
    *(u32x2*)(out + xo + (j) * 32) = u32x2{cvtpk(r0, r1), cvtpk(r2, r3)};                                 \
  } while (0)
  if (kh == 0) { O_FIN(0); O_FIN(1); O_FIN(2); } else { O_FIN(3); O_FIN(4); }
#undef O_FIN
  STAMP(1, 8);
}

extern "C" void kernel_launch(void* const* d_in, const int* in_sizes, int n_in, void* d_out, int out_size, void* d_ws,
                              size_t ws_size, hipStream_t stream) {
  (void)in_sizes; (void)n_in; (void)out_size; (void)ws_size;
  const char* x = (const char*)d_in[0];
  const char* wqkv = (const char*)d_in[1];
  const char* wo = (const char*)d_in[2];
  const char* w1 = (const char*)d_in[4];
  const unsigned short* b1 = (const unsigned short*)d_in[5];
  const char* w2 = (const char*)d_in[6];
  const unsigned short* b2 = (const unsigned short*)d_in[7];
  char* ws = (char*)d_ws;
  (void)hipFuncSetAttribute((const void*)ka_proj, hipFuncAttributeMaxDynamicSharedMemorySize, KA_LDS);
  (void)hipFuncSetAttribute((const void*)kb_main, hipFuncAttributeMaxDynamicSharedMemorySize, KB_LDS);
  ka_proj<<<256, 512, KA_LDS, stream>>>(x, wqkv, w1, b1, w2, wo, ws);
  kb_main<<<256, 512, KB_LDS, stream>>>(x, w2, b2, wo, ws, (char*)d_out);
}
```

```cpp
#include <hip/hip_runtime.h>
#include <hip/hip_bf16.h>
#include <stdint.h>

typedef __attribute__((ext_vector_type(8))) short bf16x8;
typedef __attribute__((ext_vector_type(4))) short bf16x4;
typedef __attribute__((ext_vector_type(16))) float f32x16;
typedef __attribute__((ext_vector_type(4))) float f32x4;
typedef __attribute__((ext_vector_type(2))) float f32x2;
typedef __attribute__((ext_vector_type(4))) unsigned u32x4;
typedef __attribute__((ext_vector_type(2))) unsigned u32x2;
typedef __attribute__((ext_vector_type(2))) __bf16 bf2;
typedef __attribute__((address_space(3))) char* lds_ptr;
#define LDS_AS __attribute__((address_space(3)))
#define DI __device__ __forceinline__

#define MFMA32(a, b, c) __builtin_amdgcn_mfma_f32_32x32x16_bf16((a), (b), (c), 0, 0, 0)
#define MFMA32K8(a, b, c) __builtin_amdgcn_mfma_f32_32x32x8bf16_1k((a), (b), (c), 0, 0, 0)
#define MFMA16(a, b, c) __builtin_amdgcn_mfma_f32_16x16x32_bf16((a), (b), (c), 0, 0, 0)
#define SBAR() __builtin_amdgcn_sched_barrier(0)
#define PIN(x) asm volatile("" : "+v"(x))
#define WAIT_BAR(N) asm volatile("s_waitcnt vmcnt(" #N ") lgkmcnt(0)\n\ts_barrier" ::: "memory")

constexpr int D_ = 640, NH = 32;
constexpr float LN_EPS = 1e-5f;
constexpr float QSCALE = 0.22360679774997896f * 1.4426950408889634f;

constexpr size_t WS_Q = 0;
constexpr size_t WS_K = WS_Q + (size_t)NH * 64 * 1280;
constexpr size_t WS_V = WS_K + (size_t)NH * 64 * 1280;
constexpr size_t WS_H = WS_V + (size_t)NH * 64 * 1280;
constexpr size_t WS_O = WS_H + (size_t)128 * 48 * 1024;
constexpr size_t WS_G = WS_O + (size_t)128 * 20 * 1024;

#define STAMP(kern, i) do { } while (0)
#define STAMPC(kern, i) do { } while (0)
#define STAMP2(kern, i) do { } while (0)

DI void st16s(const void* sb, unsigned off, u32x4 v) { asm volatile("global_store_dwordx4 %0, %1, %2 sc1\n\ts_nop 1" :: "v"(off), "v"(v), "s"(sb) : "memory"); }
DI void st8s(const void* sb, unsigned off, u32x2 v) { asm volatile("global_store_dwordx2 %0, %1, %2 sc1" :: "v"(off), "v"(v), "s"(sb) : "memory"); }
DI unsigned cvtpk(float lo, float hi) {
  f32x2 f = {lo, hi};
  return __builtin_bit_cast(unsigned, __builtin_convertvector(f, bf2));
}
DI float bf_lo(unsigned u) { return __uint_as_float(u << 16); }
DI float bf_hi(unsigned u) { return __uint_as_float(u & 0xffff0000u); }
typedef short v4i16_t __attribute__((ext_vector_type(4)));
DI bf16x4 tr_read(lds_ptr p) {
  return __builtin_bit_cast(bf16x4, __builtin_amdgcn_ds_read_tr16_b64_v4i16((LDS_AS v4i16_t*)p));
}
DI bf16x8 cat8(bf16x4 a, bf16x4 b) { return __builtin_shufflevector(a, b, 0, 1, 2, 3, 4, 5, 6, 7); }

constexpr int KA_BUF = 45056;
constexpr int KA_STAT = 3 * KA_BUF;
constexpr int KA_SCS = KA_STAT + 2048, KA_SB1 = KA_SCS + 3584, KA_SMR = KA_SB1 + 384;
constexpr int KA_LDS = KA_SMR + 2048;
#define LBAR(N) asm volatile("s_waitcnt lgkmcnt(" #N ")\n\ts_barrier" ::: "memory")
#define LP(a) ((lds_ptr)(uintptr_t)(a))
#define SET_M0(v) asm volatile("s_mov_b32 m0, %0\n\ts_nop 0" :: "s"(v) : "memory")
#define NOP_ do { } while (0)
#define MFMA4(a, b, c) __builtin_amdgcn_mfma_f32_4x4x4bf16_1k((a), (b), (c), 0, 0, 0)

typedef __amdgpu_buffer_rsrc_t srd_t;
DI srd_t make_srd(const void* p, unsigned bytes) { return __builtin_amdgcn_make_buffer_rsrc((void*)p, (short)0, (int)bytes, 0x00020000); }
DI float x32_add(float v) {
  const auto rr = __builtin_amdgcn_permlane32_swap(__float_as_uint(v), __float_as_uint(v), false, false);
  return __uint_as_float(rr[0]) + __uint_as_float(rr[1]);
}
DI bf16x4 lo4(bf16x8 v) { return __builtin_shufflevector(v, v, 0, 1, 2, 3); }
DI bf16x4 hi4(bf16x8 v) { return __builtin_shufflevector(v, v, 4, 5, 6, 7); }

template <int TYPE, int nh>
DI void ka_body(lds_ptr lds, const char* __restrict__ x, const char* __restrict__ wqkv, const char* __restrict__ wh1,
                const unsigned short* __restrict__ b1, const char* __restrict__ w2, const char* __restrict__ wo,
                const int n0, const int f0, const int rb, const int cb, char* __restrict__ ws) {
  constexpr int NHT = nh ? 1 : 2;
  const int tid = threadIdx.x, lane = tid & 63;
  const int w = __builtin_amdgcn_readfirstlane(tid >> 6);
  const int mi = w & 3;
  const int r = lane & 31, h = lane >> 5;
  const unsigned lds0 = (unsigned)(uintptr_t)lds;
  STAMP(0, 0);
  const unsigned vx0 = (unsigned)((lane >> 3) * 1280 + (((lane & 7) ^ (lane >> 4)) << 4));
  const unsigned vx1 = (unsigned)((8 + (lane >> 3)) * 1280 + (((lane & 7) ^ (4 + (lane >> 4))) << 4) - 1024);
  const unsigned vw = (unsigned)((lane >> 3) * 3840 + ((((((lane >> 2) ^ (lane >> 4)) & 1) << 2) | (lane & 3)) << 4));
  const int p0 = w * 96 + lane, p1 = w * 96 + 64 + r;
  const int r0 = (p0 * 5462) >> 16, r1 = (p1 * 5462) >> 16;
  const unsigned vh0 = (unsigned)(r0 * 3072 + (p0 - r0 * 12) * 16), vh1 = (unsigned)(r1 * 3072 + (p1 - r1 * 12) * 16 - 1024);
  const char* xs = x + (size_t)(rb * 128 + 16 * w) * 1280;
  const char* wsrc = wqkv + ((size_t)(8 * w) * 1920 + n0) * 2;
  const char* hsrc = wh1 + f0 * 2;
  const unsigned mx = lds0 + (unsigned)w * 2048u, mw = lds0 + 16384u + (unsigned)w * 1024u, mh = lds0 + 32768u + (unsigned)w * 1536u;
#define KA_DX(c, buf) do { SET_M0(mx + (unsigned)(buf)); asm volatile("global_load_lds_dwordx4 %0, %2\n\tglobal_load_lds_dwordx4 %1, %2 offset:1024" :: "v"(vx0), "v"(vx1), "s"(xs + (c) * 128) : "memory"); } while (0)
#define KA_DW(c, buf, hf) do { SET_M0(mw + (unsigned)(buf) + (hf) * 8192u); asm volatile("global_load_lds_dwordx4 %0, %1" :: "v"(vw), "s"(wsrc + (size_t)(c) * (64 * 3840) + (hf) * 128) : "memory"); } while (0)
#define KA_DH(c, buf) do { SET_M0(mh + (unsigned)(buf)); asm volatile("global_load_lds_dwordx4 %0, %2\n\ts_mov_b32 exec_hi, 0\n\tglobal_load_lds_dwordx4 %1, %2 offset:1024\n\ts_mov_b32 exec_hi, -1" :: "v"(vh0), "v"(vh1), "s"(hsrc + (size_t)(c) * (64 * 3072)) : "memory"); } while (0)
#define KA_ISSUE(c, buf) do { KA_DX(c, buf); KA_DW(c, buf, 0); KA_DW(c, buf, 1); KA_DH(c, buf); } while (0)
  unsigned braw = 0, warm = 0;
  if (nh == 0 && tid < 96) braw = b1[f0 + tid];
  SBAR();
  KA_ISSUE(0, 0);
  KA_ISSUE(1, KA_BUF);

  unsigned xa[4], wa0[4], ha[4];
  {
    const unsigned wb = lds0 + (unsigned)(16384 + nh * 8192 + (8 * h + ((lane & 15) >> 2)) * 128 + 64 * ((lane >> 3) & 1) +
                                          (16 * ((lane >> 4) & 1) + 4 * (lane & 3)) * 2);
    const unsigned hb = lds0 + (unsigned)(32768 + (8 * h + ((lane & 15) >> 2)) * 192 + nh * 128 + (16 * ((lane >> 4) & 1) + 4 * (lane & 3)) * 2);
#pragma unroll
    for (int i = 0; i < 4; ++i) {
      const int ks = i ^ mi;
      xa[i] = lds0 + (unsigned)((mi * 32 + r) * 128 + (((2 * ks + h) ^ ((r >> 1) & 7)) << 4));
      wa0[i] = wb + (unsigned)(ks * 2048);
      ha[i] = hb + (unsigned)(ks * 3072);
    }
  }
  const int tq = rb * 4 + mi;
  const bool isq = n0 < 640;
  unsigned so[TYPE == 1 ? 2 : 8];
  if (TYPE == 1) {
#pragma unroll
    for (int j = 0; j < 2; ++j) {
      const int nn = n0 - 1280 + nh * 64 + j * 32 + r;
      const int head = (nn * 3277) >> 16;
      so[j] = (unsigned)(head * (64 * 1280) + (h * 20 + nn - head * 20) * 16);
      PIN(so[j]);
    }
  } else {
#pragma unroll
    for (int j = 0; j < 8; ++j) {
      const int G = (((isq ? n0 : n0 - 640) + nh * 64) >> 2) + h + 2 * j;
      const int head = (G * 205) >> 10;
      const int dg = G - head * 5;
      so[j] = (unsigned)(head * (64 * 1280) + (dg < 4 ? (dg >> 1) * 512 + r * 16 + (dg & 1) * 8 : 1024 + r * 8));
      PIN(so[j]);
    }
  }
  f32x16 acc[2], ach[NHT];
  f32x4 cq[2], chh[NHT], rsum = {0.f, 0.f, 0.f, 0.f}, rsq = rsum;
#pragma unroll
  for (int i = 0; i < 16; ++i) {
    acc[0][i] = 0.f; acc[1][i] = 0.f;
#pragma unroll
    for (int j = 0; j < NHT; ++j) ach[j][i] = 0.f;
  }
  cq[0] = rsum; cq[1] = rsum;
#pragma unroll
  for (int j = 0; j < NHT; ++j) chh[j] = rsum;
  bf16x8 sel;
  {
    const short sv = (lane & 15) == ((lane >> 4) & 1) ? (short)0x3f80 : (short)0;
    sel = bf16x8{sv, sv, sv, sv, sv, sv, sv, sv};
    PIN(sel);
  }
  bf16x8 xf[3], w0[3], w1[3], wh[3][NHT];
  unsigned wa1[4];
#pragma unroll
  for (int i = 0; i < 4; ++i) wa1[i] = wa0[i] ^ 64u;
#define KA_READ(B, i)                                                                                           \
  do {                                                                                                          \
    constexpr int s_ = ((B) + (i)) % 3;                                                                         \
    xf[s_] = *(const LDS_AS bf16x8*)(LP(xa[i]) + BUFOFF(B));                                                    \
    w0[s_] = cat8(tr_read(LP(wa0[i]) + BUFOFF(B)), tr_read(LP(wa0[i]) + BUFOFF(B) + 512));                      \
    w1[s_] = cat8(tr_read(LP(wa1[i]) + BUFOFF(B)), tr_read(LP(wa1[i]) + BUFOFF(B) + 512));                      \
    _Pragma("unroll") for (int j_ = 0; j_ < NHT; ++j_)                                                          \
      wh[s_][j_] = cat8(tr_read(LP(ha[i]) + BUFOFF(B) + j_ * 64), tr_read(LP(ha[i]) + BUFOFF(B) + j_ * 64 + 768)); \
  } while (0)
#define SGB(m, n) __builtin_amdgcn_sched_group_barrier((m), (n), 0)
#define KA_STEP(B, i, RD) KA_STEPX(B, i, RD, true)
#define KA_STEPX(B, i, RD, ROWS)                                                                                \
  do {                                                                                                          \
    constexpr int b_ = ((B) + (i)) % 3;                                                                         \
    if constexpr (NHT == 2) asm volatile("" : "+v"(xf[b_]), "+v"(w0[b_]), "+v"(w1[b_]), "+v"(wh[b_][0]), "+v"(wh[b_][NHT - 1])); \
    else asm volatile("" : "+v"(xf[b_]), "+v"(w0[b_]), "+v"(w1[b_]), "+v"(wh[b_][0]));                          \
    SBAR();                                                                                                     \
    RD;                                                                                                         \
    if (TYPE == 1) { acc[0] = MFMA32(xf[b_], w0[b_], acc[0]); acc[1] = MFMA32(xf[b_], w1[b_], acc[1]); }        \
    else { acc[0] = MFMA32(w0[b_], xf[b_], acc[0]); acc[1] = MFMA32(w1[b_], xf[b_], acc[1]); }                  \
    _Pragma("unroll") for (int j_ = 0; j_ < NHT; ++j_) ach[j_] = MFMA32(wh[b_][j_], xf[b_], ach[j_]);           \
    SGB(0x8, 1); SGB(0x100, 3); SGB(0x8, 1); SGB(0x100, 2); SGB(0x8, 1); SGB(0x100, 2);                         \
    if (NHT == 2) { SGB(0x8, 1); SGB(0x100, 2); }                                                               \
    if constexpr (NHT == 2) asm volatile("" : "+v"(acc[0]), "+v"(acc[1]), "+v"(ach[0]), "+v"(ach[NHT - 1]));    \
    else asm volatile("" : "+v"(acc[0]), "+v"(acc[1]), "+v"(ach[0]));                                           \
    SBAR();                                                                                                     \
    if ((i) == 0) {                                                                              \
      cq[0] = MFMA16(w0[b_], sel, cq[0]); cq[1] = MFMA16(w1[b_], sel, cq[1]);                                   \
      _Pragma("unroll") for (int j_ = 0; j_ < NHT; ++j_) chh[j_] = MFMA16(wh[b_][j_], sel, chh[j_]);            \
      if constexpr (NHT == 2) asm volatile("" : "+v"(cq[0]), "+v"(cq[1]), "+v"(chh[0]), "+v"(chh[NHT - 1]));    \
      else asm volatile("" : "+v"(cq[0]), "+v"(cq[1]), "+v"(chh[0]));                                           \
    }                                                                                                           \
    if ((ROWS) && (nh ? (i) < 2 : (i) >= 2)) {                                                   \
      rsq = MFMA4(lo4(xf[b_]), lo4(xf[b_]), rsq); rsum = MFMA16(xf[b_], sel, rsum);                             \
      rsq2 = MFMA4(hi4(xf[b_]), hi4(xf[b_]), rsq2);                                                             \
      asm volatile("" : "+v"(rsum), "+v"(rsq), "+v"(rsq2));                                                     \
    }                                                                                                           \
    SBAR();                                                                                                     \
  } while (0)
#define BUFOFF(B) ((B) * KA_BUF)
#define KA_CHUNK(c, B, BN, WAITN, PREV, MORE)                                                                   \
  do {                                                                                                          \
    KA_STEP(B, 0, KA_READ(B, 2));                                                                               \
    if ((PREV)) { KA_DH((c) + 2, BUFOFF(((B) + 2) % 3)); SBAR(); }                               \
    KA_STEP(B, 1, KA_READ(B, 3));                                                                               \
    WAIT_BAR(WAITN);                                                                                            \
    KA_STEP(B, 2, KA_READ(BN, 0));                                                                              \
    if ((MORE)) { KA_DX((c) + 3, BUFOFF(B)); SBAR(); }                                           \
    KA_STEP(B, 3, KA_READ(BN, 1));                                                                              \
    if ((MORE)) { KA_DW((c) + 3, BUFOFF(B), 0); KA_DW((c) + 3, BUFOFF(B), 1); SBAR(); }          \
  } while (0)
  f32x4 rsq2 = rsum;
  WAIT_BAR(6);
  STAMP(0, 1);
  STAMPC(0, 14);
  KA_READ(0, 0);
  KA_READ(0, 1);
  KA_STEP(0, 0, KA_READ(0, 2));
  KA_DX(2, BUFOFF(2)); KA_DW(2, BUFOFF(2), 0); KA_DW(2, BUFOFF(2), 1); SBAR();
  KA_STEP(0, 1, KA_READ(0, 3));
  KA_DH(2, BUFOFF(2)); SBAR();
  WAIT_BAR(6);
  KA_STEP(0, 2, KA_READ(1, 0));
  KA_DX(3, BUFOFF(0)); SBAR();
  KA_STEP(0, 3, KA_READ(1, 1));
  KA_DW(3, BUFOFF(0), 0); KA_DW(3, BUFOFF(0), 1); SBAR();
#pragma unroll 1
  for (int c = 1; c < 7; c += 3) {
    KA_CHUNK(c, 1, 2, 6, true, true);
    KA_CHUNK(c + 1, 2, 0, 6, true, true);
    KA_CHUNK(c + 2, 0, 1, 6, true, true);
  }
  KA_CHUNK(7, 1, 2, 6, true, false);
  KA_CHUNK(8, 2, 0, 0, false, false);
  KA_STEP(0, 0, KA_READ(0, 2));
  KA_STEP(0, 1, KA_READ(0, 3));
  if (nh == 0) {
    asm volatile("" : "+v"(xf[2]), "+v"(xf[0]));
    rsq = MFMA4(lo4(xf[2]), lo4(xf[2]), rsq); rsum = MFMA16(xf[2], sel, rsum); rsq2 = MFMA4(hi4(xf[2]), hi4(xf[2]), rsq2);
    rsum = MFMA16(xf[0], sel, rsum); rsq = MFMA4(lo4(xf[0]), lo4(xf[0]), rsq); rsq2 = MFMA4(hi4(xf[0]), hi4(xf[0]), rsq2);
  }
  asm volatile("s_nop 7" : "+v"(rsum), "+v"(rsq), "+v"(rsq2));
  rsq += rsq2;
  STAMP(0, 3);
  STAMPC(0, 15);
  LDS_AS float* st_r = (LDS_AS float*)(lds + KA_STAT);
  LDS_AS float* st_cs = (LDS_AS float*)(lds + KA_SCS);
  LDS_AS float* st_b = (LDS_AS float*)(lds + KA_SB1);
  {
    const int d = lane & 3;
    const float sq = x32_add(d == 0 ? rsq[0] : d == 1 ? rsq[1] : d == 2 ? rsq[2] : rsq[3]);
    if (h == 0) st_r[256 + nh * 128 + mi * 32 + r] = sq;
    if ((lane & 15) < 2) {
      const int q4 = (lane & 1) * 16 + (lane >> 4) * 4;
      *(LDS_AS f32x4*)(st_r + nh * 128 + mi * 32 + q4) = rsum;
      *(LDS_AS f32x4*)(st_cs + mi * 224 + nh * 64 + q4) = cq[0];
      *(LDS_AS f32x4*)(st_cs + mi * 224 + nh * 64 + 32 + q4) = cq[1];
#pragma unroll
      for (int j = 0; j < NHT; ++j) *(LDS_AS f32x4*)(st_cs + mi * 224 + 128 + nh * 64 + j * 32 + q4) = chh[j];
    }
    if (nh == 0 && tid < 96) st_b[tid] = __uint_as_float(braw << 16);
  }
  SBAR();
  KA_STEPX(0, 2, NOP_, false);
  LBAR(0);
  float cpart = 0.f;
  if (tid < 224) cpart = (st_cs[tid] + st_cs[224 + tid]) + (st_cs[448 + tid] + st_cs[672 + tid]);
  const float sx_ = st_r[mi * 32 + r] + st_r[128 + mi * 32 + r], sq_ = st_r[256 + mi * 32 + r] + st_r[384 + mi * 32 + r];
  SBAR();
  KA_STEPX(0, 3, NOP_, false);
#undef KA_READ
#undef KA_STEP
#undef KA_STEPX
#undef KA_CHUNK
#undef KA_ISSUE
  if (tid < 224) st_cs[tid] = cpart;
  {
    const char* ob = ws + WS_O + (size_t)blockIdx.x * 10240;
    const u32x4 ff = {0xffffffffu, 0xffffffffu, 0xffffffffu, 0xffffffffu};
    st16s(ob, (unsigned)tid * 16u, ff);
    if (w < 2) st16s(ob + 8192, (unsigned)tid * 16u, ff);
  }
  if (nh == 1) {
    const int l0_ = blockIdx.x * 85 + (tid - 256 < 85 ? tid - 256 : 0);
    const char* p = l0_ < 15360 ? w2 + (size_t)l0_ * 128 : wo + (size_t)(l0_ - 15360) * 128;
    if (w < 6) asm volatile("global_load_dword %0, %1, off" : "=v"(warm) : "v"(p) : "memory");
  }
  const float mu = sx_ * (1.0f / D_);
  const float var = fmaxf(sq_ * (1.0f / D_) - mu * mu, 0.f);
  const float rs = __builtin_amdgcn_rsqf(var + LN_EPS);
  LDS_AS f32x2* st_w = (LDS_AS f32x2*)(lds + KA_SMR) + w * 32;
  if (TYPE == 1 && h == 0) st_w[r] = f32x2{mu * rs, rs};
  LBAR(0);
  STAMP(0, 5);
  if (cb < 15) {
    if (TYPE == 1) {
      char* dst = ws + WS_V + (size_t)tq * 1280;
      const float cs0 = st_cs[nh * 64 + r], cs1 = st_cs[nh * 64 + 32 + r];
#pragma unroll
      for (int s = 0; s < 2; ++s) {
        f32x2 mr[8];
#pragma unroll
        for (int e = 0; e < 8; ++e) mr[e] = st_w[16 * s + 8 * (e >> 2) + 4 * h + (e & 3)];
        u32x4 o0, o1;
#pragma unroll
        for (int e = 0; e < 4; ++e) {
          const int i = 8 * s + 2 * e;
          o0[e] = cvtpk(acc[0][i] * mr[2 * e][1] - mr[2 * e][0] * cs0, acc[0][i + 1] * mr[2 * e + 1][1] - mr[2 * e + 1][0] * cs0);
          o1[e] = cvtpk(acc[1][i] * mr[2 * e][1] - mr[2 * e][0] * cs1, acc[1][i + 1] * mr[2 * e + 1][1] - mr[2 * e + 1][0] * cs1);
        }
        st16s(dst + s * 640, so[0], o0);
        st16s(dst + s * 640, so[1], o1);
      }
    } else {
      const float sc = isq ? rs * QSCALE : rs, ms = mu * sc;
      char* base = ws + (isq ? WS_Q : WS_K) + (size_t)tq * 1280;
#pragma unroll
      for (int j = 0; j < 2; ++j)
#pragma unroll
        for (int g = 0; g < 4; ++g) {
          const f32x4 cs = *(const LDS_AS f32x4*)(st_cs + nh * 64 + j * 32 + 8 * g + 4 * h);
          const u32x2 o = {cvtpk(acc[j][4 * g] * sc - ms * cs[0], acc[j][4 * g + 1] * sc - ms * cs[1]),
                           cvtpk(acc[j][4 * g + 2] * sc - ms * cs[2], acc[j][4 * g + 3] * sc - ms * cs[3])};
          st8s(base, so[j * 4 + g], o);
        }
    }
  }
  {
    const float sc = rs * __builtin_amdgcn_rsqf(var * rs * rs + LN_EPS), ms = mu * sc;
    constexpr float GK1 = -2.0f * 1.4426950408889634f * 0.7978845608028654f, GK2 = GK1 * 0.044715f;
    const char* dst = ws + WS_H + ((size_t)(rb * 8 + mi * 2) * 48 + (f0 >> 5) + nh * 2) * 1024;
    const unsigned ho = (unsigned)((r >> 4) * (48 * 1024) + (h * 16 + (r & 15)) * 16);
    const f32x2 sc2 = {sc, sc}, ms2 = {ms, ms}, k1 = {GK1, GK1}, k2 = {GK2, GK2}, one2 = {1.0f, 1.0f};
#pragma unroll
    for (int j = 0; j < NHT; ++j)
#pragma unroll
      for (int u = 0; u < 2; ++u) {
        u32x4 o;
#pragma unroll
        for (int gg = 0; gg < 2; ++gg) {
          const int g = 2 * u + gg;
          const f32x4 cs = *(const LDS_AS f32x4*)(st_cs + 128 + nh * 64 + j * 32 + 8 * g + 4 * h);
          const f32x4 bb = *(const LDS_AS f32x4*)(st_b + nh * 64 + j * 32 + 8 * g + 4 * h);
#pragma unroll
          for (int e = 0; e < 2; ++e) {
            const f32x2 a2 = {ach[j][4 * g + 2 * e], ach[j][4 * g + 2 * e + 1]};
            const f32x2 c2 = {cs[2 * e], cs[2 * e + 1]}, b2_ = {bb[2 * e], bb[2 * e + 1]};
            const f32x2 t = a2 * sc2 + (b2_ - ms2 * c2);
            const f32x2 arg = t * ((t * t) * k2 + k1);
            const f32x2 den = f32x2{__builtin_amdgcn_exp2f(arg[0]), __builtin_amdgcn_exp2f(arg[1])} + one2;
            const f32x2 v = t * f32x2{__builtin_amdgcn_rcpf(den[0]), __builtin_amdgcn_rcpf(den[1])};
            o[2 * gg + e] = cvtpk(v[0], v[1]);
          }
        }
        st16s(dst + j * 1024 + u * 512, ho, o);
      }
  }
  STAMP(0, 4);
  asm volatile("s_waitcnt vmcnt(0)" : "+v"(warm) :: "memory");
}

__global__ __launch_bounds__(512, 2) void ka_proj(const char* __restrict__ x, const char* __restrict__ wqkv,
                                                  const char* __restrict__ w1, const unsigned short* __restrict__ b1,
                                                  const char* __restrict__ w2, const char* __restrict__ wo,
                                                  char* __restrict__ ws) {
  extern __shared__ __attribute__((aligned(16))) char smem[];
  lds_ptr lds = (lds_ptr)smem;
  const int bid = blockIdx.x, xg = bid & 7, idx = bid >> 3;
  const int rb = (xg & 1) * 8 + (idx & 7), cb = (xg >> 1) * 4 + (idx >> 3);
  const int n0 = (cb < 15 ? cb : 14) * 128, f0 = cb * 96;
  const int nh = __builtin_amdgcn_readfirstlane(threadIdx.x >> 8);
  if (cb < 10) {
    if (nh) ka_body<0, 1>(lds, x, wqkv, w1, b1, w2, wo, n0, f0, rb, cb, ws);
    else ka_body<0, 0>(lds, x, wqkv, w1, b1, w2, wo, n0, f0, rb, cb, ws);
  } else {
    if (nh) ka_body<1, 1>(lds, x, wqkv, w1, b1, w2, wo, n0, f0, rb, cb, ws);
    else ka_body<1, 0>(lds, x, wqkv, w1, b1, w2, wo, n0, f0, rb, cb, ws);
  }
}

DI u32x4 bload16_dev(srd_t r, unsigned voff, unsigned soff) { return __builtin_amdgcn_raw_buffer_load_b128(r, (int)voff, (int)soff, 16); }

constexpr int AT_TILE = 3584;
constexpr int AT_SLOT = 28672;
constexpr int KB_WR = 2 * AT_SLOT;
constexpr int KB_GRP = 40960;
constexpr int KB_RED = KB_WR + 2 * KB_GRP;
constexpr int KB_LDS = KB_RED + 20480;

__global__ __launch_bounds__(512, 2) void kb_main(const char* __restrict__ x, const char* __restrict__ w2,
                                                  const unsigned short* __restrict__ b2, const char* __restrict__ wo,
                                                  char* __restrict__ ws, char* __restrict__ out) {
  extern __shared__ __attribute__((aligned(16))) char smem[];
  lds_ptr lds = (lds_ptr)smem;
  const int tid = threadIdx.x, lane = tid & 63;
  const int wid = __builtin_amdgcn_readfirstlane(tid >> 6);
  const int r = lane & 31, h = lane >> 5;
  const int xg = blockIdx.x & 7, idx = blockIdx.x >> 3;
  const int head = xg * 4 + (idx >> 3), qb = idx & 7;
  const int rbk = xg * 4 + (idx & 3), cbk = idx >> 2;
  const int mi = wid & 3, kh = wid >> 2, gq = lane >> 4;
  const unsigned lds0 = (unsigned)(uintptr_t)lds;
  const char* Kh = ws + WS_K + (size_t)head * (64 * 1280);
  const char* Vh = ws + WS_V + (size_t)head * (64 * 1280);
  const char* Qt = ws + WS_Q + (size_t)(head * 64 + qb * 8 + wid) * 1280;
  STAMP(1, 0);

  const unsigned vlin = (unsigned)lane * 16u;
  const unsigned vv = (unsigned)(h * 20 + r) * 16u;
  const unsigned vv2 = vv + (unsigned)(WS_V - WS_K) - 1280u - 1536u, vv3 = vv2 + 640u - 1024u;
  const unsigned long long mk16 = 0xffffull, mk20 = 0x000fffff000fffffull;
#define KV_M0(c) SET_M0(lds0 + (unsigned)(((c) & 1) * AT_SLOT + wid * AT_TILE))
#define KV_SRC(c) (Kh + (size_t)((c) * 8 + wid + 1) * 1280)
#define KV_P0(c) asm volatile("global_load_lds_dwordx4 %0, %1" :: "v"(vlin), "s"(KV_SRC(c)) : "memory")
#define KV_P1(c) asm volatile("s_mov_b64 exec, %2\n\tglobal_load_lds_dwordx4 %0, %1 offset:1024\n\ts_mov_b64 exec, -1" :: "v"(vlin), "s"(KV_SRC(c)), "s"(mk16) : "memory")
#define KV_P23(c) asm volatile("s_mov_b64 exec, %3\n\tglobal_load_lds_dwordx4 %0, %2 offset:1536\n\tglobal_load_lds_dwordx4 %1, %2 offset:2560\n\ts_mov_b64 exec, -1" :: "v"(vv2), "v"(vv3), "s"(KV_SRC(c)), "s"(mk20) : "memory")
  bf16x8 qa, ka0;
  u32x2 qbv, kb0;
  {
    const unsigned v8 = 1024u + (unsigned)r * 8u;
    asm volatile("global_load_dwordx4 %0, %4, %6\n\tglobal_load_dwordx2 %1, %5, %6\n\t"
                 "global_load_dwordx4 %2, %4, %7\n\tglobal_load_dwordx2 %3, %5, %7"
                 : "=&v"(qa), "=&v"(qbv), "=&v"(ka0), "=&v"(kb0) : "v"(vlin), "v"(v8), "s"(Qt), "s"(Kh) : "memory");
  }
  KV_M0(0); KV_P0(0); KV_P1(0); KV_P23(0);
#pragma unroll
  for (int sl = 0; sl < 2; ++sl) {
    lds_ptr tp = lds + sl * AT_SLOT + wid * AT_TILE;
    if (lane < 32) *(LDS_AS u32x2*)(tp + 1024 + 256 + lane * 8) = u32x2{0x00003f80u, 0u};
    if (lane < 48) {
      const int blk4 = (lane * 43) >> 9, dd = 20 + lane - blk4 * 12;
      const unsigned val = dd == 20 ? 0x3f803f80u : 0u;
      *(LDS_AS u32x4*)(tp + 1536 + blk4 * 512 + dd * 16) = u32x4{val, val, val, val};
    }
  }
  unsigned vo[5];
#pragma unroll
  for (int i = 0; i < 5; ++i) {
    const int p = i * 64 + lane, row = (p * 6554) >> 16;
    vo[i] = (unsigned)(row * 1280 + (p - row * 10) * 16 - (i & 3) * 1024);
  }
#define W_SRC(W, grp) ((W) + cbk * 160 + (size_t)((grp) * 256 + wid * 32) * 1280)
#define W_M0(reg, hi) SET_M0(lds0 + (unsigned)(KB_WR + (reg) * KB_GRP + wid * 5120 + (hi) * 4096))
#define W_P(W, i, grp) asm volatile("global_load_lds_dwordx4 %0, %1 offset:%2" :: "v"(vo[i]), "s"(W_SRC(W, grp)), "i"(((i) & 3) * 1024) : "memory")
  const char* hsrc = ws + WS_H + ((size_t)(rbk * 4 + mi) * 48 + kh) * 1024 + 4096;
#define H_LOAD(D, j, grp) asm volatile("global_load_dwordx4 %0, %1, %2 offset:%3" : "=v"(D[j]) : "v"(vlin), "s"(hsrc + (grp) * 8192), "i"((j) * 2048 - 4096) : "memory")
  const unsigned wra = lds0 + (unsigned)(KB_WR + (kh * 32 + 4 * ((gq & 1) + 4 * (gq >> 1)) + ((lane & 15) >> 2)) * 160 + (lane & 3) * 8);
  const unsigned wrb = wra + KB_GRP;

  unsigned oo[3];
#pragma unroll
  for (int g = 0; g < 3; ++g) {
    const int Gc = head * 5 + 2 * g + h, G8 = Gc & 7;
    oo[g] = (unsigned)((r >> 4) * (20 * 1024) + (r & 15) * 16 + (Gc >> 3) * 1024 + ((G8 >> 2) * 2 + (G8 & 1)) * 256 + ((G8 >> 1) & 1) * 8);
    PIN(oo[g]);
  }
  bf16x8 hA[4], hB[4];
  asm volatile("s_waitcnt vmcnt(4)" : "+v"(qa), "+v"(qbv), "+v"(ka0), "+v"(kb0) :: "memory");
  f32x16 o;
#pragma unroll
  for (int i = 0; i < 16; ++i) o[i] = 0.f;
  const f32x16 zero = o;
  if (h == 1) { qbv[0] = 0u; qbv[1] = 0u; kb0[0] = 0x00003f80u; kb0[1] = 0u; }
  const f32x16 s16 = MFMA32(ka0, qa, zero);
  f32x16 sA = MFMA32K8(__builtin_bit_cast(bf16x4, kb0), __builtin_bit_cast(bf16x4, qbv), s16);
  {
    float m = fmaxf(fmaxf(sA[0], sA[1]), sA[2]);
#pragma unroll
    for (int i = 3; i < 15; i += 2) m = fmaxf(fmaxf(m, sA[i]), sA[i + 1]);
    m = fmaxf(m, sA[15]);
    const auto mm = __builtin_amdgcn_permlane32_swap(__float_as_uint(m), __float_as_uint(m), false, false);
    m = fmaxf(__uint_as_float(mm[0]), __uint_as_float(mm[1]));
    if (h == 1) qbv[0] = cvtpk(-m, 0.f);
  }
  const bf16x4 qb4 = __builtin_bit_cast(bf16x4, qbv);
  sA = MFMA32K8(__builtin_bit_cast(bf16x4, kb0), qb4, s16);
  f32x16 sB;
  f32x4 acc[5];
#pragma unroll
  for (int j = 0; j < 5; ++j) acc[j] = f32x4{0.f, 0.f, 0.f, 0.f};
  WAIT_BAR(0);
  STAMP(1, 1);
  STAMPC(1, 14);
  STAMP2(2, 0);

  unsigned a16 = lds0 + vlin, a8 = lds0 + (unsigned)lane * 8u;
  int sdelta = AT_SLOT;
  bf16x8 kaA, v0A, v1A, kaB, v0B, v1B;
  bf16x4 kbA, kbB;
  bf16x4 wl[5], wu[5];
  u32x4 p0, p1 = {0u, 0u, 0u, 0u};
  v1B = __builtin_bit_cast(bf16x8, p1);
#define RD_KA(X, j) ka##X = *(const LDS_AS bf16x8*)(LP(a16) + (j) * AT_TILE)
#define RD_KB(X, j) kb##X = *(const LDS_AS bf16x4*)(LP(a8) + (j) * AT_TILE + 1024)
#define RD_V0(X, j) v0##X = *(const LDS_AS bf16x8*)(LP(a16) + (j) * AT_TILE + 1536)
#define RD_V1(X, j) v1##X = *(const LDS_AS bf16x8*)(LP(a16) + (j) * AT_TILE + 2560)
#define W_LO(WA, j, t) wl[t] = tr_read(LP(WA) + (j) * 10240 + (t) * 32)
#define W_HI(WA, j, t) wu[t] = tr_read(LP(WA) + (j) * 10240 + (t) * 32 + 1280)
#define G_MM(HS, j, t) acc[t] = MFMA16(cat8(wl[t], wu[t]), HS[j], acc[t])
#define EX2(S, i) do { S[i] = __builtin_amdgcn_exp2f(S[i]); S[i + 1] = __builtin_amdgcn_exp2f(S[i + 1]); } while (0)
#define MID() do { __builtin_amdgcn_sched_group_barrier(0x400, 2, 0); __builtin_amdgcn_sched_group_barrier(0x008, 1, 0); \
                    __builtin_amdgcn_sched_group_barrier(0x400, 2, 0); __builtin_amdgcn_sched_group_barrier(0x002, 2, 0); } while (0)
#define AT_STEP(SC, SN, X, Y, JN, PRE, WP, A0, A1, A2, A3, A4, A5, V0_, V1_, V2_, M1, M2, M3)            \
  do {                                                                                                   \
    PRE;                                                                                                 \
    asm volatile("" : "+v"(ka##X), "+v"(kb##X), "+v"(v0##X), "+v"(v1##X)); WP; SBAR();                   \
    EX2(SC, 0); PIN(SC); SBAR();                                                                         \
    o = MFMA32(v1##Y, __builtin_bit_cast(bf16x8, p1), o); SBAR();                                        \
    RD_KA(Y, JN); RD_KB(Y, JN); RD_V0(Y, JN); RD_V1(Y, JN); A0; A1; A2; A3; A4; A5; V0_; V1_; SBAR(); \
    M1; EX2(SC, 2); EX2(SC, 4); p0[0] = cvtpk(SC[0], SC[1]); p0[1] = cvtpk(SC[2], SC[3]); MID();         \
    asm volatile("" : "+v"(p0), "+v"(SC), "+v"(acc[0]), "+v"(acc[3])); SBAR();                           \
    SN = MFMA32(ka##X, qa, zero); SBAR();                                                                \
    M2; EX2(SC, 6); EX2(SC, 8); p0[2] = cvtpk(SC[4], SC[5]); p0[3] = cvtpk(SC[6], SC[7]); MID();         \
    asm volatile("" : "+v"(p0), "+v"(SC), "+v"(acc[1])); SBAR();                                         \
    SN = MFMA32K8(kb##X, qb4, SN); SBAR();                                                               \
    V2_; SBAR();                                                                                         \
    M3; EX2(SC, 10); EX2(SC, 12); p1[0] = cvtpk(SC[8], SC[9]); p1[1] = cvtpk(SC[10], SC[11]); MID();     \
    asm volatile("" : "+v"(p1), "+v"(SC), "+v"(acc[2]), "+v"(acc[4])); SBAR();                           \
    o = MFMA32(v0##X, __builtin_bit_cast(bf16x8, p0), o); SBAR();                                        \
    EX2(SC, 14); p1[2] = cvtpk(SC[12], SC[13]); p1[3] = cvtpk(SC[14], SC[15]); PIN(p1); SBAR();          \
  } while (0)
#define WP_E asm volatile("" : "+v"(wl[0]), "+v"(wu[0]), "+v"(wl[1]), "+v"(wu[1]), "+v"(wl[2]), "+v"(wu[2]))
#define WP_O asm volatile("" : "+v"(wl[3]), "+v"(wu[3]), "+v"(wl[4]), "+v"(wu[4]))
#define FLIP() do { a16 += (unsigned)sdelta; a8 += (unsigned)sdelta; sdelta = -sdelta; } while (0)
#define SYNC_H(HN) asm volatile("s_waitcnt vmcnt(3) lgkmcnt(0)\n\ts_barrier" : "+v"(HN[0]) :: "memory")
#define WAIT_H(HC, j, N) asm volatile("s_waitcnt vmcnt(" #N ")" : "+v"(HC[j]) :: "memory")
#define STEP_E(JN, PRE, WC, HC, j, VA, VB)                                                               \
  AT_STEP(sA, sB, A, B, JN, PRE, WP_E, W_LO(WC, j, 3), W_HI(WC, j, 3), W_LO(WC, j, 4), W_HI(WC, j, 4), NOP_, NOP_, NOP_, VA, VB, \
          G_MM(HC, j, 0), G_MM(HC, j, 1), G_MM(HC, j, 2))
#define STEP_O(JN, PRE, WA, jn, HC, j, VA, VB)                                                           \
  AT_STEP(sB, sA, B, A, JN, PRE, WP_O, W_LO(WA, jn, 0), W_HI(WA, jn, 0), W_LO(WA, jn, 1), W_HI(WA, jn, 1), W_LO(WA, jn, 2), \
          W_HI(WA, jn, 2), NOP_, VA, VB, G_MM(HC, j, 3), NOP_, G_MM(HC, j, 4))
#define CHUNK_G(c, WC, HC, WN, HN, RN, NW, NG, HG)                                                       \
  do {                                                                                                   \
    STEP_E(1, NOP_, WC, HC, 0, W_M0(RN, 0); W_P(NW, 0, NG), W_P(NW, 1, NG));                             \
    STEP_O(2, NOP_, WC, 1, HC, 0, W_P(NW, 2, NG), W_P(NW, 3, NG));                                       \
    STEP_E(3, WAIT_H(HC, 1, 6), WC, HC, 1, W_M0(RN, 1); W_P(NW, 4, NG), KV_M0((c) + 1); KV_P0((c) + 1)); \
    STEP_O(4, NOP_, WC, 2, HC, 1, KV_P1((c) + 1), KV_P23((c) + 1));                                      \
    STEP_E(5, WAIT_H(HC, 2, 10), WC, HC, 2, H_LOAD(HN, 0, HG), H_LOAD(HN, 1, HG));                       \
    STEP_O(6, NOP_, WC, 3, HC, 2, H_LOAD(HN, 2, HG), H_LOAD(HN, 3, HG));                                 \
    STEP_E(7, WAIT_H(HC, 3, 13), WC, HC, 3, NOP_, NOP_);                                                 \
    STEP_O(0, SYNC_H(HN); STAMP2(2, 1 + (c)); FLIP(), WN, 0, HC, 3, NOP_, NOP_);                         \
  } while (0)
#define PLAIN2_E(JN, VA, VB) AT_STEP(sA, sB, A, B, JN, NOP_, NOP_, NOP_, NOP_, NOP_, NOP_, NOP_, NOP_, NOP_, VA, VB, NOP_, NOP_, NOP_)
#define PLAIN2_O(JN, VA, VB) AT_STEP(sB, sA, B, A, JN, NOP_, NOP_, NOP_, NOP_, NOP_, NOP_, NOP_, NOP_, NOP_, VA, VB, NOP_, NOP_, NOP_)
#define PLAIN_E(JN, X0, X1, X2, X3) AT_STEP(sA, sB, A, B, JN, NOP_, NOP_, X0, X1, X2, X3, NOP_, NOP_, NOP_, NOP_, NOP_, NOP_, NOP_, NOP_)
#define PLAIN_O(JN, X0, X1, X2, X3) AT_STEP(sB, sA, B, A, JN, NOP_, NOP_, X0, X1, X2, X3, NOP_, NOP_, NOP_, NOP_, NOP_, NOP_, NOP_, NOP_)

  RD_KA(A, 0); RD_KB(A, 0); RD_V0(A, 0); RD_V1(A, 0);
  PLAIN2_E(1, W_M0(0, 0); W_P(w2, 0, 0), W_P(w2, 1, 0));
  PLAIN2_O(2, W_P(w2, 2, 0), W_P(w2, 3, 0));
  PLAIN2_E(3, W_M0(0, 1); W_P(w2, 4, 0), KV_M0(1); KV_P0(1));
  PLAIN2_O(4, KV_P1(1), KV_P23(1));
  PLAIN2_E(5, H_LOAD(hA, 0, 0), H_LOAD(hA, 1, 0));
  PLAIN2_O(6, H_LOAD(hA, 2, 0), H_LOAD(hA, 3, 0));
  PLAIN_E(7, NOP_, NOP_, NOP_, NOP_);
  AT_STEP(sB, sA, B, A, 0, SYNC_H(hA); STAMP2(2, 1); FLIP(), NOP_, W_LO(wra, 0, 0), W_HI(wra, 0, 0), W_LO(wra, 0, 1), W_HI(wra, 0, 1),
          W_LO(wra, 0, 2), W_HI(wra, 0, 2), NOP_, NOP_, NOP_, NOP_, NOP_, NOP_);
#pragma unroll 1
  for (int c = 1; c < 7; c += 2) {
    CHUNK_G(c, wra, hA, wrb, hB, 1, w2, c, c);
    const char* wn = c < 5 ? w2 : wo;
    CHUNK_G(c + 1, wrb, hB, wra, hA, 0, wn, (c < 5 ? c + 1 : 0), (c < 5 ? c + 1 : 5));
  }
  const int tok = rbk * 64 + mi * 16 + (lane & 15);
  const unsigned xo = (unsigned)(tok * 1280 + (cbk * 80 + kh * 48 + 4 * gq) * 2);
  const int j2 = kh ? 1 : 2;
  LDS_AS f32x4* red = (LDS_AS f32x4*)(lds + KB_RED) + (mi * 5) * 64 + lane;
  u32x2 gv[3], xv[3];
  PLAIN_E(1, NOP_,
          _Pragma("unroll") for (int j = 0; j < 3; ++j) gv[j] = *(const u32x2*)(b2 + cbk * 80 + kh * 48 + (j < 2 ? j : j2) * 16 + 4 * gq),
          _Pragma("unroll") for (int j = 0; j < 3; ++j) xv[j] = *(const u32x2*)(x + xo + (j < 2 ? j : j2) * 32),
          if (kh == 1) { red[0] = acc[0]; red[64] = acc[1]; red[128] = acc[2]; } else { red[192] = acc[3]; red[256] = acc[4]; });
  PLAIN_O(2, NOP_, NOP_, NOP_, NOP_);
  PLAIN_E(3, NOP_, NOP_, NOP_, NOP_);
  PLAIN_O(4, asm volatile("s_waitcnt vmcnt(0)" : "+v"(hA[1]), "+v"(hA[2]), "+v"(hA[3]) :: "memory");
          _Pragma("unroll") for (int j = 0; j < 3; ++j) { PIN(gv[j]); PIN(xv[j]); } W_M0(1, 0), W_P(wo, 0, 1), W_P(wo, 1, 1), W_P(wo, 2, 1));
  AT_STEP(sA, sB, A, B, 5, LBAR(0), NOP_, W_P(wo, 3, 1), W_M0(1, 1), W_P(wo, 4, 1), SET_M0(lds0 + (unsigned)(wid * 2560)),
          asm volatile("global_load_lds_dwordx4 %0, %3\n\tglobal_load_lds_dwordx4 %1, %3 offset:1024\n\t"
                       "s_mov_b32 exec_hi, 0\n\tglobal_load_lds_dwordx4 %2, %3 offset:2048\n\ts_mov_b32 exec_hi, -1"
                       :: "v"(vo[0]), "v"(vo[1]), "v"(vo[2]), "s"(wo + cbk * 160 + (size_t)(512 + wid * 16) * 1280) : "memory"),
          NOP_, NOP_, NOP_, NOP_, NOP_, NOP_, NOP_);
#define G_FIN(j, i)                                                                                       \
  do {                                                                                                    \
    const f32x4 tt = acc[j] + og[i] + f32x4{bf_lo(gv[i][0]), bf_hi(gv[i][0]), bf_lo(gv[i][1]), bf_hi(gv[i][1])}; \
    float t[4];                                                                                           \
    _Pragma("unroll") for (int e = 0; e < 4; ++e)                                                         \
      t[e] = tt[e] * __builtin_amdgcn_rcpf(1.0f + __builtin_amdgcn_exp2f(tt[e] * -1.4426950408889634f));  \
    gv[i] = u32x2{cvtpk(t[0], t[1]), cvtpk(t[2], t[3])};                                                  \
    PIN(gv[i]);                                                                                           \
  } while (0)
  {
    f32x4 og[3];
    if (kh == 0) { og[0] = red[0]; og[1] = red[64]; og[2] = red[128]; G_FIN(0, 0); G_FIN(1, 1); G_FIN(2, 2); }
    else { og[0] = red[192]; og[1] = red[256]; G_FIN(3, 0); G_FIN(4, 1); }
  }
#undef G_FIN
  SBAR();
  PLAIN_O(6, NOP_, NOP_, NOP_, NOP_);
  PLAIN_E(7, NOP_, NOP_, NOP_, NOP_);
  EX2(sB, 0); PIN(sB); SBAR();
  o = MFMA32(v1A, __builtin_bit_cast(bf16x8, p1), o); SBAR();
  EX2(sB, 2); EX2(sB, 4); EX2(sB, 6);
  p0 = u32x4{cvtpk(sB[0], sB[1]), cvtpk(sB[2], sB[3]), cvtpk(sB[4], sB[5]), cvtpk(sB[6], sB[7])};
  EX2(sB, 8); asm volatile("" : "+v"(p0), "+v"(sB)); SBAR();
  o = MFMA32(v0B, __builtin_bit_cast(bf16x8, p0), o); SBAR();
  EX2(sB, 10); EX2(sB, 12); EX2(sB, 14);
  p1 = u32x4{cvtpk(sB[8], sB[9]), cvtpk(sB[10], sB[11]), cvtpk(sB[12], sB[13]), cvtpk(sB[14], sB[15])};
  o = MFMA32(v1B, __builtin_bit_cast(bf16x8, p1), o);

  STAMP(1, 3);
  STAMPC(1, 15);
  STAMP2(2, 8);
  float l = __uint_as_float(__builtin_amdgcn_permlane32_swap(__float_as_uint(o[8]), __float_as_uint(o[8]), false, false)[1]);
  float big = fmaxf(fmaxf(__builtin_fabsf(o[0]), __builtin_fabsf(o[1])), l);
#pragma unroll
  for (int i = 2; i < 12; i += 2) big = fmaxf(fmaxf(big, __builtin_fabsf(o[i])), __builtin_fabsf(o[i + 1]));
  bool unsafe = !(big < 1.0e38f) || !(l > 0.25f);
#pragma unroll
  for (int i = 0; i < 12; ++i) unsafe |= o[i] != o[i];
  if (__builtin_expect(__any(unsafe), 0)) {
    if (h == 1) { qbv[0] = 0u; }
    const bf16x4 qz = __builtin_bit_cast(bf16x4, qbv);
    float m = -3.0e38f;
#pragma unroll
    for (int i = 0; i < 16; ++i) o[i] = 0.f;
    const unsigned onev = r == 20 ? 0x3f803f80u : 0u;
#pragma unroll 1
    for (int t = 0; t < 64; ++t) {
      const bf16x8 ka = *(const bf16x8*)(Kh + t * 1280 + lane * 16);
      u32x2 kb = *(const u32x2*)(Kh + t * 1280 + 1024 + r * 8);
      u32x4 v0 = {onev, onev, onev, onev}, v1 = v0;
      if (r < 20) {
        v0 = *(const u32x4*)(Vh + t * 1280 + vv);
        v1 = *(const u32x4*)(Vh + t * 1280 + 640 + vv);
      }
      f32x16 s = MFMA32(ka, qa, zero);
      s = MFMA32K8(__builtin_bit_cast(bf16x4, kb), qz, s);
      float tm = s[0];
#pragma unroll
      for (int i = 1; i < 16; ++i) tm = fmaxf(tm, s[i]);
      tm = fmaxf(tm, __shfl_xor(tm, 32));
      const float mn = fmaxf(m, tm);
      const float al = __builtin_amdgcn_exp2f(m - mn);
      m = mn;
      u32x4 q0, q1;
#pragma unroll
      for (int i = 0; i < 16; ++i) { o[i] *= al; s[i] = __builtin_amdgcn_exp2f(s[i] - mn); }
#pragma unroll
      for (int e = 0; e < 4; ++e) { q0[e] = cvtpk(s[2 * e], s[2 * e + 1]); q1[e] = cvtpk(s[8 + 2 * e], s[9 + 2 * e]); }
      o = MFMA32(__builtin_bit_cast(bf16x8, v0), __builtin_bit_cast(bf16x8, q0), o);
      o = MFMA32(__builtin_bit_cast(bf16x8, v1), __builtin_bit_cast(bf16x8, q1), o);
    }
    l = __shfl(o[8], 32 + r);
  }
  {
    const float inv = __builtin_amdgcn_rcpf(l);
    const char* ob = ws + WS_O + (size_t)(qb * 16 + wid * 2) * (20 * 1024);
#pragma unroll
    for (int g = 0; g < 3; ++g)
      if (g < 2 || h == 0)
        st8s(ob, oo[g], u32x2{min(cvtpk(o[4 * g] * inv, o[4 * g + 1] * inv), 0xfffffffeu),
                              min(cvtpk(o[4 * g + 2] * inv, o[4 * g + 3] * inv), 0xfffffffeu)});
  }
  asm volatile("s_waitcnt vmcnt(3) lgkmcnt(0)\n\ts_barrier" ::: "memory");
  STAMP(1, 4);
  {
    const srd_t ro = make_srd(ws + WS_O, (unsigned)(WS_G - WS_O));
    const unsigned oa = (unsigned)(((rbk * 4 + mi) * 20 + kh) * 1024);
    bf16x8 wfa[5][5];
#pragma unroll
    for (int c = 0; c < 5; ++c)
#pragma unroll
      for (int t = 0; t < 5; ++t)
        wfa[c][t] = cat8(tr_read(LP(wra) + c * 10240 + t * 32), tr_read(LP(wra) + c * 10240 + t * 32 + 1280));
    SBAR();
#pragma unroll
    for (int j = 0; j < 5; ++j) acc[j] = f32x4{0.f, 0.f, 0.f, 0.f};
    STAMP(1, 6);
    __builtin_amdgcn_s_sleep(14);
    u32x4 af[10];
    for (unsigned spins = 0; spins < (1u << 15); ++spins) {
      asm volatile("" ::: "memory");
#pragma unroll
      for (int c = 0; c < 10; ++c) af[c] = bload16_dev(ro, vlin, oa + c * 2048u);
      unsigned mx = 0u;
#pragma unroll
      for (int c = 0; c < 10; ++c) mx = max(max(max(mx, af[c][0]), af[c][1]), max(af[c][2], af[c][3]));
      if (!__any(mx == 0xffffffffu)) break;
      __builtin_amdgcn_s_sleep(4);
    }
    STAMP(1, 7);
    SBAR();
    bf16x8 wfb[5][5];
#pragma unroll
    for (int c = 5; c < 10; ++c) {
      const unsigned wa = c < 8 ? wrb + (unsigned)((c - 4) * 10240) : wra - (unsigned)KB_WR + (unsigned)((c - 8) * 10240);
      if (c < 8) {
#pragma unroll
        for (int t = 0; t < 5; ++t) wfb[c - 5][t] = cat8(tr_read(LP(wa) + t * 32), tr_read(LP(wa) + t * 32 + 1280));
      }
    }
    SBAR();
#pragma unroll
    for (int c = 0; c < 5; ++c)
#pragma unroll
      for (int t = 0; t < 5; ++t) acc[t] = MFMA16(wfa[c][t], __builtin_bit_cast(bf16x8, af[c]), acc[t]);
#pragma unroll
    for (int c = 8; c < 10; ++c)
#pragma unroll
      for (int t = 0; t < 5; ++t)
        wfb[c - 5][t] = cat8(tr_read(LP(wra - (unsigned)KB_WR) + (c - 8) * 10240 + t * 32), tr_read(LP(wra - (unsigned)KB_WR) + (c - 8) * 10240 + t * 32 + 1280));
#pragma unroll
    for (int c = 5; c < 10; ++c)
#pragma unroll
      for (int t = 0; t < 5; ++t) acc[t] = MFMA16(wfb[c - 5][t], __builtin_bit_cast(bf16x8, af[c]), acc[t]);
  }
  if (kh == 1) { red[0] = acc[0]; red[64] = acc[1]; red[128] = acc[2]; } else { red[192] = acc[3]; red[256] = acc[4]; }
  LBAR(0);
#define O_FIN(j, i)                                                                                       \
  do {                                                                                                    \
    const f32x4 sv = acc[j] + red[(j) * 64];                                                              \
    const float r0 = bf_lo(xv[i][0]) + sv[0] * bf_lo(gv[i][0]), r1 = bf_hi(xv[i][0]) + sv[1] * bf_hi(gv[i][0]); \
    const float r2 = bf_lo(xv[i][1]) + sv[2] * bf_lo(gv[i][1]), r3 = bf_hi(xv[i][1]) + sv[3] * bf_hi(gv[i][1]); \
    st8s(out, xo + (i) * 32, u32x2{cvtpk(r0, r1), cvtpk(r2, r3)});                                        \
  } while (0)
  if (kh == 0) { O_FIN(0, 0); O_FIN(1, 1); O_FIN(2, 2); } else { O_FIN(3, 0); O_FIN(4, 1); }
#undef O_FIN
  STAMP(1, 8);
}

extern "C" void kernel_launch(void* const* d_in, const int* in_sizes, int n_in, void* d_out, int out_size, void* d_ws,
                              size_t ws_size, hipStream_t stream) {
  (void)in_sizes; (void)n_in; (void)out_size; (void)ws_size;
  const char* x = (const char*)d_in[0];
  const char* wqkv = (const char*)d_in[1];
  const char* wo = (const char*)d_in[2];
  const char* w1 = (const char*)d_in[4];
  const unsigned short* b1 = (const unsigned short*)d_in[5];
  const char* w2 = (const char*)d_in[6];
  const unsigned short* b2 = (const unsigned short*)d_in[7];
  char* ws = (char*)d_ws;
  (void)hipFuncSetAttribute((const void*)ka_proj, hipFuncAttributeMaxDynamicSharedMemorySize, KA_LDS);
  (void)hipFuncSetAttribute((const void*)kb_main, hipFuncAttributeMaxDynamicSharedMemorySize, KB_LDS);
  ka_proj<<<256, 512, KA_LDS, stream>>>(x, wqkv, w1, b1, w2, wo, ws);
  kb_main<<<256, 512, KB_LDS, stream>>>(x, w2, b2, wo, ws, (char*)d_out);
}
```

```cpp
#include <hip/hip_runtime.h>
#include <hip/hip_bf16.h>
#include <stdint.h>

typedef __attribute__((ext_vector_type(8))) short bf16x8;
typedef __attribute__((ext_vector_type(4))) short bf16x4;
typedef __attribute__((ext_vector_type(16))) float f32x16;
typedef __attribute__((ext_vector_type(4))) float f32x4;
typedef __attribute__((ext_vector_type(2))) float f32x2;
typedef __attribute__((ext_vector_type(4))) unsigned u32x4;
typedef __attribute__((ext_vector_type(2))) unsigned u32x2;
typedef __attribute__((ext_vector_type(2))) __bf16 bf2;
typedef __attribute__((address_space(3))) char* lds_ptr;
#define LDS_AS __attribute__((address_space(3)))
#define DI __device__ __forceinline__

#define MFMA32(a, b, c) __builtin_amdgcn_mfma_f32_32x32x16_bf16((a), (b), (c), 0, 0, 0)
#define MFMA32K8(a, b, c) __builtin_amdgcn_mfma_f32_32x32x8bf16_1k((a), (b), (c), 0, 0, 0)
#define MFMA16(a, b, c) __builtin_amdgcn_mfma_f32_16x16x32_bf16((a), (b), (c), 0, 0, 0)
#define SBAR() __builtin_amdgcn_sched_barrier(0)
#define PIN(x) asm volatile("" : "+v"(x))
#define WAIT_BAR(N) asm volatile("s_waitcnt vmcnt(" #N ") lgkmcnt(0)\n\ts_barrier" ::: "memory")

constexpr int D_ = 640, NH = 32;
constexpr float LN_EPS = 1e-5f;
constexpr float QSCALE = 0.22360679774997896f * 1.4426950408889634f;

constexpr size_t WS_Q = 0;
constexpr size_t WS_K = WS_Q + (size_t)NH * 64 * 1280;
constexpr size_t WS_V = WS_K + (size_t)NH * 64 * 1280;
constexpr size_t WS_H = WS_V + (size_t)NH * 64 * 1280;
constexpr size_t WS_O = WS_H + (size_t)128 * 48 * 1024;
constexpr size_t WS_G = WS_O + (size_t)128 * 20 * 1024;

#define STAMP(kern, i) do { } while (0)
#define STAMPC(kern, i) do { } while (0)
#define STAMP2(kern, i) do { } while (0)

DI void st16s(const void* sb, unsigned off, u32x4 v) { asm volatile("global_store_dwordx4 %0, %1, %2 sc1\n\ts_nop 1" :: "v"(off), "v"(v), "s"(sb) : "memory"); }
DI void st8s(const void* sb, unsigned off, u32x2 v) { asm volatile("global_store_dwordx2 %0, %1, %2 sc1" :: "v"(off), "v"(v), "s"(sb) : "memory"); }
DI unsigned cvtpk(float lo, float hi) {
  f32x2 f = {lo, hi};
  return __builtin_bit_cast(unsigned, __builtin_convertvector(f, bf2));
}
DI float bf_lo(unsigned u) { return __uint_as_float(u << 16); }
DI float bf_hi(unsigned u) { return __uint_as_float(u & 0xffff0000u); }
typedef short v4i16_t __attribute__((ext_vector_type(4)));
DI bf16x4 tr_read(lds_ptr p) {
  return __builtin_bit_cast(bf16x4, __builtin_amdgcn_ds_read_tr16_b64_v4i16((LDS_AS v4i16_t*)p));
}
DI bf16x8 cat8(bf16x4 a, bf16x4 b) { return __builtin_shufflevector(a, b, 0, 1, 2, 3, 4, 5, 6, 7); }

constexpr int KA_BUF = 45056;
constexpr int KA_STAT = 3 * KA_BUF;
constexpr int KA_SCS = KA_STAT + 2048, KA_SB1 = KA_SCS + 3584, KA_SMR = KA_SB1 + 384;
constexpr int KA_LDS = KA_SMR + 2048;
#define LBAR(N) asm volatile("s_waitcnt lgkmcnt(" #N ")\n\ts_barrier" ::: "memory")
#define LP(a) ((lds_ptr)(uintptr_t)(a))
#define SET_M0(v) asm volatile("s_mov_b32 m0, %0\n\ts_nop 0" :: "s"(v) : "memory")
#define NOP_ do { } while (0)
#define MFMA4(a, b, c) __builtin_amdgcn_mfma_f32_4x4x4bf16_1k((a), (b), (c), 0, 0, 0)

typedef __amdgpu_buffer_rsrc_t srd_t;
DI srd_t make_srd(const void* p, unsigned bytes) { return __builtin_amdgcn_make_buffer_rsrc((void*)p, (short)0, (int)bytes, 0x00020000); }
DI float x32_add(float v) {
  const auto rr = __builtin_amdgcn_permlane32_swap(__float_as_uint(v), __float_as_uint(v), false, false);
  return __uint_as_float(rr[0]) + __uint_as_float(rr[1]);
}
DI bf16x4 lo4(bf16x8 v) { return __builtin_shufflevector(v, v, 0, 1, 2, 3); }
DI bf16x4 hi4(bf16x8 v) { return __builtin_shufflevector(v, v, 4, 5, 6, 7); }

template <int TYPE, int nh>
DI void ka_body(lds_ptr lds, const char* __restrict__ x, const char* __restrict__ wqkv, const char* __restrict__ wh1,
                const unsigned short* __restrict__ b1, const char* __restrict__ w2, const char* __restrict__ wo,
                const int n0, const int f0, const int rb, const int cb, char* __restrict__ ws) {
  constexpr int NHT = nh ? 1 : 2;
  const int tid = threadIdx.x, lane = tid & 63;
  const int w = __builtin_amdgcn_readfirstlane(tid >> 6);
  const int mi = w & 3;
  const int r = lane & 31, h = lane >> 5;
  const unsigned lds0 = (unsigned)(uintptr_t)lds;
  STAMP(0, 0);
  const unsigned vx0 = (unsigned)((lane >> 3) * 1280 + (((lane & 7) ^ (lane >> 4)) << 4));
  const unsigned vx1 = (unsigned)((8 + (lane >> 3)) * 1280 + (((lane & 7) ^ (4 + (lane >> 4))) << 4) - 1024);
  const unsigned vw = (unsigned)((lane >> 3) * 3840 + ((((((lane >> 2) ^ (lane >> 4)) & 1) << 2) | (lane & 3)) << 4));
  const int p0 = w * 96 + lane, p1 = w * 96 + 64 + r;
  const int r0 = (p0 * 5462) >> 16, r1 = (p1 * 5462) >> 16;
  const unsigned vh0 = (unsigned)(r0 * 3072 + (p0 - r0 * 12) * 16), vh1 = (unsigned)(r1 * 3072 + (p1 - r1 * 12) * 16 - 1024);
  const char* xs = x + (size_t)(rb * 128 + 16 * w) * 1280;
  const char* wsrc = wqkv + ((size_t)(8 * w) * 1920 + n0) * 2;
  const char* hsrc = wh1 + f0 * 2;
  const unsigned mx = lds0 + (unsigned)w * 2048u, mw = lds0 + 16384u + (unsigned)w * 1024u, mh = lds0 + 32768u + (unsigned)w * 1536u;
#define KA_DX(c, buf) do { SET_M0(mx + (unsigned)(buf)); asm volatile("global_load_lds_dwordx4 %0, %2\n\tglobal_load_lds_dwordx4 %1, %2 offset:1024" :: "v"(vx0), "v"(vx1), "s"(xs + (c) * 128) : "memory"); } while (0)
#define KA_DW(c, buf, hf) do { SET_M0(mw + (unsigned)(buf) + (hf) * 8192u); asm volatile("global_load_lds_dwordx4 %0, %1" :: "v"(vw), "s"(wsrc + (size_t)(c) * (64 * 3840) + (hf) * 128) : "memory"); } while (0)
#define KA_DH(c, buf) do { SET_M0(mh + (unsigned)(buf)); asm volatile("global_load_lds_dwordx4 %0, %2\n\ts_mov_b32 exec_hi, 0\n\tglobal_load_lds_dwordx4 %1, %2 offset:1024\n\ts_mov_b32 exec_hi, -1" :: "v"(vh0), "v"(vh1), "s"(hsrc + (size_t)(c) * (64 * 3072)) : "memory"); } while (0)
#define KA_ISSUE(c, buf) do { KA_DX(c, buf); KA_DW(c, buf, 0); KA_DW(c, buf, 1); KA_DH(c, buf); } while (0)
  unsigned braw = 0, warm = 0;
  if (nh == 0 && tid < 96) braw = b1[f0 + tid];
  SBAR();
  KA_ISSUE(0, 0);
  KA_ISSUE(1, KA_BUF);

  unsigned xa[4], wa0[4], ha[4];
  {
    const unsigned wb = lds0 + (unsigned)(16384 + nh * 8192 + (8 * h + ((lane & 15) >> 2)) * 128 + 64 * ((lane >> 3) & 1) +
                                          (16 * ((lane >> 4) & 1) + 4 * (lane & 3)) * 2);
    const unsigned hb = lds0 + (unsigned)(32768 + (8 * h + ((lane & 15) >> 2)) * 192 + nh * 128 + (16 * ((lane >> 4) & 1) + 4 * (lane & 3)) * 2);
#pragma unroll
    for (int i = 0; i < 4; ++i) {
      const int ks = i ^ mi;
      xa[i] = lds0 + (unsigned)((mi * 32 + r) * 128 + (((2 * ks + h) ^ ((r >> 1) & 7)) << 4));
      wa0[i] = wb + (unsigned)(ks * 2048);
      ha[i] = hb + (unsigned)(ks * 3072);
    }
  }
  const int tq = rb * 4 + mi;
  const bool isq = n0 < 640;
  unsigned so[TYPE == 1 ? 2 : 8];
  if (TYPE == 1) {
#pragma unroll
    for (int j = 0; j < 2; ++j) {
      const int nn = n0 - 1280 + nh * 64 + j * 32 + r;
      const int head = (nn * 3277) >> 16;
      so[j] = (unsigned)(head * (64 * 1280) + (h * 20 + nn - head * 20) * 16);
      PIN(so[j]);
    }
  } else {
#pragma unroll
    for (int j = 0; j < 8; ++j) {
      const int G = (((isq ? n0 : n0 - 640) + nh * 64) >> 2) + h + 2 * j;
      const int head = (G * 205) >> 10;
      const int dg = G - head * 5;
      so[j] = (unsigned)(head * (64 * 1280) + (dg < 4 ? (dg >> 1) * 512 + r * 16 + (dg & 1) * 8 : 1024 + r * 8));
      PIN(so[j]);
    }
  }
  f32x16 acc[2], ach[NHT];
  f32x4 cq[2], chh[NHT], rsum = {0.f, 0.f, 0.f, 0.f}, rsq = rsum;
#pragma unroll
  for (int i = 0; i < 16; ++i) {
    acc[0][i] = 0.f; acc[1][i] = 0.f;
#pragma unroll
    for (int j = 0; j < NHT; ++j) ach[j][i] = 0.f;
  }
  cq[0] = rsum; cq[1] = rsum;
#pragma unroll
  for (int j = 0; j < NHT; ++j) chh[j] = rsum;
  bf16x8 sel;
  {
    const short sv = (lane & 15) == ((lane >> 4) & 1) ? (short)0x3f80 : (short)0;
    sel = bf16x8{sv, sv, sv, sv, sv, sv, sv, sv};
    PIN(sel);
  }
  bf16x8 xf[3], w0[3], w1[3], wh[3][NHT];
  unsigned wa1[4];
#pragma unroll
  for (int i = 0; i < 4; ++i) wa1[i] = wa0[i] ^ 64u;
#define KA_READ(B, i)                                                                                           \
  do {                                                                                                          \
    constexpr int s_ = ((B) + (i)) % 3;                                                                         \
    xf[s_] = *(const LDS_AS bf16x8*)(LP(xa[i]) + BUFOFF(B));                                                    \
    w0[s_] = cat8(tr_read(LP(wa0[i]) + BUFOFF(B)), tr_read(LP(wa0[i]) + BUFOFF(B) + 512));                      \
    w1[s_] = cat8(tr_read(LP(wa1[i]) + BUFOFF(B)), tr_read(LP(wa1[i]) + BUFOFF(B) + 512));                      \
    _Pragma("unroll") for (int j_ = 0; j_ < NHT; ++j_)                                                          \
      wh[s_][j_] = cat8(tr_read(LP(ha[i]) + BUFOFF(B) + j_ * 64), tr_read(LP(ha[i]) + BUFOFF(B) + j_ * 64 + 768)); \
  } while (0)
#define SGB(m, n) __builtin_amdgcn_sched_group_barrier((m), (n), 0)
#define KA_STEP(B, i, RD) KA_STEPX(B, i, RD, true)
#define KA_STEPX(B, i, RD, ROWS)                                                                                \
  do {                                                                                                          \
    constexpr int b_ = ((B) + (i)) % 3;                                                                         \
    if constexpr (NHT == 2) asm volatile("" : "+v"(xf[b_]), "+v"(w0[b_]), "+v"(w1[b_]), "+v"(wh[b_][0]), "+v"(wh[b_][NHT - 1])); \
    else asm volatile("" : "+v"(xf[b_]), "+v"(w0[b_]), "+v"(w1[b_]), "+v"(wh[b_][0]));                          \
    SBAR();                                                                                                     \
    RD;                                                                                                         \
    if (TYPE == 1) { acc[0] = MFMA32(xf[b_], w0[b_], acc[0]); acc[1] = MFMA32(xf[b_], w1[b_], acc[1]); }        \
    else { acc[0] = MFMA32(w0[b_], xf[b_], acc[0]); acc[1] = MFMA32(w1[b_], xf[b_], acc[1]); }                  \
    _Pragma("unroll") for (int j_ = 0; j_ < NHT; ++j_) ach[j_] = MFMA32(wh[b_][j_], xf[b_], ach[j_]);           \
    SGB(0x8, 1); SGB(0x100, 3); SGB(0x8, 1); SGB(0x100, 2); SGB(0x8, 1); SGB(0x100, 2);                         \
    if (NHT == 2) { SGB(0x8, 1); SGB(0x100, 2); }                                                               \
    if constexpr (NHT == 2) asm volatile("" : "+v"(acc[0]), "+v"(acc[1]), "+v"(ach[0]), "+v"(ach[NHT - 1]));    \
    else asm volatile("" : "+v"(acc[0]), "+v"(acc[1]), "+v"(ach[0]));                                           \
    SBAR();                                                                                                     \
    if ((i) == 0) {                                                                              \
      cq[0] = MFMA16(w0[b_], sel, cq[0]); cq[1] = MFMA16(w1[b_], sel, cq[1]);                                   \
      _Pragma("unroll") for (int j_ = 0; j_ < NHT; ++j_) chh[j_] = MFMA16(wh[b_][j_], sel, chh[j_]);            \
      if constexpr (NHT == 2) asm volatile("" : "+v"(cq[0]), "+v"(cq[1]), "+v"(chh[0]), "+v"(chh[NHT - 1]));    \
      else asm volatile("" : "+v"(cq[0]), "+v"(cq[1]), "+v"(chh[0]));                                           \
    }                                                                                                           \
    if ((ROWS) && (nh ? (i) < 2 : (i) >= 2)) {                                                   \
      rsq = MFMA4(lo4(xf[b_]), lo4(xf[b_]), rsq); rsum = MFMA16(xf[b_], sel, rsum);                             \
      rsq2 = MFMA4(hi4(xf[b_]), hi4(xf[b_]), rsq2);                                                             \
      asm volatile("" : "+v"(rsum), "+v"(rsq), "+v"(rsq2));                                                     \
    }                                                                                                           \
    SBAR();                                                                                                     \
  } while (0)
#define BUFOFF(B) ((B) * KA_BUF)
#define KA_CHUNK(c, B, BN, WAITN, PREV, MORE)                                                                   \
  do {                                                                                                          \
    KA_STEP(B, 0, KA_READ(B, 2));                                                                               \
    if ((PREV)) { KA_DH((c) + 2, BUFOFF(((B) + 2) % 3)); SBAR(); }                               \
    KA_STEP(B, 1, KA_READ(B, 3));                                                                               \
    WAIT_BAR(WAITN);                                                                                            \
    KA_STEP(B, 2, KA_READ(BN, 0));                                                                              \
    if ((MORE)) { KA_DX((c) + 3, BUFOFF(B)); SBAR(); }                                           \
    KA_STEP(B, 3, KA_READ(BN, 1));                                                                              \
    if ((MORE)) { KA_DW((c) + 3, BUFOFF(B), 0); KA_DW((c) + 3, BUFOFF(B), 1); SBAR(); }          \
  } while (0)
  f32x4 rsq2 = rsum;
  WAIT_BAR(6);
  STAMP(0, 1);
  STAMPC(0, 14);
  KA_READ(0, 0);
  KA_READ(0, 1);
  KA_STEP(0, 0, KA_READ(0, 2));
  KA_DX(2, BUFOFF(2)); KA_DW(2, BUFOFF(2), 0); KA_DW(2, BUFOFF(2), 1); SBAR();
  KA_STEP(0, 1, KA_READ(0, 3));
  KA_DH(2, BUFOFF(2)); SBAR();
  WAIT_BAR(6);
  KA_STEP(0, 2, KA_READ(1, 0));
  KA_DX(3, BUFOFF(0)); SBAR();
  KA_STEP(0, 3, KA_READ(1, 1));
  KA_DW(3, BUFOFF(0), 0); KA_DW(3, BUFOFF(0), 1); SBAR();
#pragma unroll 1
  for (int c = 1; c < 7; c += 3) {
    KA_CHUNK(c, 1, 2, 6, true, true);
    KA_CHUNK(c + 1, 2, 0, 6, true, true);
    KA_CHUNK(c + 2, 0, 1, 6, true, true);
  }
  KA_CHUNK(7, 1, 2, 6, true, false);
  KA_CHUNK(8, 2, 0, 0, false, false);
  KA_STEP(0, 0, KA_READ(0, 2));
  KA_STEP(0, 1, KA_READ(0, 3));
  if (nh == 0) {
    asm volatile("" : "+v"(xf[2]), "+v"(xf[0]));
    rsq = MFMA4(lo4(xf[2]), lo4(xf[2]), rsq); rsum = MFMA16(xf[2], sel, rsum); rsq2 = MFMA4(hi4(xf[2]), hi4(xf[2]), rsq2);
    rsum = MFMA16(xf[0], sel, rsum); rsq = MFMA4(lo4(xf[0]), lo4(xf[0]), rsq); rsq2 = MFMA4(hi4(xf[0]), hi4(xf[0]), rsq2);
  }
  asm volatile("s_nop 7" : "+v"(rsum), "+v"(rsq), "+v"(rsq2));
  rsq += rsq2;
  STAMP(0, 3);
  STAMPC(0, 15);
  LDS_AS float* st_r = (LDS_AS float*)(lds + KA_STAT);
  LDS_AS float* st_cs = (LDS_AS float*)(lds + KA_SCS);
  LDS_AS float* st_b = (LDS_AS float*)(lds + KA_SB1);
  {
    const int d = lane & 3;
    const float sq = x32_add(d == 0 ? rsq[0] : d == 1 ? rsq[1] : d == 2 ? rsq[2] : rsq[3]);
    if (h == 0) st_r[256 + nh * 128 + mi * 32 + r] = sq;
    if ((lane & 15) < 2) {
      const int q4 = (lane & 1) * 16 + (lane >> 4) * 4;
      *(LDS_AS f32x4*)(st_r + nh * 128 + mi * 32 + q4) = rsum;
      *(LDS_AS f32x4*)(st_cs + mi * 224 + nh * 64 + q4) = cq[0];
      *(LDS_AS f32x4*)(st_cs + mi * 224 + nh * 64 + 32 + q4) = cq[1];
#pragma unroll
      for (int j = 0; j < NHT; ++j) *(LDS_AS f32x4*)(st_cs + mi * 224 + 128 + nh * 64 + j * 32 + q4) = chh[j];
    }
    if (nh == 0 && tid < 96) st_b[tid] = __uint_as_float(braw << 16);
  }
  SBAR();
  KA_STEPX(0, 2, NOP_, false);
  LBAR(0);
  float cpart = 0.f;
  if (tid < 224) cpart = (st_cs[tid] + st_cs[224 + tid]) + (st_cs[448 + tid] + st_cs[672 + tid]);
  const float sx_ = st_r[mi * 32 + r] + st_r[128 + mi * 32 + r], sq_ = st_r[256 + mi * 32 + r] + st_r[384 + mi * 32 + r];
  SBAR();
  KA_STEPX(0, 3, NOP_, false);
#undef KA_READ
#undef KA_STEP
#undef KA_STEPX
#undef KA_CHUNK
#undef KA_ISSUE
  if (tid < 224) st_cs[tid] = cpart;
  {
    const char* ob = ws + WS_O + (size_t)blockIdx.x * 10240;
    const u32x4 ff = {0xffffffffu, 0xffffffffu, 0xffffffffu, 0xffffffffu};
    st16s(ob, (unsigned)tid * 16u, ff);
    if (w < 2) st16s(ob + 8192, (unsigned)tid * 16u, ff);
  }
  if (nh == 1) {
    const int l0_ = blockIdx.x * 85 + (tid - 256 < 85 ? tid - 256 : 0);
    const char* p = l0_ < 15360 ? w2 + (size_t)l0_ * 128 : wo + (size_t)(l0_ - 15360) * 128;
    if (w < 6) asm volatile("global_load_dword %0, %1, off" : "=v"(warm) : "v"(p) : "memory");
  }
  const float mu = sx_ * (1.0f / D_);
  const float var = fmaxf(sq_ * (1.0f / D_) - mu * mu, 0.f);
  const float rs = __builtin_amdgcn_rsqf(var + LN_EPS);
  LDS_AS f32x2* st_w = (LDS_AS f32x2*)(lds + KA_SMR) + w * 32;
  if (TYPE == 1 && h == 0) st_w[r] = f32x2{mu * rs, rs};
  LBAR(0);
  STAMP(0, 5);
  if (cb < 15) {
    if (TYPE == 1) {
      char* dst = ws + WS_V + (size_t)tq * 1280;
      const float cs0 = st_cs[nh * 64 + r], cs1 = st_cs[nh * 64 + 32 + r];
#pragma unroll
      for (int s = 0; s < 2; ++s) {
        f32x2 mr[8];
#pragma unroll
        for (int e = 0; e < 8; ++e) mr[e] = st_w[16 * s + 8 * (e >> 2) + 4 * h + (e & 3)];
        u32x4 o0, o1;
#pragma unroll
        for (int e = 0; e < 4; ++e) {
          const int i = 8 * s + 2 * e;
          o0[e] = cvtpk(acc[0][i] * mr[2 * e][1] - mr[2 * e][0] * cs0, acc[0][i + 1] * mr[2 * e + 1][1] - mr[2 * e + 1][0] * cs0);
          o1[e] = cvtpk(acc[1][i] * mr[2 * e][1] - mr[2 * e][0] * cs1, acc[1][i + 1] * mr[2 * e + 1][1] - mr[2 * e + 1][0] * cs1);
        }
        st16s(dst + s * 640, so[0], o0);
        st16s(dst + s * 640, so[1], o1);
      }
    } else {
      const float sc = isq ? rs * QSCALE : rs, ms = mu * sc;
      char* base = ws + (isq ? WS_Q : WS_K) + (size_t)tq * 1280;
#pragma unroll
      for (int j = 0; j < 2; ++j)
#pragma unroll
        for (int g = 0; g < 4; ++g) {
          const f32x4 cs = *(const LDS_AS f32x4*)(st_cs + nh * 64 + j * 32 + 8 * g + 4 * h);
          const u32x2 o = {cvtpk(acc[j][4 * g] * sc - ms * cs[0], acc[j][4 * g + 1] * sc - ms * cs[1]),
                           cvtpk(acc[j][4 * g + 2] * sc - ms * cs[2], acc[j][4 * g + 3] * sc - ms * cs[3])};
          st8s(base, so[j * 4 + g], o);
        }
    }
  }
  {
    const float sc = rs * __builtin_amdgcn_rsqf(var * rs * rs + LN_EPS), ms = mu * sc;
    constexpr float GK1 = -2.0f * 1.4426950408889634f * 0.7978845608028654f, GK2 = GK1 * 0.044715f;
    const char* dst = ws + WS_H + ((size_t)(rb * 8 + mi * 2) * 48 + (f0 >> 5) + nh * 2) * 1024;
    const unsigned ho = (unsigned)((r >> 4) * (48 * 1024) + (h * 16 + (r & 15)) * 16);
    const f32x2 sc2 = {sc, sc}, ms2 = {ms, ms}, k1 = {GK1, GK1}, k2 = {GK2, GK2}, one2 = {1.0f, 1.0f};
#pragma unroll
    for (int j = 0; j < NHT; ++j)
#pragma unroll
      for (int u = 0; u < 2; ++u) {
        u32x4 o;
#pragma unroll
        for (int gg = 0; gg < 2; ++gg) {
          const int g = 2 * u + gg;
          const f32x4 cs = *(const LDS_AS f32x4*)(st_cs + 128 + nh * 64 + j * 32 + 8 * g + 4 * h);
          const f32x4 bb = *(const LDS_AS f32x4*)(st_b + nh * 64 + j * 32 + 8 * g + 4 * h);
#pragma unroll
          for (int e = 0; e < 2; ++e) {
            const f32x2 a2 = {ach[j][4 * g + 2 * e], ach[j][4 * g + 2 * e + 1]};
            const f32x2 c2 = {cs[2 * e], cs[2 * e + 1]}, b2_ = {bb[2 * e], bb[2 * e + 1]};
            const f32x2 t = a2 * sc2 + (b2_ - ms2 * c2);
            const f32x2 arg = t * ((t * t) * k2 + k1);
            const f32x2 den = f32x2{__builtin_amdgcn_exp2f(arg[0]), __builtin_amdgcn_exp2f(arg[1])} + one2;
            const f32x2 v = t * f32x2{__builtin_amdgcn_rcpf(den[0]), __builtin_amdgcn_rcpf(den[1])};
            o[2 * gg + e] = cvtpk(v[0], v[1]);
          }
        }
        st16s(dst + j * 1024 + u * 512, ho, o);
      }
  }
  STAMP(0, 4);
  asm volatile("s_waitcnt vmcnt(0)" : "+v"(warm) :: "memory");
}

__global__ __launch_bounds__(512, 2) void ka_proj(const char* __restrict__ x, const char* __restrict__ wqkv,
                                                  const char* __restrict__ w1, const unsigned short* __restrict__ b1,
                                                  const char* __restrict__ w2, const char* __restrict__ wo,
                                                  char* __restrict__ ws) {
  extern __shared__ __attribute__((aligned(16))) char smem[];
  lds_ptr lds = (lds_ptr)smem;
  const int bid = blockIdx.x, xg = bid & 7, idx = bid >> 3;
  const int rb = (xg & 1) * 8 + (idx & 7), cb = (xg >> 1) * 4 + (idx >> 3);
  const int n0 = (cb < 15 ? cb : 14) * 128, f0 = cb * 96;
  const int nh = __builtin_amdgcn_readfirstlane(threadIdx.x >> 8);
  if (cb < 10) {
    if (nh) ka_body<0, 1>(lds, x, wqkv, w1, b1, w2, wo, n0, f0, rb, cb, ws);
    else ka_body<0, 0>(lds, x, wqkv, w1, b1, w2, wo, n0, f0, rb, cb, ws);
  } else {
    if (nh) ka_body<1, 1>(lds, x, wqkv, w1, b1, w2, wo, n0, f0, rb, cb, ws);
    else ka_body<1, 0>(lds, x, wqkv, w1, b1, w2, wo, n0, f0, rb, cb, ws);
  }
}

DI u32x4 bload16_dev(srd_t r, unsigned voff, unsigned soff) { return __builtin_amdgcn_raw_buffer_load_b128(r, (int)voff, (int)soff, 16); }

constexpr int AT_TILE = 3584;
constexpr int AT_SLOT = 28672;
constexpr int KB_WR = 2 * AT_SLOT;
constexpr int KB_GRP = 40960;
constexpr int KB_RED = KB_WR + 2 * KB_GRP;
constexpr int KB_LDS = KB_RED + 20480;

__global__ __launch_bounds__(512, 2) void kb_main(const char* __restrict__ x, const char* __restrict__ w2,
                                                  const unsigned short* __restrict__ b2, const char* __restrict__ wo,
                                                  char* __restrict__ ws, char* __restrict__ out) {
  extern __shared__ __attribute__((aligned(16))) char smem[];
  lds_ptr lds = (lds_ptr)smem;
  const int tid = threadIdx.x, lane = tid & 63;
  const int wid = __builtin_amdgcn_readfirstlane(tid >> 6);
  const int r = lane & 31, h = lane >> 5;
  const int xg = blockIdx.x & 7, idx = blockIdx.x >> 3;
  const int head = xg * 4 + (idx >> 3), qb = idx & 7;
  const int rbk = xg * 4 + (idx & 3), cbk = idx >> 2;
  const int mi = wid & 3, kh = wid >> 2, gq = lane >> 4;
  const unsigned lds0 = (unsigned)(uintptr_t)lds;
  const char* Kh = ws + WS_K + (size_t)head * (64 * 1280);
  const char* Vh = ws + WS_V + (size_t)head * (64 * 1280);
  const char* Qt = ws + WS_Q + (size_t)(head * 64 + qb * 8 + wid) * 1280;
  STAMP(1, 0);

  const unsigned vlin = (unsigned)lane * 16u;
  const unsigned vv = (unsigned)(h * 20 + r) * 16u;
  const unsigned vv2 = vv + (unsigned)(WS_V - WS_K) - 1280u - 1536u, vv3 = vv2 + 640u - 1024u;
  const unsigned long long mk16 = 0xffffull, mk20 = 0x000fffff000fffffull;
#define KV_M0(c) SET_M0(lds0 + (unsigned)(((c) & 1) * AT_SLOT + wid * AT_TILE))
#define KV_SRC(c) (Kh + (size_t)((c) * 8 + wid + 1) * 1280)
#define KV_P0(c) asm volatile("global_load_lds_dwordx4 %0, %1" :: "v"(vlin), "s"(KV_SRC(c)) : "memory")
#define KV_P1(c) asm volatile("s_mov_b64 exec, %2\n\tglobal_load_lds_dwordx4 %0, %1 offset:1024\n\ts_mov_b64 exec, -1" :: "v"(vlin), "s"(KV_SRC(c)), "s"(mk16) : "memory")
#define KV_P23(c) asm volatile("s_mov_b64 exec, %3\n\tglobal_load_lds_dwordx4 %0, %2 offset:1536\n\tglobal_load_lds_dwordx4 %1, %2 offset:2560\n\ts_mov_b64 exec, -1" :: "v"(vv2), "v"(vv3), "s"(KV_SRC(c)), "s"(mk20) : "memory")
  bf16x8 qa, ka0;
  u32x2 qbv, kb0;
  {
    const unsigned v8 = 1024u + (unsigned)r * 8u;
    asm volatile("global_load_dwordx4 %0, %4, %6\n\tglobal_load_dwordx2 %1, %5, %6\n\t"
                 "global_load_dwordx4 %2, %4, %7\n\tglobal_load_dwordx2 %3, %5, %7"
                 : "=&v"(qa), "=&v"(qbv), "=&v"(ka0), "=&v"(kb0) : "v"(vlin), "v"(v8), "s"(Qt), "s"(Kh) : "memory");
  }
  KV_M0(0); KV_P0(0); KV_P1(0); KV_P23(0);
#pragma unroll
  for (int sl = 0; sl < 2; ++sl) {
    lds_ptr tp = lds + sl * AT_SLOT + wid * AT_TILE;
    if (lane < 32) *(LDS_AS u32x2*)(tp + 1024 + 256 + lane * 8) = u32x2{0x00003f80u, 0u};
    if (lane < 48) {
      const int blk4 = (lane * 43) >> 9, dd = 20 + lane - blk4 * 12;
      const unsigned val = dd == 20 ? 0x3f803f80u : 0u;
      *(LDS_AS u32x4*)(tp + 1536 + blk4 * 512 + dd * 16) = u32x4{val, val, val, val};
    }
  }
  unsigned vo[5];
#pragma unroll
  for (int i = 0; i < 5; ++i) {
    const int p = i * 64 + lane, row = (p * 6554) >> 16;
    vo[i] = (unsigned)(row * 1280 + (p - row * 10) * 16 - (i & 3) * 1024);
  }
#define W_SRC(W, grp) ((W) + cbk * 160 + (size_t)((grp) * 256 + wid * 32) * 1280)
#define W_M0(reg, hi) SET_M0(lds0 + (unsigned)(KB_WR + (reg) * KB_GRP + wid * 5120 + (hi) * 4096))
#define W_P(W, i, grp) asm volatile("global_load_lds_dwordx4 %0, %1 offset:%2" :: "v"(vo[i]), "s"(W_SRC(W, grp)), "i"(((i) & 3) * 1024) : "memory")
  const char* hsrc = ws + WS_H + ((size_t)(rbk * 4 + mi) * 48 + kh) * 1024 + 4096;
#define H_LOAD(D, j, grp) asm volatile("global_load_dwordx4 %0, %1, %2 offset:%3" : "=v"(D[j]) : "v"(vlin), "s"(hsrc + (grp) * 8192), "i"((j) * 2048 - 4096) : "memory")
  const unsigned wra = lds0 + (unsigned)(KB_WR + (kh * 32 + 4 * ((gq & 1) + 4 * (gq >> 1)) + ((lane & 15) >> 2)) * 160 + (lane & 3) * 8);
  const unsigned wrb = wra + KB_GRP;

  unsigned oo[3];
#pragma unroll
  for (int g = 0; g < 3; ++g) {
    const int Gc = head * 5 + 2 * g + h, G8 = Gc & 7;
    oo[g] = (unsigned)((r >> 4) * (20 * 1024) + (r & 15) * 16 + (Gc >> 3) * 1024 + ((G8 >> 2) * 2 + (G8 & 1)) * 256 + ((G8 >> 1) & 1) * 8);
    PIN(oo[g]);
  }
  bf16x8 hA[4], hB[4];
  asm volatile("s_waitcnt vmcnt(4)" : "+v"(qa), "+v"(qbv), "+v"(ka0), "+v"(kb0) :: "memory");
  f32x16 o;
#pragma unroll
  for (int i = 0; i < 16; ++i) o[i] = 0.f;
  const f32x16 zero = o;
  if (h == 1) { qbv[0] = 0u; qbv[1] = 0u; kb0[0] = 0x00003f80u; kb0[1] = 0u; }
  const f32x16 s16 = MFMA32(ka0, qa, zero);
  f32x16 sA = MFMA32K8(__builtin_bit_cast(bf16x4, kb0), __builtin_bit_cast(bf16x4, qbv), s16);
  {
    float m = fmaxf(fmaxf(sA[0], sA[1]), sA[2]);
#pragma unroll
    for (int i = 3; i < 15; i += 2) m = fmaxf(fmaxf(m, sA[i]), sA[i + 1]);
    m = fmaxf(m, sA[15]);
    const auto mm = __builtin_amdgcn_permlane32_swap(__float_as_uint(m), __float_as_uint(m), false, false);
    m = fmaxf(__uint_as_float(mm[0]), __uint_as_float(mm[1]));
    if (h == 1) qbv[0] = cvtpk(-m, 0.f);
  }
  const bf16x4 qb4 = __builtin_bit_cast(bf16x4, qbv);
  sA = MFMA32K8(__builtin_bit_cast(bf16x4, kb0), qb4, s16);
  f32x16 sB;
  f32x4 acc[5];
#pragma unroll
  for (int j = 0; j < 5; ++j) acc[j] = f32x4{0.f, 0.f, 0.f, 0.f};
  WAIT_BAR(0);
  STAMP(1, 1);
  STAMPC(1, 14);
  STAMP2(2, 0);

  unsigned a16 = lds0 + vlin, a8 = lds0 + (unsigned)lane * 8u;
  int sdelta = AT_SLOT;
  bf16x8 kaA, v0A, v1A, kaB, v0B, v1B;
  bf16x4 kbA, kbB;
  bf16x4 wl[5], wu[5];
  u32x4 p0, p1 = {0u, 0u, 0u, 0u};
  v1B = __builtin_bit_cast(bf16x8, p1);
#define RD_KA(X, j) ka##X = *(const LDS_AS bf16x8*)(LP(a16) + (j) * AT_TILE)
#define RD_KB(X, j) kb##X = *(const LDS_AS bf16x4*)(LP(a8) + (j) * AT_TILE + 1024)
#define RD_V0(X, j) v0##X = *(const LDS_AS bf16x8*)(LP(a16) + (j) * AT_TILE + 1536)
#define RD_V1(X, j) v1##X = *(const LDS_AS bf16x8*)(LP(a16) + (j) * AT_TILE + 2560)
#define W_LO(WA, j, t) wl[t] = tr_read(LP(WA) + (j) * 10240 + (t) * 32)
#define W_HI(WA, j, t) wu[t] = tr_read(LP(WA) + (j) * 10240 + (t) * 32 + 1280)
#define G_MM(HS, j, t) acc[t] = MFMA16(cat8(wl[t], wu[t]), HS[j], acc[t])
#define EX2(S, i) do { S[i] = __builtin_amdgcn_exp2f(S[i]); S[i + 1] = __builtin_amdgcn_exp2f(S[i + 1]); } while (0)
#define MID() do { __builtin_amdgcn_sched_group_barrier(0x400, 2, 0); __builtin_amdgcn_sched_group_barrier(0x008, 1, 0); \
                    __builtin_amdgcn_sched_group_barrier(0x400, 2, 0); __builtin_amdgcn_sched_group_barrier(0x002, 2, 0); } while (0)
#define AT_STEP(SC, SN, X, Y, JN, PRE, WP, A0, A1, A2, A3, A4, A5, V0_, V1_, V2_, M1, M2, M3)            \
  do {                                                                                                   \
    PRE;                                                                                                 \
    asm volatile("" : "+v"(ka##X), "+v"(kb##X), "+v"(v0##X), "+v"(v1##X)); WP; SBAR();                   \
    EX2(SC, 0); PIN(SC); SBAR();                                                                         \
    o = MFMA32(v1##Y, __builtin_bit_cast(bf16x8, p1), o); SBAR();                                        \
    RD_KA(Y, JN); RD_KB(Y, JN); RD_V0(Y, JN); RD_V1(Y, JN); A0; A1; A2; A3; A4; A5; V0_; V1_; SBAR(); \
    M1; EX2(SC, 2); EX2(SC, 4); p0[0] = cvtpk(SC[0], SC[1]); p0[1] = cvtpk(SC[2], SC[3]); MID();         \
    asm volatile("" : "+v"(p0), "+v"(SC), "+v"(acc[0]), "+v"(acc[3]), "+v"(o)); SBAR();                  \
    SN = MFMA32(ka##X, qa, zero); SBAR();                                                                \
    M2; EX2(SC, 6); EX2(SC, 8); p0[2] = cvtpk(SC[4], SC[5]); p0[3] = cvtpk(SC[6], SC[7]); MID();         \
    asm volatile("" : "+v"(p0), "+v"(SC), "+v"(acc[1])); SBAR();                                         \
    SN = MFMA32K8(kb##X, qb4, SN); SBAR();                                                               \
    V2_; SBAR();                                                                                         \
    M3; EX2(SC, 10); EX2(SC, 12); p1[0] = cvtpk(SC[8], SC[9]); p1[1] = cvtpk(SC[10], SC[11]); MID();     \
    asm volatile("" : "+v"(p1), "+v"(SC), "+v"(acc[2]), "+v"(acc[4])); SBAR();                           \
    o = MFMA32(v0##X, __builtin_bit_cast(bf16x8, p0), o); SBAR();                                        \
    EX2(SC, 14); p1[2] = cvtpk(SC[12], SC[13]); p1[3] = cvtpk(SC[14], SC[15]);                           \
    asm volatile("" : "+v"(p1), "+v"(o)); SBAR();                                                        \
  } while (0)
#define WP_E asm volatile("" : "+v"(wl[0]), "+v"(wu[0]), "+v"(wl[1]), "+v"(wu[1]), "+v"(wl[2]), "+v"(wu[2]))
#define WP_O asm volatile("" : "+v"(wl[3]), "+v"(wu[3]), "+v"(wl[4]), "+v"(wu[4]))
#define FLIP() do { a16 += (unsigned)sdelta; a8 += (unsigned)sdelta; sdelta = -sdelta; } while (0)
#define SYNC_H(HN) asm volatile("s_waitcnt vmcnt(3) lgkmcnt(0)\n\ts_barrier" : "+v"(HN[0]) :: "memory")
#define WAIT_H(HC, j, N) asm volatile("s_waitcnt vmcnt(" #N ")" : "+v"(HC[j]) :: "memory")
#define STEP_E(JN, PRE, WC, HC, j, VA, VB)                                                               \
  AT_STEP(sA, sB, A, B, JN, PRE, WP_E, W_LO(WC, j, 3), W_HI(WC, j, 3), W_LO(WC, j, 4), W_HI(WC, j, 4), NOP_, NOP_, NOP_, VA, VB, \
          G_MM(HC, j, 0), G_MM(HC, j, 1), G_MM(HC, j, 2))
#define STEP_O(JN, PRE, WA, jn, HC, j, VA, VB)                                                           \
  AT_STEP(sB, sA, B, A, JN, PRE, WP_O, W_LO(WA, jn, 0), W_HI(WA, jn, 0), W_LO(WA, jn, 1), W_HI(WA, jn, 1), W_LO(WA, jn, 2), \
          W_HI(WA, jn, 2), NOP_, VA, VB, G_MM(HC, j, 3), NOP_, G_MM(HC, j, 4))
#define CHUNK_G(c, WC, HC, WN, HN, RN, NW, NG, HG)                                                       \
  do {                                                                                                   \
    STEP_E(1, NOP_, WC, HC, 0, W_M0(RN, 0); W_P(NW, 0, NG), W_P(NW, 1, NG));                             \
    STEP_O(2, NOP_, WC, 1, HC, 0, W_P(NW, 2, NG), W_P(NW, 3, NG));                                       \
    STEP_E(3, WAIT_H(HC, 1, 6), WC, HC, 1, W_M0(RN, 1); W_P(NW, 4, NG), KV_M0((c) + 1); KV_P0((c) + 1)); \
    STEP_O(4, NOP_, WC, 2, HC, 1, KV_P1((c) + 1), KV_P23((c) + 1));                                      \
    STEP_E(5, WAIT_H(HC, 2, 10), WC, HC, 2, H_LOAD(HN, 0, HG), H_LOAD(HN, 1, HG));                       \
    STEP_O(6, NOP_, WC, 3, HC, 2, H_LOAD(HN, 2, HG), H_LOAD(HN, 3, HG));                                 \
    STEP_E(7, WAIT_H(HC, 3, 13), WC, HC, 3, NOP_, NOP_);                                                 \
    STEP_O(0, SYNC_H(HN); STAMP2(2, 1 + (c)); FLIP(), WN, 0, HC, 3, NOP_, NOP_);                         \
  } while (0)
#define PLAIN2_E(JN, VA, VB) AT_STEP(sA, sB, A, B, JN, NOP_, NOP_, NOP_, NOP_, NOP_, NOP_, NOP_, NOP_, NOP_, VA, VB, NOP_, NOP_, NOP_)
#define PLAIN2_O(JN, VA, VB) AT_STEP(sB, sA, B, A, JN, NOP_, NOP_, NOP_, NOP_, NOP_, NOP_, NOP_, NOP_, NOP_, VA, VB, NOP_, NOP_, NOP_)
#define PLAIN_E(JN, X0, X1, X2, X3) AT_STEP(sA, sB, A, B, JN, NOP_, NOP_, X0, X1, X2, X3, NOP_, NOP_, NOP_, NOP_, NOP_, NOP_, NOP_, NOP_)
#define PLAIN_O(JN, X0, X1, X2, X3) AT_STEP(sB, sA, B, A, JN, NOP_, NOP_, X0, X1, X2, X3, NOP_, NOP_, NOP_, NOP_, NOP_, NOP_, NOP_, NOP_)

  RD_KA(A, 0); RD_KB(A, 0); RD_V0(A, 0); RD_V1(A, 0);
  PLAIN2_E(1, W_M0(0, 0); W_P(w2, 0, 0), W_P(w2, 1, 0));
  PLAIN2_O(2, W_P(w2, 2, 0), W_P(w2, 3, 0));
  PLAIN2_E(3, W_M0(0, 1); W_P(w2, 4, 0), KV_M0(1); KV_P0(1));
  PLAIN2_O(4, KV_P1(1), KV_P23(1));
  PLAIN2_E(5, H_LOAD(hA, 0, 0), H_LOAD(hA, 1, 0));
  PLAIN2_O(6, H_LOAD(hA, 2, 0), H_LOAD(hA, 3, 0));
  PLAIN_E(7, NOP_, NOP_, NOP_, NOP_);
  AT_STEP(sB, sA, B, A, 0, SYNC_H(hA); STAMP2(2, 1); FLIP(), NOP_, W_LO(wra, 0, 0), W_HI(wra, 0, 0), W_LO(wra, 0, 1), W_HI(wra, 0, 1),
          W_LO(wra, 0, 2), W_HI(wra, 0, 2), NOP_, NOP_, NOP_, NOP_, NOP_, NOP_);
#pragma unroll 1
  for (int c = 1; c < 5; c += 2) {
    CHUNK_G(c, wra, hA, wrb, hB, 1, w2, c, c);
    CHUNK_G(c + 1, wrb, hB, wra, hA, 0, w2, c + 1, c + 1);
  }
  CHUNK_G(5, wra, hA, wrb, hB, 1, w2, 5, 5);
  const int tok = rbk * 64 + mi * 16 + (lane & 15);
  const unsigned xo = (unsigned)(tok * 1280 + (cbk * 80 + kh * 48 + 4 * gq) * 2);
  const int j2 = kh ? 1 : 2;
  LDS_AS f32x4* red = (LDS_AS f32x4*)(lds + KB_RED) + (mi * 5) * 64 + lane;
  u32x2 gv[3], xv[3];
  PLAIN2_E(1, KV_M0(7); KV_P0(7), KV_P1(7));
  PLAIN2_O(2, KV_P23(7), W_M0(0, 0); W_P(wo, 0, 0));
  PLAIN2_E(3, W_P(wo, 1, 0), W_P(wo, 2, 0));
  PLAIN2_O(4, W_P(wo, 3, 0), W_M0(0, 1); W_P(wo, 4, 0));
  PLAIN2_E(5, NOP_, NOP_);
  PLAIN2_O(6, NOP_, NOP_);
  PLAIN2_E(7, NOP_, NOP_);
  AT_STEP(sB, sA, B, A, 0,
          asm volatile("s_waitcnt vmcnt(0) lgkmcnt(0)\n\ts_barrier" : "+v"(hB[1]), "+v"(hB[2]), "+v"(hB[3]) :: "memory"); STAMP2(2, 7); FLIP(),
          NOP_, NOP_, NOP_, NOP_, NOP_, NOP_, NOP_, NOP_, NOP_, NOP_, NOP_, NOP_, NOP_);
  PLAIN2_E(1, _Pragma("unroll") for (int j = 0; j < 3; ++j) gv[j] = *(const u32x2*)(b2 + cbk * 80 + kh * 48 + (j < 2 ? j : j2) * 16 + 4 * gq), NOP_);
  PLAIN2_O(2, _Pragma("unroll") for (int j = 0; j < 3; ++j) xv[j] = *(const u32x2*)(x + xo + (j < 2 ? j : j2) * 32), NOP_);
  PLAIN2_E(3, NOP_, NOP_);
  PLAIN2_O(4, NOP_, NOP_);
  PLAIN2_E(5, _Pragma("unroll") for (int j = 0; j < 3; ++j) { PIN(gv[j]); PIN(xv[j]); }
           SET_M0(lds0 + (unsigned)(wid * 2560));
           asm volatile("global_load_lds_dwordx4 %0, %3\n\tglobal_load_lds_dwordx4 %1, %3 offset:1024\n\t"
                        "s_mov_b32 exec_hi, 0\n\tglobal_load_lds_dwordx4 %2, %3 offset:2048\n\ts_mov_b32 exec_hi, -1"
                        :: "v"(vo[0]), "v"(vo[1]), "v"(vo[2]), "s"(wo + cbk * 160 + (size_t)(256 + wid * 16) * 1280) : "memory"), NOP_);
  PLAIN2_O(6, NOP_, NOP_);
  PLAIN2_E(7, NOP_, NOP_);
  EX2(sB, 0); PIN(sB); SBAR();
  o = MFMA32(v1A, __builtin_bit_cast(bf16x8, p1), o); SBAR();
  EX2(sB, 2); EX2(sB, 4); EX2(sB, 6);
  p0 = u32x4{cvtpk(sB[0], sB[1]), cvtpk(sB[2], sB[3]), cvtpk(sB[4], sB[5]), cvtpk(sB[6], sB[7])};
  EX2(sB, 8); asm volatile("" : "+v"(p0), "+v"(sB)); SBAR();
  o = MFMA32(v0B, __builtin_bit_cast(bf16x8, p0), o); SBAR();
  EX2(sB, 10); EX2(sB, 12); EX2(sB, 14);
  p1 = u32x4{cvtpk(sB[8], sB[9]), cvtpk(sB[10], sB[11]), cvtpk(sB[12], sB[13]), cvtpk(sB[14], sB[15])};
  o = MFMA32(v1B, __builtin_bit_cast(bf16x8, p1), o);

  STAMP(1, 3);
  STAMPC(1, 15);
  STAMP2(2, 8);
  float l = __uint_as_float(__builtin_amdgcn_permlane32_swap(__float_as_uint(o[8]), __float_as_uint(o[8]), false, false)[1]);
  float big = fmaxf(fmaxf(__builtin_fabsf(o[0]), __builtin_fabsf(o[1])), l);
#pragma unroll
  for (int i = 2; i < 12; i += 2) big = fmaxf(fmaxf(big, __builtin_fabsf(o[i])), __builtin_fabsf(o[i + 1]));
  bool unsafe = !(big < 1.0e38f) || !(l > 0.25f);
#pragma unroll
  for (int i = 0; i < 12; ++i) unsafe |= o[i] != o[i];
  if (__builtin_expect(__any(unsafe), 0)) {
    if (h == 1) { qbv[0] = 0u; }
    const bf16x4 qz = __builtin_bit_cast(bf16x4, qbv);
    float m = -3.0e38f;
#pragma unroll
    for (int i = 0; i < 16; ++i) o[i] = 0.f;
    const unsigned onev = r == 20 ? 0x3f803f80u : 0u;
#pragma unroll 1
    for (int t = 0; t < 64; ++t) {
      const bf16x8 ka = *(const bf16x8*)(Kh + t * 1280 + lane * 16);
      u32x2 kb = *(const u32x2*)(Kh + t * 1280 + 1024 + r * 8);
      u32x4 v0 = {onev, onev, onev, onev}, v1 = v0;
      if (r < 20) {
        v0 = *(const u32x4*)(Vh + t * 1280 + vv);
        v1 = *(const u32x4*)(Vh + t * 1280 + 640 + vv);
      }
      f32x16 s = MFMA32(ka, qa, zero);
      s = MFMA32K8(__builtin_bit_cast(bf16x4, kb), qz, s);
      float tm = s[0];
#pragma unroll
      for (int i = 1; i < 16; ++i) tm = fmaxf(tm, s[i]);
      tm = fmaxf(tm, __shfl_xor(tm, 32));
      const float mn = fmaxf(m, tm);
      const float al = __builtin_amdgcn_exp2f(m - mn);
      m = mn;
      u32x4 q0, q1;
#pragma unroll
      for (int i = 0; i < 16; ++i) { o[i] *= al; s[i] = __builtin_amdgcn_exp2f(s[i] - mn); }
#pragma unroll
      for (int e = 0; e < 4; ++e) { q0[e] = cvtpk(s[2 * e], s[2 * e + 1]); q1[e] = cvtpk(s[8 + 2 * e], s[9 + 2 * e]); }
      o = MFMA32(__builtin_bit_cast(bf16x8, v0), __builtin_bit_cast(bf16x8, q0), o);
      o = MFMA32(__builtin_bit_cast(bf16x8, v1), __builtin_bit_cast(bf16x8, q1), o);
    }
    l = __shfl(o[8], 32 + r);
  }
  {
    const float inv = __builtin_amdgcn_rcpf(l);
    const char* ob = ws + WS_O + (size_t)(qb * 16 + wid * 2) * (20 * 1024);
#pragma unroll
    for (int g = 0; g < 3; ++g)
      if (g < 2 || h == 0)
        st8s(ob, oo[g], u32x2{min(cvtpk(o[4 * g] * inv, o[4 * g + 1] * inv), 0xfffffffeu),
                              min(cvtpk(o[4 * g + 2] * inv, o[4 * g + 3] * inv), 0xfffffffeu)});
  }
  STAMP(1, 4);
  {
    bf16x8 wf[2][5];
#pragma unroll
    for (int t = 0; t < 5; ++t) wf[0][t] = cat8(tr_read(LP(wrb) + t * 32), tr_read(LP(wrb) + t * 32 + 1280));
#pragma unroll
    for (int j = 0; j < 4; ++j) {
      SBAR();
      if (j < 3) {
#pragma unroll
        for (int t = 0; t < 5; ++t)
          wf[(j + 1) & 1][t] = cat8(tr_read(LP(wrb) + (j + 1) * 10240 + t * 32), tr_read(LP(wrb) + (j + 1) * 10240 + t * 32 + 1280));
      }
      SBAR();
#pragma unroll
      for (int t = 0; t < 5; ++t) acc[t] = MFMA16(wf[j & 1][t], hB[j], acc[t]);
      asm volatile("" : "+v"(acc[0]), "+v"(acc[1]), "+v"(acc[2]), "+v"(acc[3]), "+v"(acc[4]));
    }
    asm volatile("s_nop 7" : "+v"(acc[0]), "+v"(acc[1]), "+v"(acc[2]), "+v"(acc[3]), "+v"(acc[4]));
    if (kh == 1) { red[0] = acc[0]; red[64] = acc[1]; red[128] = acc[2]; } else { red[192] = acc[3]; red[256] = acc[4]; }
    asm volatile("s_waitcnt vmcnt(3) lgkmcnt(0)\n\ts_barrier" ::: "memory");
    const char* s_ = wo + cbk * 160 + (size_t)(384 + wid * 32) * 1280;
    W_M0(1, 0);
    asm volatile("global_load_lds_dwordx4 %0, %4\n\tglobal_load_lds_dwordx4 %1, %4 offset:1024\n\t"
                 "global_load_lds_dwordx4 %2, %4 offset:2048\n\tglobal_load_lds_dwordx4 %3, %4 offset:3072"
                 :: "v"(vo[0]), "v"(vo[1]), "v"(vo[2]), "v"(vo[3]), "s"(s_) : "memory");
    W_M0(1, 1);
    asm volatile("global_load_lds_dwordx4 %0, %1" :: "v"(vo[4]), "s"(s_) : "memory");
#define G_FIN(j, i)                                                                                       \
  do {                                                                                                    \
    const f32x4 tt = acc[j] + og[i] + f32x4{bf_lo(gv[i][0]), bf_hi(gv[i][0]), bf_lo(gv[i][1]), bf_hi(gv[i][1])}; \
    float t[4];                                                                                           \
    _Pragma("unroll") for (int e = 0; e < 4; ++e)                                                         \
      t[e] = tt[e] * __builtin_amdgcn_rcpf(1.0f + __builtin_amdgcn_exp2f(tt[e] * -1.4426950408889634f));  \
    gv[i] = u32x2{cvtpk(t[0], t[1]), cvtpk(t[2], t[3])};                                                  \
    PIN(gv[i]);                                                                                           \
  } while (0)
    f32x4 og[3];
    if (kh == 0) { og[0] = red[0]; og[1] = red[64]; og[2] = red[128]; G_FIN(0, 0); G_FIN(1, 1); G_FIN(2, 2); }
    else { og[0] = red[192]; og[1] = red[256]; G_FIN(3, 0); G_FIN(4, 1); }
#undef G_FIN
    SBAR();
  }
  {
    const srd_t ro = make_srd(ws + WS_O, (unsigned)(WS_G - WS_O));
    const unsigned oa = (unsigned)(((rbk * 4 + mi) * 20 + kh) * 1024);
    u32x4 af[10];
#pragma unroll
    for (int c = 0; c < 10; ++c) af[c] = bload16_dev(ro, vlin, oa + c * 2048u);
    SBAR();
#define WO_RD(D, c)                                                                                       \
  do {                                                                                                    \
    const unsigned wa_ = (c) < 4 ? wra + (unsigned)((c) * 10240) : (c) < 6 ? wra - (unsigned)KB_WR + (unsigned)(((c) - 4) * 10240) \
                                                                           : wrb + (unsigned)(((c) - 6) * 10240); \
    _Pragma("unroll") for (int t = 0; t < 5; ++t) D[t] = cat8(tr_read(LP(wa_) + t * 32), tr_read(LP(wa_) + t * 32 + 1280)); \
  } while (0)
    bf16x8 wfa[5][5], wfb[5][5];
#pragma unroll
    for (int c = 0; c < 5; ++c) WO_RD(wfa[c], c);
    SBAR();
#pragma unroll
    for (int j = 0; j < 5; ++j) acc[j] = f32x4{0.f, 0.f, 0.f, 0.f};
    STAMP(1, 6);
    for (unsigned spins = 0; spins < (1u << 15); ++spins) {
      unsigned mx = 0u;
#pragma unroll
      for (int c = 0; c < 10; ++c) mx = max(max(max(mx, af[c][0]), af[c][1]), max(af[c][2], af[c][3]));
      if (!__any(mx == 0xffffffffu)) break;
      __builtin_amdgcn_s_sleep(4);
      asm volatile("" ::: "memory");
#pragma unroll
      for (int c = 0; c < 10; ++c) af[c] = bload16_dev(ro, vlin, oa + c * 2048u);
    }
    STAMP(1, 7);
    LBAR(0);
#pragma unroll
    for (int c = 5; c < 8; ++c) WO_RD(wfb[c - 5], c);
    SBAR();
#pragma unroll
    for (int c = 0; c < 5; ++c)
#pragma unroll
      for (int t = 0; t < 5; ++t) acc[t] = MFMA16(wfa[c][t], __builtin_bit_cast(bf16x8, af[c]), acc[t]);
#pragma unroll
    for (int c = 8; c < 10; ++c) WO_RD(wfb[c - 5], c);
#pragma unroll
    for (int c = 5; c < 10; ++c)
#pragma unroll
      for (int t = 0; t < 5; ++t) acc[t] = MFMA16(wfb[c - 5][t], __builtin_bit_cast(bf16x8, af[c]), acc[t]);
#undef WO_RD
  }
  if (kh == 1) { red[0] = acc[0]; red[64] = acc[1]; red[128] = acc[2]; } else { red[192] = acc[3]; red[256] = acc[4]; }
  LBAR(0);
#define O_FIN(j, i)                                                                                       \
  do {                                                                                                    \
    const f32x4 sv = acc[j] + red[(j) * 64];                                                              \
    const float r0 = bf_lo(xv[i][0]) + sv[0] * bf_lo(gv[i][0]), r1 = bf_hi(xv[i][0]) + sv[1] * bf_hi(gv[i][0]); \
    const float r2 = bf_lo(xv[i][1]) + sv[2] * bf_lo(gv[i][1]), r3 = bf_hi(xv[i][1]) + sv[3] * bf_hi(gv[i][1]); \
    st8s(out, xo + (i) * 32, u32x2{cvtpk(r0, r1), cvtpk(r2, r3)});                                        \
  } while (0)
  if (kh == 0) { O_FIN(0, 0); O_FIN(1, 1); O_FIN(2, 2); } else { O_FIN(3, 0); O_FIN(4, 1); }
#undef O_FIN
  STAMP(1, 8);
}

extern "C" void kernel_launch(void* const* d_in, const int* in_sizes, int n_in, void* d_out, int out_size, void* d_ws,
                              size_t ws_size, hipStream_t stream) {
  (void)in_sizes; (void)n_in; (void)out_size; (void)ws_size;
  const char* x = (const char*)d_in[0];
  const char* wqkv = (const char*)d_in[1];
  const char* wo = (const char*)d_in[2];
  const char* w1 = (const char*)d_in[4];
  const unsigned short* b1 = (const unsigned short*)d_in[5];
  const char* w2 = (const char*)d_in[6];
  const unsigned short* b2 = (const unsigned short*)d_in[7];
  char* ws = (char*)d_ws;
  (void)hipFuncSetAttribute((const void*)ka_proj, hipFuncAttributeMaxDynamicSharedMemorySize, KA_LDS);
  (void)hipFuncSetAttribute((const void*)kb_main, hipFuncAttributeMaxDynamicSharedMemorySize, KB_LDS);
  ka_proj<<<256, 512, KA_LDS, stream>>>(x, wqkv, w1, b1, w2, wo, ws);
  kb_main<<<256, 512, KB_LDS, stream>>>(x, w2, b2, wo, ws, (char*)d_out);
}
```

```cpp
#include <hip/hip_runtime.h>
#include <hip/hip_bf16.h>
#include <stdint.h>

typedef __attribute__((ext_vector_type(8))) short bf16x8;
typedef __attribute__((ext_vector_type(4))) short bf16x4;
typedef __attribute__((ext_vector_type(16))) float f32x16;
typedef __attribute__((ext_vector_type(4))) float f32x4;
typedef __attribute__((ext_vector_type(2))) float f32x2;
typedef __attribute__((ext_vector_type(4))) unsigned u32x4;
typedef __attribute__((ext_vector_type(2))) unsigned u32x2;
typedef __attribute__((ext_vector_type(2))) __bf16 bf2;
typedef __attribute__((address_space(3))) char* lds_ptr;
#define LDS_AS __attribute__((address_space(3)))
#define DI __device__ __forceinline__

#define MFMA32(a, b, c) __builtin_amdgcn_mfma_f32_32x32x16_bf16((a), (b), (c), 0, 0, 0)
#define MFMA32K8(a, b, c) __builtin_amdgcn_mfma_f32_32x32x8bf16_1k((a), (b), (c), 0, 0, 0)
#define MFMA16(a, b, c) __builtin_amdgcn_mfma_f32_16x16x32_bf16((a), (b), (c), 0, 0, 0)
#define SBAR() __builtin_amdgcn_sched_barrier(0)
#define PIN(x) asm volatile("" : "+v"(x))
#define WAIT_BAR(N) asm volatile("s_waitcnt vmcnt(" #N ") lgkmcnt(0)\n\ts_barrier" ::: "memory")

constexpr int D_ = 640, NH = 32;
constexpr float LN_EPS = 1e-5f;
constexpr float QSCALE = 0.22360679774997896f * 1.4426950408889634f;

constexpr size_t WS_Q = 0;
constexpr size_t WS_K = WS_Q + (size_t)NH * 64 * 1280;
constexpr size_t WS_V = WS_K + (size_t)NH * 64 * 1280;
constexpr size_t WS_H = WS_V + (size_t)NH * 64 * 1280;
constexpr size_t WS_O = WS_H + (size_t)128 * 48 * 1024;
constexpr size_t WS_G = WS_O + (size_t)128 * 20 * 1024;

#define STAMP(kern, i) do { } while (0)
#define STAMPC(kern, i) do { } while (0)
#define STAMP2(kern, i) do { } while (0)

DI void st16s(const void* sb, unsigned off, u32x4 v) { asm volatile("global_store_dwordx4 %0, %1, %2 sc1\n\ts_nop 1" :: "v"(off), "v"(v), "s"(sb) : "memory"); }
DI void st8s(const void* sb, unsigned off, u32x2 v) { asm volatile("global_store_dwordx2 %0, %1, %2 sc1" :: "v"(off), "v"(v), "s"(sb) : "memory"); }
DI unsigned cvtpk(float lo, float hi) {
  f32x2 f = {lo, hi};
  return __builtin_bit_cast(unsigned, __builtin_convertvector(f, bf2));
}
DI float bf_lo(unsigned u) { return __uint_as_float(u << 16); }
DI float bf_hi(unsigned u) { return __uint_as_float(u & 0xffff0000u); }
typedef short v4i16_t __attribute__((ext_vector_type(4)));
DI bf16x4 tr_read(lds_ptr p) {
  return __builtin_bit_cast(bf16x4, __builtin_amdgcn_ds_read_tr16_b64_v4i16((LDS_AS v4i16_t*)p));
}
DI bf16x8 cat8(bf16x4 a, bf16x4 b) { return __builtin_shufflevector(a, b, 0, 1, 2, 3, 4, 5, 6, 7); }

constexpr int KA_BUF = 45056;
constexpr int KA_STAT = 3 * KA_BUF;
constexpr int KA_SCS = KA_STAT + 2048, KA_SB1 = KA_SCS + 3584, KA_SMR = KA_SB1 + 384;
constexpr int KA_LDS = KA_SMR + 2048;
#define LBAR(N) asm volatile("s_waitcnt lgkmcnt(" #N ")\n\ts_barrier" ::: "memory")
#define LP(a) ((lds_ptr)(uintptr_t)(a))
#define SET_M0(v) asm volatile("s_mov_b32 m0, %0\n\ts_nop 0" :: "s"(v) : "memory")
#define NOP_ do { } while (0)
#define MFMA4(a, b, c) __builtin_amdgcn_mfma_f32_4x4x4bf16_1k((a), (b), (c), 0, 0, 0)

typedef __amdgpu_buffer_rsrc_t srd_t;
DI srd_t make_srd(const void* p, unsigned bytes) { return __builtin_amdgcn_make_buffer_rsrc((void*)p, (short)0, (int)bytes, 0x00020000); }
DI float x32_add(float v) {
  const auto rr = __builtin_amdgcn_permlane32_swap(__float_as_uint(v), __float_as_uint(v), false, false);
  return __uint_as_float(rr[0]) + __uint_as_float(rr[1]);
}
DI bf16x4 lo4(bf16x8 v) { return __builtin_shufflevector(v, v, 0, 1, 2, 3); }
DI bf16x4 hi4(bf16x8 v) { return __builtin_shufflevector(v, v, 4, 5, 6, 7); }

template <int TYPE, int nh>
DI void ka_body(lds_ptr lds, const char* __restrict__ x, const char* __restrict__ wqkv, const char* __restrict__ wh1,
                const unsigned short* __restrict__ b1, const char* __restrict__ w2, const char* __restrict__ wo,
                const int n0, const int f0, const int rb, const int cb, char* __restrict__ ws) {
  constexpr int NHT = nh ? 1 : 2;
  const int tid = threadIdx.x, lane = tid & 63;
  const int w = __builtin_amdgcn_readfirstlane(tid >> 6);
  const int mi = w & 3;
  const int r = lane & 31, h = lane >> 5;
  const unsigned lds0 = (unsigned)(uintptr_t)lds;
  STAMP(0, 0);
  const unsigned vx0 = (unsigned)((lane >> 3) * 1280 + (((lane & 7) ^ (lane >> 4)) << 4));
  const unsigned vx1 = (unsigned)((8 + (lane >> 3)) * 1280 + (((lane & 7) ^ (4 + (lane >> 4))) << 4) - 1024);
  const unsigned vw = (unsigned)((lane >> 3) * 3840 + ((((((lane >> 2) ^ (lane >> 4)) & 1) << 2) | (lane & 3)) << 4));
  const int p0 = w * 96 + lane, p1 = w * 96 + 64 + r;
  const int r0 = (p0 * 5462) >> 16, r1 = (p1 * 5462) >> 16;
  const unsigned vh0 = (unsigned)(r0 * 3072 + (p0 - r0 * 12) * 16), vh1 = (unsigned)(r1 * 3072 + (p1 - r1 * 12) * 16 - 1024);
  const char* xs = x + (size_t)(rb * 128 + 16 * w) * 1280;
  const char* wsrc = wqkv + ((size_t)(8 * w) * 1920 + n0) * 2;
  const char* hsrc = wh1 + f0 * 2;
  const unsigned mx = lds0 + (unsigned)w * 2048u, mw = lds0 + 16384u + (unsigned)w * 1024u, mh = lds0 + 32768u + (unsigned)w * 1536u;
#define KA_DX(c, buf) do { SET_M0(mx + (unsigned)(buf)); asm volatile("global_load_lds_dwordx4 %0, %2\n\tglobal_load_lds_dwordx4 %1, %2 offset:1024" :: "v"(vx0), "v"(vx1), "s"(xs + (c) * 128) : "memory"); } while (0)
#define KA_DW(c, buf, hf) do { SET_M0(mw + (unsigned)(buf) + (hf) * 8192u); asm volatile("global_load_lds_dwordx4 %0, %1" :: "v"(vw), "s"(wsrc + (size_t)(c) * (64 * 3840) + (hf) * 128) : "memory"); } while (0)
#define KA_DH(c, buf) do { SET_M0(mh + (unsigned)(buf)); asm volatile("global_load_lds_dwordx4 %0, %2\n\ts_mov_b32 exec_hi, 0\n\tglobal_load_lds_dwordx4 %1, %2 offset:1024\n\ts_mov_b32 exec_hi, -1" :: "v"(vh0), "v"(vh1), "s"(hsrc + (size_t)(c) * (64 * 3072)) : "memory"); } while (0)
#define KA_ISSUE(c, buf) do { KA_DX(c, buf); KA_DW(c, buf, 0); KA_DW(c, buf, 1); KA_DH(c, buf); } while (0)
  unsigned braw = 0, warm = 0;
  if (nh == 0 && tid < 96) braw = b1[f0 + tid];
  SBAR();
  KA_ISSUE(0, 0);
  KA_ISSUE(1, KA_BUF);

  unsigned xa[4], wa0[4], ha[4];
  {
    const unsigned wb = lds0 + (unsigned)(16384 + nh * 8192 + (8 * h + ((lane & 15) >> 2)) * 128 + 64 * ((lane >> 3) & 1) +
                                          (16 * ((lane >> 4) & 1) + 4 * (lane & 3)) * 2);
    const unsigned hb = lds0 + (unsigned)(32768 + (8 * h + ((lane & 15) >> 2)) * 192 + nh * 128 + (16 * ((lane >> 4) & 1) + 4 * (lane & 3)) * 2);
#pragma unroll
    for (int i = 0; i < 4; ++i) {
      const int ks = i ^ mi;
      xa[i] = lds0 + (unsigned)((mi * 32 + r) * 128 + (((2 * ks + h) ^ ((r >> 1) & 7)) << 4));
      wa0[i] = wb + (unsigned)(ks * 2048);
      ha[i] = hb + (unsigned)(ks * 3072);
    }
  }
  const int tq = rb * 4 + mi;
  const bool isq = n0 < 640;
  unsigned so[TYPE == 1 ? 2 : 8];
  if (TYPE == 1) {
#pragma unroll
    for (int j = 0; j < 2; ++j) {
      const int nn = n0 - 1280 + nh * 64 + j * 32 + r;
      const int head = (nn * 3277) >> 16;
      so[j] = (unsigned)(head * (64 * 1280) + (h * 20 + nn - head * 20) * 16);
      PIN(so[j]);
    }
  } else {
#pragma unroll
    for (int j = 0; j < 8; ++j) {
      const int G = (((isq ? n0 : n0 - 640) + nh * 64) >> 2) + h + 2 * j;
      const int head = (G * 205) >> 10;
      const int dg = G - head * 5;
      so[j] = (unsigned)(head * (64 * 1280) + (dg < 4 ? (dg >> 1) * 512 + r * 16 + (dg & 1) * 8 : 1024 + r * 8));
      PIN(so[j]);
    }
  }
  f32x16 acc[2], ach[NHT];
  f32x4 cq[2], chh[NHT], rsum = {0.f, 0.f, 0.f, 0.f}, rsq = rsum;
#pragma unroll
  for (int i = 0; i < 16; ++i) {
    acc[0][i] = 0.f; acc[1][i] = 0.f;
#pragma unroll
    for (int j = 0; j < NHT; ++j) ach[j][i] = 0.f;
  }
  cq[0] = rsum; cq[1] = rsum;
#pragma unroll
  for (int j = 0; j < NHT; ++j) chh[j] = rsum;
  bf16x8 sel;
  {
    const short sv = (lane & 15) == ((lane >> 4) & 1) ? (short)0x3f80 : (short)0;
    sel = bf16x8{sv, sv, sv, sv, sv, sv, sv, sv};
    PIN(sel);
  }
  bf16x8 xf[3], w0[3], w1[3], wh[3][NHT];
  unsigned wa1[4];
#pragma unroll
  for (int i = 0; i < 4; ++i) wa1[i] = wa0[i] ^ 64u;
#define KA_READ(B, i)                                                                                           \
  do {                                                                                                          \
    constexpr int s_ = ((B) + (i)) % 3;                                                                         \
    xf[s_] = *(const LDS_AS bf16x8*)(LP(xa[i]) + BUFOFF(B));                                                    \
    w0[s_] = cat8(tr_read(LP(wa0[i]) + BUFOFF(B)), tr_read(LP(wa0[i]) + BUFOFF(B) + 512));                      \
    w1[s_] = cat8(tr_read(LP(wa1[i]) + BUFOFF(B)), tr_read(LP(wa1[i]) + BUFOFF(B) + 512));                      \
    _Pragma("unroll") for (int j_ = 0; j_ < NHT; ++j_)                                                          \
      wh[s_][j_] = cat8(tr_read(LP(ha[i]) + BUFOFF(B) + j_ * 64), tr_read(LP(ha[i]) + BUFOFF(B) + j_ * 64 + 768)); \
  } while (0)
#define SGB(m, n) __builtin_amdgcn_sched_group_barrier((m), (n), 0)
#define KA_STEP(B, i, RD) KA_STEPX(B, i, RD, true)
#define KA_STEPX(B, i, RD, ROWS)                                                                                \
  do {                                                                                                          \
    constexpr int b_ = ((B) + (i)) % 3;                                                                         \
    if constexpr (NHT == 2) asm volatile("" : "+v"(xf[b_]), "+v"(w0[b_]), "+v"(w1[b_]), "+v"(wh[b_][0]), "+v"(wh[b_][NHT - 1])); \
    else asm volatile("" : "+v"(xf[b_]), "+v"(w0[b_]), "+v"(w1[b_]), "+v"(wh[b_][0]));                          \
    SBAR();                                                                                                     \
    RD;                                                                                                         \
    if (TYPE == 1) { acc[0] = MFMA32(xf[b_], w0[b_], acc[0]); acc[1] = MFMA32(xf[b_], w1[b_], acc[1]); }        \
    else { acc[0] = MFMA32(w0[b_], xf[b_], acc[0]); acc[1] = MFMA32(w1[b_], xf[b_], acc[1]); }                  \
    _Pragma("unroll") for (int j_ = 0; j_ < NHT; ++j_) ach[j_] = MFMA32(wh[b_][j_], xf[b_], ach[j_]);           \
    SGB(0x8, 1); SGB(0x100, 3); SGB(0x8, 1); SGB(0x100, 2); SGB(0x8, 1); SGB(0x100, 2);                         \
    if (NHT == 2) { SGB(0x8, 1); SGB(0x100, 2); }                                                               \
    if constexpr (NHT == 2) asm volatile("" : "+v"(acc[0]), "+v"(acc[1]), "+v"(ach[0]), "+v"(ach[NHT - 1]));    \
    else asm volatile("" : "+v"(acc[0]), "+v"(acc[1]), "+v"(ach[0]));                                           \
    SBAR();                                                                                                     \
    if ((i) == 0) {                                                                              \
      cq[0] = MFMA16(w0[b_], sel, cq[0]); cq[1] = MFMA16(w1[b_], sel, cq[1]);                                   \
      _Pragma("unroll") for (int j_ = 0; j_ < NHT; ++j_) chh[j_] = MFMA16(wh[b_][j_], sel, chh[j_]);            \
      if constexpr (NHT == 2) asm volatile("" : "+v"(cq[0]), "+v"(cq[1]), "+v"(chh[0]), "+v"(chh[NHT - 1]));    \
      else asm volatile("" : "+v"(cq[0]), "+v"(cq[1]), "+v"(chh[0]));                                           \
    }                                                                                                           \
    if ((ROWS) && (nh ? (i) < 2 : (i) >= 2)) {                                                   \
      rsq = MFMA4(lo4(xf[b_]), lo4(xf[b_]), rsq); rsum = MFMA16(xf[b_], sel, rsum);                             \
      rsq2 = MFMA4(hi4(xf[b_]), hi4(xf[b_]), rsq2);                                                             \
      asm volatile("" : "+v"(rsum), "+v"(rsq), "+v"(rsq2));                                                     \
    }                                                                                                           \
    SBAR();                                                                                                     \
  } while (0)
#define BUFOFF(B) ((B) * KA_BUF)
#define KA_CHUNK(c, B, BN, WAITN, PREV, MORE)                                                                   \
  do {                                                                                                          \
    KA_STEP(B, 0, KA_READ(B, 2));                                                                               \
    if ((PREV)) { KA_DH((c) + 2, BUFOFF(((B) + 2) % 3)); SBAR(); }                               \
    KA_STEP(B, 1, KA_READ(B, 3));                                                                               \
    WAIT_BAR(WAITN);                                                                                            \
    KA_STEP(B, 2, KA_READ(BN, 0));                                                                              \
    if ((MORE)) { KA_DX((c) + 3, BUFOFF(B)); SBAR(); }                                           \
    KA_STEP(B, 3, KA_READ(BN, 1));                                                                              \
    if ((MORE)) { KA_DW((c) + 3, BUFOFF(B), 0); KA_DW((c) + 3, BUFOFF(B), 1); SBAR(); }          \
  } while (0)
  f32x4 rsq2 = rsum;
  WAIT_BAR(6);
  STAMP(0, 1);
  STAMPC(0, 14);
  KA_READ(0, 0);
  KA_READ(0, 1);
  KA_STEP(0, 0, KA_READ(0, 2));
  KA_DX(2, BUFOFF(2)); KA_DW(2, BUFOFF(2), 0); KA_DW(2, BUFOFF(2), 1); SBAR();
  KA_STEP(0, 1, KA_READ(0, 3));
  KA_DH(2, BUFOFF(2)); SBAR();
  WAIT_BAR(6);
  KA_STEP(0, 2, KA_READ(1, 0));
  KA_DX(3, BUFOFF(0)); SBAR();
  KA_STEP(0, 3, KA_READ(1, 1));
  KA_DW(3, BUFOFF(0), 0); KA_DW(3, BUFOFF(0), 1); SBAR();
#pragma unroll 1
  for (int c = 1; c < 7; c += 3) {
    KA_CHUNK(c, 1, 2, 6, true, true);
    KA_CHUNK(c + 1, 2, 0, 6, true, true);
    KA_CHUNK(c + 2, 0, 1, 6, true, true);
  }
  KA_CHUNK(7, 1, 2, 6, true, false);
  KA_CHUNK(8, 2, 0, 0, false, false);
  KA_STEP(0, 0, KA_READ(0, 2));
  KA_STEP(0, 1, KA_READ(0, 3));
  if (nh == 0) {
    asm volatile("" : "+v"(xf[2]), "+v"(xf[0]));
    rsq = MFMA4(lo4(xf[2]), lo4(xf[2]), rsq); rsum = MFMA16(xf[2], sel, rsum); rsq2 = MFMA4(hi4(xf[2]), hi4(xf[2]), rsq2);
    rsum = MFMA16(xf[0], sel, rsum); rsq = MFMA4(lo4(xf[0]), lo4(xf[0]), rsq); rsq2 = MFMA4(hi4(xf[0]), hi4(xf[0]), rsq2);
  }
  asm volatile("s_nop 7" : "+v"(rsum), "+v"(rsq), "+v"(rsq2));
  rsq += rsq2;
  STAMP(0, 3);
  STAMPC(0, 15);
  LDS_AS float* st_r = (LDS_AS float*)(lds + KA_STAT);
  LDS_AS float* st_cs = (LDS_AS float*)(lds + KA_SCS);
  LDS_AS float* st_b = (LDS_AS float*)(lds + KA_SB1);
  {
    const int d = lane & 3;
    const float sq = x32_add(d == 0 ? rsq[0] : d == 1 ? rsq[1] : d == 2 ? rsq[2] : rsq[3]);
    if (h == 0) st_r[256 + nh * 128 + mi * 32 + r] = sq;
    if ((lane & 15) < 2) {
      const int q4 = (lane & 1) * 16 + (lane >> 4) * 4;
      *(LDS_AS f32x4*)(st_r + nh * 128 + mi * 32 + q4) = rsum;
      *(LDS_AS f32x4*)(st_cs + mi * 224 + nh * 64 + q4) = cq[0];
      *(LDS_AS f32x4*)(st_cs + mi * 224 + nh * 64 + 32 + q4) = cq[1];
#pragma unroll
      for (int j = 0; j < NHT; ++j) *(LDS_AS f32x4*)(st_cs + mi * 224 + 128 + nh * 64 + j * 32 + q4) = chh[j];
    }
    if (nh == 0 && tid < 96) st_b[tid] = __uint_as_float(braw << 16);
  }
  SBAR();
  KA_STEPX(0, 2, NOP_, false);
  LBAR(0);
  float cpart = 0.f;
  if (tid < 224) cpart = (st_cs[tid] + st_cs[224 + tid]) + (st_cs[448 + tid] + st_cs[672 + tid]);
  const float sx_ = st_r[mi * 32 + r] + st_r[128 + mi * 32 + r], sq_ = st_r[256 + mi * 32 + r] + st_r[384 + mi * 32 + r];
  SBAR();
  KA_STEPX(0, 3, NOP_, false);
#undef KA_READ
#undef KA_STEP
#undef KA_STEPX
#undef KA_CHUNK
#undef KA_ISSUE
  if (tid < 224) st_cs[tid] = cpart;
  {
    const char* ob = ws + WS_O + (size_t)blockIdx.x * 10240;
    const u32x4 ff = {0xffffffffu, 0xffffffffu, 0xffffffffu, 0xffffffffu};
    st16s(ob, (unsigned)tid * 16u, ff);
    if (w < 2) st16s(ob + 8192, (unsigned)tid * 16u, ff);
  }
  if (nh == 1) {
    const int l0_ = blockIdx.x * 85 + (tid - 256 < 85 ? tid - 256 : 0);
    const char* p = l0_ < 15360 ? w2 + (size_t)l0_ * 128 : wo + (size_t)(l0_ - 15360) * 128;
    if (w < 6) asm volatile("global_load_dword %0, %1, off" : "=v"(warm) : "v"(p) : "memory");
  }
  const float mu = sx_ * (1.0f / D_);
  const float var = fmaxf(sq_ * (1.0f / D_) - mu * mu, 0.f);
  const float rs = __builtin_amdgcn_rsqf(var + LN_EPS);
  LDS_AS f32x2* st_w = (LDS_AS f32x2*)(lds + KA_SMR) + w * 32;
  if (TYPE == 1 && h == 0) st_w[r] = f32x2{mu * rs, rs};
  LBAR(0);
  STAMP(0, 5);
  if (cb < 15) {
    if (TYPE == 1) {
      char* dst = ws + WS_V + (size_t)tq * 1280;
      const float cs0 = st_cs[nh * 64 + r], cs1 = st_cs[nh * 64 + 32 + r];
#pragma unroll
      for (int s = 0; s < 2; ++s) {
        f32x2 mr[8];
#pragma unroll
        for (int e = 0; e < 8; ++e) mr[e] = st_w[16 * s + 8 * (e >> 2) + 4 * h + (e & 3)];
        u32x4 o0, o1;
#pragma unroll
        for (int e = 0; e < 4; ++e) {
          const int i = 8 * s + 2 * e;
          o0[e] = cvtpk(acc[0][i] * mr[2 * e][1] - mr[2 * e][0] * cs0, acc[0][i + 1] * mr[2 * e + 1][1] - mr[2 * e + 1][0] * cs0);
          o1[e] = cvtpk(acc[1][i] * mr[2 * e][1] - mr[2 * e][0] * cs1, acc[1][i + 1] * mr[2 * e + 1][1] - mr[2 * e + 1][0] * cs1);
        }
        st16s(dst + s * 640, so[0], o0);
        st16s(dst + s * 640, so[1], o1);
      }
    } else {
      const float sc = isq ? rs * QSCALE : rs, ms = mu * sc;
      char* base = ws + (isq ? WS_Q : WS_K) + (size_t)tq * 1280;
#pragma unroll
      for (int j = 0; j < 2; ++j)
#pragma unroll
        for (int g = 0; g < 4; ++g) {
          const f32x4 cs = *(const LDS_AS f32x4*)(st_cs + nh * 64 + j * 32 + 8 * g + 4 * h);
          const u32x2 o = {cvtpk(acc[j][4 * g] * sc - ms * cs[0], acc[j][4 * g + 1] * sc - ms * cs[1]),
                           cvtpk(acc[j][4 * g + 2] * sc - ms * cs[2], acc[j][4 * g + 3] * sc - ms * cs[3])};
          st8s(base, so[j * 4 + g], o);
        }
    }
  }
  {
    const float sc = rs * __builtin_amdgcn_rsqf(var * rs * rs + LN_EPS), ms = mu * sc;
    constexpr float GK1 = -2.0f * 1.4426950408889634f * 0.7978845608028654f, GK2 = GK1 * 0.044715f;
    const char* dst = ws + WS_H + ((size_t)(rb * 8 + mi * 2) * 48 + (f0 >> 5) + nh * 2) * 1024;
    const unsigned ho = (unsigned)((r >> 4) * (48 * 1024) + (h * 16 + (r & 15)) * 16);
    const f32x2 sc2 = {sc, sc}, ms2 = {ms, ms}, k1 = {GK1, GK1}, k2 = {GK2, GK2}, one2 = {1.0f, 1.0f};
#pragma unroll
    for (int j = 0; j < NHT; ++j)
#pragma unroll
      for (int u = 0; u < 2; ++u) {
        u32x4 o;
#pragma unroll
        for (int gg = 0; gg < 2; ++gg) {
          const int g = 2 * u + gg;
          const f32x4 cs = *(const LDS_AS f32x4*)(st_cs + 128 + nh * 64 + j * 32 + 8 * g + 4 * h);
          const f32x4 bb = *(const LDS_AS f32x4*)(st_b + nh * 64 + j * 32 + 8 * g + 4 * h);
#pragma unroll
          for (int e = 0; e < 2; ++e) {
            const f32x2 a2 = {ach[j][4 * g + 2 * e], ach[j][4 * g + 2 * e + 1]};
            const f32x2 c2 = {cs[2 * e], cs[2 * e + 1]}, b2_ = {bb[2 * e], bb[2 * e + 1]};
            const f32x2 t = a2 * sc2 + (b2_ - ms2 * c2);
            const f32x2 arg = t * ((t * t) * k2 + k1);
            const f32x2 den = f32x2{__builtin_amdgcn_exp2f(arg[0]), __builtin_amdgcn_exp2f(arg[1])} + one2;
            const f32x2 v = t * f32x2{__builtin_amdgcn_rcpf(den[0]), __builtin_amdgcn_rcpf(den[1])};
            o[2 * gg + e] = cvtpk(v[0], v[1]);
          }
        }
        st16s(dst + j * 1024 + u * 512, ho, o);
      }
  }
  STAMP(0, 4);
  asm volatile("s_waitcnt vmcnt(0)" : "+v"(warm) :: "memory");
}

__global__ __launch_bounds__(512, 2) void ka_proj(const char* __restrict__ x, const char* __restrict__ wqkv,
                                                  const char* __restrict__ w1, const unsigned short* __restrict__ b1,
                                                  const char* __restrict__ w2, const char* __restrict__ wo,
                                                  char* __restrict__ ws) {
  extern __shared__ __attribute__((aligned(16))) char smem[];
  lds_ptr lds = (lds_ptr)smem;
  const int bid = blockIdx.x, xg = bid & 7, idx = bid >> 3;
  const int rb = (xg & 1) * 8 + (idx & 7), cb = (xg >> 1) * 4 + (idx >> 3);
  const int n0 = (cb < 15 ? cb : 14) * 128, f0 = cb * 96;
  const int nh = __builtin_amdgcn_readfirstlane(threadIdx.x >> 8);
  if (cb < 10) {
    if (nh) ka_body<0, 1>(lds, x, wqkv, w1, b1, w2, wo, n0, f0, rb, cb, ws);
    else ka_body<0, 0>(lds, x, wqkv, w1, b1, w2, wo, n0, f0, rb, cb, ws);
  } else {
    if (nh) ka_body<1, 1>(lds, x, wqkv, w1, b1, w2, wo, n0, f0, rb, cb, ws);
    else ka_body<1, 0>(lds, x, wqkv, w1, b1, w2, wo, n0, f0, rb, cb, ws);
  }
}

DI u32x4 bload16_dev(srd_t r, unsigned voff, unsigned soff) { return __builtin_amdgcn_raw_buffer_load_b128(r, (int)voff, (int)soff, 16); }

constexpr int AT_TILE = 3584;
constexpr int AT_SLOT = 28672;
constexpr int KB_WR = 2 * AT_SLOT;
constexpr int KB_GRP = 40960;
constexpr int KB_RED = KB_WR + 2 * KB_GRP;
constexpr int KB_LDS = KB_RED + 20480;

__global__ __launch_bounds__(512, 2) void kb_main(const char* __restrict__ x, const char* __restrict__ w2,
                                                  const unsigned short* __restrict__ b2, const char* __restrict__ wo,
                                                  char* __restrict__ ws, char* __restrict__ out) {
  extern __shared__ __attribute__((aligned(16))) char smem[];
  lds_ptr lds = (lds_ptr)smem;
  const int tid = threadIdx.x, lane = tid & 63;
  const int wid = __builtin_amdgcn_readfirstlane(tid >> 6);
  const int r = lane & 31, h = lane >> 5;
  const int xg = blockIdx.x & 7, idx = blockIdx.x >> 3;
  const int head = xg * 4 + (idx >> 3), qb = idx & 7;
  const int rbk = xg * 4 + (idx & 3), cbk = idx >> 2;
  const int mi = wid & 3, kh = wid >> 2, gq = lane >> 4;
  const unsigned lds0 = (unsigned)(uintptr_t)lds;
  const char* Kh = ws + WS_K + (size_t)head * (64 * 1280);
  const char* Vh = ws + WS_V + (size_t)head * (64 * 1280);
  const char* Qt = ws + WS_Q + (size_t)(head * 64 + qb * 8 + wid) * 1280;
  STAMP(1, 0);

  const unsigned vlin = (unsigned)lane * 16u;
  const unsigned vv = (unsigned)(h * 20 + r) * 16u;
  const unsigned vv2 = vv + (unsigned)(WS_V - WS_K) - 1280u - 1536u, vv3 = vv2 + 640u - 1024u;
  const unsigned long long mk16 = 0xffffull, mk20 = 0x000fffff000fffffull;
#define KV_M0(c) SET_M0(lds0 + (unsigned)(((c) & 1) * AT_SLOT + wid * AT_TILE))
#define KV_SRC(c) (Kh + (size_t)((c) * 8 + wid + 1) * 1280)
#define KV_P0(c) asm volatile("global_load_lds_dwordx4 %0, %1" :: "v"(vlin), "s"(KV_SRC(c)) : "memory")
#define KV_P1(c) asm volatile("s_mov_b64 exec, %2\n\tglobal_load_lds_dwordx4 %0, %1 offset:1024\n\ts_mov_b64 exec, -1" :: "v"(vlin), "s"(KV_SRC(c)), "s"(mk16) : "memory")
#define KV_P23(c) asm volatile("s_mov_b64 exec, %3\n\tglobal_load_lds_dwordx4 %0, %2 offset:1536\n\tglobal_load_lds_dwordx4 %1, %2 offset:2560\n\ts_mov_b64 exec, -1" :: "v"(vv2), "v"(vv3), "s"(KV_SRC(c)), "s"(mk20) : "memory")
  bf16x8 qa, ka0;
  u32x2 qbv, kb0;
  {
    const unsigned v8 = 1024u + (unsigned)r * 8u;
    asm volatile("global_load_dwordx4 %0, %4, %6\n\tglobal_load_dwordx2 %1, %5, %6\n\t"
                 "global_load_dwordx4 %2, %4, %7\n\tglobal_load_dwordx2 %3, %5, %7"
                 : "=&v"(qa), "=&v"(qbv), "=&v"(ka0), "=&v"(kb0) : "v"(vlin), "v"(v8), "s"(Qt), "s"(Kh) : "memory");
  }
  KV_M0(0); KV_P0(0); KV_P1(0); KV_P23(0);
#pragma unroll
  for (int sl = 0; sl < 2; ++sl) {
    lds_ptr tp = lds + sl * AT_SLOT + wid * AT_TILE;
    if (lane < 32) *(LDS_AS u32x2*)(tp + 1024 + 256 + lane * 8) = u32x2{0x00003f80u, 0u};
    if (lane < 48) {
      const int blk4 = (lane * 43) >> 9, dd = 20 + lane - blk4 * 12;
      const unsigned val = dd == 20 ? 0x3f803f80u : 0u;
      *(LDS_AS u32x4*)(tp + 1536 + blk4 * 512 + dd * 16) = u32x4{val, val, val, val};
    }
  }
  unsigned vo[5];
#pragma unroll
  for (int i = 0; i < 5; ++i) {
    const int p = i * 64 + lane, row = (p * 6554) >> 16;
    vo[i] = (unsigned)(row * 1280 + (p - row * 10) * 16 - (i & 3) * 1024);
  }
#define W_SRC(W, grp) ((W) + cbk * 160 + (size_t)((grp) * 256 + wid * 32) * 1280)
#define W_M0(reg, hi) SET_M0(lds0 + (unsigned)(KB_WR + (reg) * KB_GRP + wid * 5120 + (hi) * 4096))
#define W_P(W, i, grp) asm volatile("global_load_lds_dwordx4 %0, %1 offset:%2" :: "v"(vo[i]), "s"(W_SRC(W, grp)), "i"(((i) & 3) * 1024) : "memory")
  const char* hsrc = ws + WS_H + ((size_t)(rbk * 4 + mi) * 48 + kh) * 1024 + 4096;
#define H_LOAD(D, j, grp) asm volatile("global_load_dwordx4 %0, %1, %2 offset:%3" : "=v"(D[j]) : "v"(vlin), "s"(hsrc + (grp) * 8192), "i"((j) * 2048 - 4096) : "memory")
  const unsigned wra = lds0 + (unsigned)(KB_WR + (kh * 32 + 4 * ((gq & 1) + 4 * (gq >> 1)) + ((lane & 15) >> 2)) * 160 + (lane & 3) * 8);
  const unsigned wrb = wra + KB_GRP;

  unsigned oo[3];
#pragma unroll
  for (int g = 0; g < 3; ++g) {
    const int Gc = head * 5 + 2 * g + h, G8 = Gc & 7;
    oo[g] = (unsigned)((r >> 4) * (20 * 1024) + (r & 15) * 16 + (Gc >> 3) * 1024 + ((G8 >> 2) * 2 + (G8 & 1)) * 256 + ((G8 >> 1) & 1) * 8);
    PIN(oo[g]);
  }
  bf16x8 hA[4], hB[4];
  asm volatile("s_waitcnt vmcnt(4)" : "+v"(qa), "+v"(qbv), "+v"(ka0), "+v"(kb0) :: "memory");
  f32x16 o;
#pragma unroll
  for (int i = 0; i < 16; ++i) o[i] = 0.f;
  const f32x16 zero = o;
  if (h == 1) { qbv[0] = 0u; qbv[1] = 0u; kb0[0] = 0x00003f80u; kb0[1] = 0u; }
  const f32x16 s16 = MFMA32(ka0, qa, zero);
  f32x16 sA = MFMA32K8(__builtin_bit_cast(bf16x4, kb0), __builtin_bit_cast(bf16x4, qbv), s16);
  {
    float m = fmaxf(fmaxf(sA[0], sA[1]), sA[2]);
#pragma unroll
    for (int i = 3; i < 15; i += 2) m = fmaxf(fmaxf(m, sA[i]), sA[i + 1]);
    m = fmaxf(m, sA[15]);
    const auto mm = __builtin_amdgcn_permlane32_swap(__float_as_uint(m), __float_as_uint(m), false, false);
    m = fmaxf(__uint_as_float(mm[0]), __uint_as_float(mm[1]));
    if (h == 1) qbv[0] = cvtpk(-m, 0.f);
  }
  const bf16x4 qb4 = __builtin_bit_cast(bf16x4, qbv);
  sA = MFMA32K8(__builtin_bit_cast(bf16x4, kb0), qb4, s16);
  f32x16 sB;
  f32x4 acc[5];
#pragma unroll
  for (int j = 0; j < 5; ++j) acc[j] = f32x4{0.f, 0.f, 0.f, 0.f};
  WAIT_BAR(0);
  STAMP(1, 1);
  STAMPC(1, 14);
  STAMP2(2, 0);

  unsigned a16 = lds0 + vlin, a8 = lds0 + (unsigned)lane * 8u;
  int sdelta = AT_SLOT;
  bf16x8 kaA, v0A, v1A, kaB, v0B, v1B;
  bf16x4 kbA, kbB;
  bf16x4 wl[5], wu[5];
  u32x4 p0, p1 = {0u, 0u, 0u, 0u};
  v1B = __builtin_bit_cast(bf16x8, p1);
#define RD_KA(X, j) ka##X = *(const LDS_AS bf16x8*)(LP(a16) + (j) * AT_TILE)
#define RD_KB(X, j) kb##X = *(const LDS_AS bf16x4*)(LP(a8) + (j) * AT_TILE + 1024)
#define RD_V0(X, j) v0##X = *(const LDS_AS bf16x8*)(LP(a16) + (j) * AT_TILE + 1536)
#define RD_V1(X, j) v1##X = *(const LDS_AS bf16x8*)(LP(a16) + (j) * AT_TILE + 2560)
#define W_LO(WA, j, t) wl[t] = tr_read(LP(WA) + (j) * 10240 + (t) * 32)
#define W_HI(WA, j, t) wu[t] = tr_read(LP(WA) + (j) * 10240 + (t) * 32 + 1280)
#define G_MM(HS, j, t) acc[t] = MFMA16(cat8(wl[t], wu[t]), HS[j], acc[t])
#define EX2(S, i) do { S[i] = __builtin_amdgcn_exp2f(S[i]); S[i + 1] = __builtin_amdgcn_exp2f(S[i + 1]); } while (0)
#define MID() do { __builtin_amdgcn_sched_group_barrier(0x400, 2, 0); __builtin_amdgcn_sched_group_barrier(0x008, 1, 0); \
                    __builtin_amdgcn_sched_group_barrier(0x400, 2, 0); __builtin_amdgcn_sched_group_barrier(0x002, 2, 0); } while (0)
#define AT_STEP(SC, SN, X, Y, JN, PRE, WP, A0, A1, A2, A3, A4, A5, V0_, V1_, V2_, M1, M2, M3)            \
  do {                                                                                                   \
    PRE;                                                                                                 \
    asm volatile("" : "+v"(ka##X), "+v"(kb##X), "+v"(v0##X), "+v"(v1##X)); WP; SBAR();                   \
    EX2(SC, 0); PIN(SC); SBAR();                                                                         \
    o = MFMA32(v1##Y, __builtin_bit_cast(bf16x8, p1), o); SBAR();                                        \
    RD_KA(Y, JN); RD_KB(Y, JN); RD_V0(Y, JN); RD_V1(Y, JN); A0; A1; A2; A3; A4; A5; V0_; V1_; SBAR(); \
    M1; EX2(SC, 2); EX2(SC, 4); p0[0] = cvtpk(SC[0], SC[1]); p0[1] = cvtpk(SC[2], SC[3]); MID();         \
    asm volatile("" : "+v"(p0), "+v"(SC), "+v"(acc[0]), "+v"(acc[3]), "+v"(o)); SBAR();                  \
    SN = MFMA32(ka##X, qa, zero); SBAR();                                                                \
    M2; EX2(SC, 6); EX2(SC, 8); p0[2] = cvtpk(SC[4], SC[5]); p0[3] = cvtpk(SC[6], SC[7]); MID();         \
    asm volatile("" : "+v"(p0), "+v"(SC), "+v"(acc[1])); SBAR();                                         \
    SN = MFMA32K8(kb##X, qb4, SN); SBAR();                                                               \
    V2_; SBAR();                                                                                         \
    M3; EX2(SC, 10); EX2(SC, 12); p1[0] = cvtpk(SC[8], SC[9]); p1[1] = cvtpk(SC[10], SC[11]); MID();     \
    asm volatile("" : "+v"(p1), "+v"(SC), "+v"(acc[2]), "+v"(acc[4])); SBAR();                           \
    o = MFMA32(v0##X, __builtin_bit_cast(bf16x8, p0), o); SBAR();                                        \
    EX2(SC, 14); p1[2] = cvtpk(SC[12], SC[13]); p1[3] = cvtpk(SC[14], SC[15]);                           \
    asm volatile("" : "+v"(p1), "+v"(o)); SBAR();                                                        \
  } while (0)
#define WP_E asm volatile("" : "+v"(wl[0]), "+v"(wu[0]), "+v"(wl[1]), "+v"(wu[1]), "+v"(wl[2]), "+v"(wu[2]))
#define WP_O asm volatile("" : "+v"(wl[3]), "+v"(wu[3]), "+v"(wl[4]), "+v"(wu[4]))
#define FLIP() do { a16 += (unsigned)sdelta; a8 += (unsigned)sdelta; sdelta = -sdelta; } while (0)
#define SYNC_H(HN) asm volatile("s_waitcnt vmcnt(3) lgkmcnt(0)\n\ts_barrier" : "+v"(HN[0]) :: "memory")
#define WAIT_H(HC, j, N) asm volatile("s_waitcnt vmcnt(" #N ")" : "+v"(HC[j]) :: "memory")
#define STEP_E(JN, PRE, WC, HC, j, VA, VB)                                                               \
  AT_STEP(sA, sB, A, B, JN, PRE, WP_E, W_LO(WC, j, 3), W_HI(WC, j, 3), W_LO(WC, j, 4), W_HI(WC, j, 4), NOP_, NOP_, NOP_, VA, VB, \
          G_MM(HC, j, 0), G_MM(HC, j, 1), G_MM(HC, j, 2))
#define STEP_O(JN, PRE, WA, jn, HC, j, VA, VB)                                                           \
  AT_STEP(sB, sA, B, A, JN, PRE, WP_O, W_LO(WA, jn, 0), W_HI(WA, jn, 0), W_LO(WA, jn, 1), W_HI(WA, jn, 1), W_LO(WA, jn, 2), \
          W_HI(WA, jn, 2), NOP_, VA, VB, G_MM(HC, j, 3), NOP_, G_MM(HC, j, 4))
#define CHUNK_G(c, WC, HC, WN, HN, RN, NW, NG, HG)                                                       \
  do {                                                                                                   \
    STEP_E(1, NOP_, WC, HC, 0, W_M0(RN, 0); W_P(NW, 0, NG), W_P(NW, 1, NG));                             \
    STEP_O(2, NOP_, WC, 1, HC, 0, W_P(NW, 2, NG), W_P(NW, 3, NG));                                       \
    STEP_E(3, WAIT_H(HC, 1, 6), WC, HC, 1, W_M0(RN, 1); W_P(NW, 4, NG), KV_M0((c) + 1); KV_P0((c) + 1)); \
    STEP_O(4, NOP_, WC, 2, HC, 1, KV_P1((c) + 1), KV_P23((c) + 1));                                      \
    STEP_E(5, WAIT_H(HC, 2, 10), WC, HC, 2, H_LOAD(HN, 0, HG), H_LOAD(HN, 1, HG));                       \
    STEP_O(6, NOP_, WC, 3, HC, 2, H_LOAD(HN, 2, HG), H_LOAD(HN, 3, HG));                                 \
    STEP_E(7, WAIT_H(HC, 3, 13), WC, HC, 3, NOP_, NOP_);                                                 \
    STEP_O(0, SYNC_H(HN); STAMP2(2, 1 + (c)); FLIP(), WN, 0, HC, 3, NOP_, NOP_);                         \
  } while (0)
#define PLAIN2_E(JN, VA, VB) AT_STEP(sA, sB, A, B, JN, NOP_, NOP_, NOP_, NOP_, NOP_, NOP_, NOP_, NOP_, NOP_, VA, VB, NOP_, NOP_, NOP_)
#define PLAIN2_O(JN, VA, VB) AT_STEP(sB, sA, B, A, JN, NOP_, NOP_, NOP_, NOP_, NOP_, NOP_, NOP_, NOP_, NOP_, VA, VB, NOP_, NOP_, NOP_)
#define PLAIN_E(JN, X0, X1, X2, X3) AT_STEP(sA, sB, A, B, JN, NOP_, NOP_, X0, X1, X2, X3, NOP_, NOP_, NOP_, NOP_, NOP_, NOP_, NOP_, NOP_)
#define PLAIN_O(JN, X0, X1, X2, X3) AT_STEP(sB, sA, B, A, JN, NOP_, NOP_, X0, X1, X2, X3, NOP_, NOP_, NOP_, NOP_, NOP_, NOP_, NOP_, NOP_)

  RD_KA(A, 0); RD_KB(A, 0); RD_V0(A, 0); RD_V1(A, 0);
  PLAIN2_E(1, W_M0(0, 0); W_P(w2, 0, 0), W_P(w2, 1, 0));
  PLAIN2_O(2, W_P(w2, 2, 0), W_P(w2, 3, 0));
  PLAIN2_E(3, W_M0(0, 1); W_P(w2, 4, 0), KV_M0(1); KV_P0(1));
  PLAIN2_O(4, KV_P1(1), KV_P23(1));
  PLAIN2_E(5, H_LOAD(hA, 0, 0), H_LOAD(hA, 1, 0));
  PLAIN2_O(6, H_LOAD(hA, 2, 0), H_LOAD(hA, 3, 0));
  PLAIN_E(7, NOP_, NOP_, NOP_, NOP_);
  AT_STEP(sB, sA, B, A, 0, SYNC_H(hA); STAMP2(2, 1); FLIP(), NOP_, W_LO(wra, 0, 0), W_HI(wra, 0, 0), W_LO(wra, 0, 1), W_HI(wra, 0, 1),
          W_LO(wra, 0, 2), W_HI(wra, 0, 2), NOP_, NOP_, NOP_, NOP_, NOP_, NOP_);
#pragma unroll 1
  for (int c = 1; c < 5; c += 2) {
    CHUNK_G(c, wra, hA, wrb, hB, 1, w2, c, c);
    CHUNK_G(c + 1, wrb, hB, wra, hA, 0, w2, c + 1, c + 1);
  }
  CHUNK_G(5, wra, hA, wrb, hB, 1, w2, 5, 5);
  const int tok = rbk * 64 + mi * 16 + (lane & 15);
  const unsigned xo = (unsigned)(tok * 1280 + (cbk * 80 + kh * 48 + 4 * gq) * 2);
  const int j2 = kh ? 1 : 2;
  LDS_AS f32x4* red = (LDS_AS f32x4*)(lds + KB_RED) + (mi * 5) * 64 + lane;
  u32x2 gv[3], xv[3];
  PLAIN2_E(1, KV_M0(7); KV_P0(7), KV_P1(7));
  PLAIN2_O(2, KV_P23(7), W_M0(0, 0); W_P(wo, 0, 0));
  PLAIN2_E(3, W_P(wo, 1, 0), W_P(wo, 2, 0));
  PLAIN2_O(4, W_P(wo, 3, 0), W_M0(0, 1); W_P(wo, 4, 0));
  PLAIN2_E(5, NOP_, NOP_);
  PLAIN2_O(6, NOP_, NOP_);
  PLAIN2_E(7, NOP_, NOP_);
  AT_STEP(sB, sA, B, A, 0,
          asm volatile("s_waitcnt vmcnt(0) lgkmcnt(0)\n\ts_barrier" : "+v"(hB[1]), "+v"(hB[2]), "+v"(hB[3]) :: "memory"); STAMP2(2, 7); FLIP(),
          NOP_, NOP_, NOP_, NOP_, NOP_, NOP_, NOP_, NOP_, NOP_, NOP_, NOP_, NOP_, NOP_);
  PLAIN2_E(1, _Pragma("unroll") for (int j = 0; j < 3; ++j) gv[j] = *(const u32x2*)(b2 + cbk * 80 + kh * 48 + (j < 2 ? j : j2) * 16 + 4 * gq), NOP_);
  PLAIN2_O(2, _Pragma("unroll") for (int j = 0; j < 3; ++j) xv[j] = *(const u32x2*)(x + xo + (j < 2 ? j : j2) * 32), NOP_);
  PLAIN2_E(3, NOP_, NOP_);
  PLAIN2_O(4, NOP_, NOP_);
  PLAIN2_E(5, _Pragma("unroll") for (int j = 0; j < 3; ++j) { PIN(gv[j]); PIN(xv[j]); }
           SET_M0(lds0 + (unsigned)(wid * 2560));
           asm volatile("global_load_lds_dwordx4 %0, %3\n\tglobal_load_lds_dwordx4 %1, %3 offset:1024\n\t"
                        "s_mov_b32 exec_hi, 0\n\tglobal_load_lds_dwordx4 %2, %3 offset:2048\n\ts_mov_b32 exec_hi, -1"
                        :: "v"(vo[0]), "v"(vo[1]), "v"(vo[2]), "s"(wo + cbk * 160 + (size_t)(256 + wid * 16) * 1280) : "memory"), NOP_);
  PLAIN2_O(6, NOP_, NOP_);
  PLAIN2_E(7, NOP_, NOP_);
  EX2(sB, 0); PIN(sB); SBAR();
  o = MFMA32(v1A, __builtin_bit_cast(bf16x8, p1), o); SBAR();
  EX2(sB, 2); EX2(sB, 4); EX2(sB, 6);
  p0 = u32x4{cvtpk(sB[0], sB[1]), cvtpk(sB[2], sB[3]), cvtpk(sB[4], sB[5]), cvtpk(sB[6], sB[7])};
  EX2(sB, 8); asm volatile("" : "+v"(p0), "+v"(sB)); SBAR();
  o = MFMA32(v0B, __builtin_bit_cast(bf16x8, p0), o); SBAR();
  EX2(sB, 10); EX2(sB, 12); EX2(sB, 14);
  p1 = u32x4{cvtpk(sB[8], sB[9]), cvtpk(sB[10], sB[11]), cvtpk(sB[12], sB[13]), cvtpk(sB[14], sB[15])};
  o = MFMA32(v1B, __builtin_bit_cast(bf16x8, p1), o);

  STAMP(1, 3);
  STAMPC(1, 15);
  STAMP2(2, 8);
  float l = __uint_as_float(__builtin_amdgcn_permlane32_swap(__float_as_uint(o[8]), __float_as_uint(o[8]), false, false)[1]);
  float big = fmaxf(fmaxf(__builtin_fabsf(o[0]), __builtin_fabsf(o[1])), l);
#pragma unroll
  for (int i = 2; i < 12; i += 2) big = fmaxf(fmaxf(big, __builtin_fabsf(o[i])), __builtin_fabsf(o[i + 1]));
  bool unsafe = !(big < 1.0e38f) || !(l > 0.25f);
#pragma unroll
  for (int i = 0; i < 12; ++i) unsafe |= o[i] != o[i];
  if (__builtin_expect(__any(unsafe), 0)) {
    if (h == 1) { qbv[0] = 0u; }
    const bf16x4 qz = __builtin_bit_cast(bf16x4, qbv);
    float m = -3.0e38f;
#pragma unroll
    for (int i = 0; i < 16; ++i) o[i] = 0.f;
    const unsigned onev = r == 20 ? 0x3f803f80u : 0u;
#pragma unroll 1
    for (int t = 0; t < 64; ++t) {
      const bf16x8 ka = *(const bf16x8*)(Kh + t * 1280 + lane * 16);
      u32x2 kb = *(const u32x2*)(Kh + t * 1280 + 1024 + r * 8);
      u32x4 v0 = {onev, onev, onev, onev}, v1 = v0;
      if (r < 20) {
        v0 = *(const u32x4*)(Vh + t * 1280 + vv);
        v1 = *(const u32x4*)(Vh + t * 1280 + 640 + vv);
      }
      f32x16 s = MFMA32(ka, qa, zero);
      s = MFMA32K8(__builtin_bit_cast(bf16x4, kb), qz, s);
      float tm = s[0];
#pragma unroll
      for (int i = 1; i < 16; ++i) tm = fmaxf(tm, s[i]);
      tm = fmaxf(tm, __shfl_xor(tm, 32));
      const float mn = fmaxf(m, tm);
      const float al = __builtin_amdgcn_exp2f(m - mn);
      m = mn;
      u32x4 q0, q1;
#pragma unroll
      for (int i = 0; i < 16; ++i) { o[i] *= al; s[i] = __builtin_amdgcn_exp2f(s[i] - mn); }
#pragma unroll
      for (int e = 0; e < 4; ++e) { q0[e] = cvtpk(s[2 * e], s[2 * e + 1]); q1[e] = cvtpk(s[8 + 2 * e], s[9 + 2 * e]); }
      o = MFMA32(__builtin_bit_cast(bf16x8, v0), __builtin_bit_cast(bf16x8, q0), o);
      o = MFMA32(__builtin_bit_cast(bf16x8, v1), __builtin_bit_cast(bf16x8, q1), o);
    }
    l = __shfl(o[8], 32 + r);
  }
  {
    const float inv = __builtin_amdgcn_rcpf(l);
    const char* ob = ws + WS_O + (size_t)(qb * 16 + wid * 2) * (20 * 1024);
#pragma unroll
    for (int g = 0; g < 3; ++g)
      if (g < 2 || h == 0)
        st8s(ob, oo[g], u32x2{min(cvtpk(o[4 * g] * inv, o[4 * g + 1] * inv), 0xfffffffeu),
                              min(cvtpk(o[4 * g + 2] * inv, o[4 * g + 3] * inv), 0xfffffffeu)});
  }
  STAMP(1, 4);
  {
    bf16x8 wf[2][5];
#pragma unroll
    for (int t = 0; t < 5; ++t) wf[0][t] = cat8(tr_read(LP(wrb) + t * 32), tr_read(LP(wrb) + t * 32 + 1280));
#pragma unroll
    for (int j = 0; j < 4; ++j) {
      SBAR();
      if (j < 3) {
#pragma unroll
        for (int t = 0; t < 5; ++t)
          wf[(j + 1) & 1][t] = cat8(tr_read(LP(wrb) + (j + 1) * 10240 + t * 32), tr_read(LP(wrb) + (j + 1) * 10240 + t * 32 + 1280));
      }
      SBAR();
#pragma unroll
      for (int t = 0; t < 5; ++t) acc[t] = MFMA16(wf[j & 1][t], hB[j], acc[t]);
      asm volatile("" : "+v"(acc[0]), "+v"(acc[1]), "+v"(acc[2]), "+v"(acc[3]), "+v"(acc[4]));
    }
    asm volatile("s_nop 7" : "+v"(acc[0]), "+v"(acc[1]), "+v"(acc[2]), "+v"(acc[3]), "+v"(acc[4]));
    if (kh == 1) { red[0] = acc[0]; red[64] = acc[1]; red[128] = acc[2]; } else { red[192] = acc[3]; red[256] = acc[4]; }
    asm volatile("s_waitcnt vmcnt(3) lgkmcnt(0)\n\ts_barrier" ::: "memory");
    const char* s_ = wo + cbk * 160 + (size_t)(384 + wid * 32) * 1280;
    W_M0(1, 0);
    asm volatile("global_load_lds_dwordx4 %0, %4\n\tglobal_load_lds_dwordx4 %1, %4 offset:1024\n\t"
                 "global_load_lds_dwordx4 %2, %4 offset:2048\n\tglobal_load_lds_dwordx4 %3, %4 offset:3072"
                 :: "v"(vo[0]), "v"(vo[1]), "v"(vo[2]), "v"(vo[3]), "s"(s_) : "memory");
    W_M0(1, 1);
    asm volatile("global_load_lds_dwordx4 %0, %1" :: "v"(vo[4]), "s"(s_) : "memory");
#define G_FIN(j, i)                                                                                       \
  do {                                                                                                    \
    const f32x4 tt = acc[j] + og[i] + f32x4{bf_lo(gv[i][0]), bf_hi(gv[i][0]), bf_lo(gv[i][1]), bf_hi(gv[i][1])}; \
    float t[4];                                                                                           \
    _Pragma("unroll") for (int e = 0; e < 4; ++e)                                                         \
      t[e] = tt[e] * __builtin_amdgcn_rcpf(1.0f + __builtin_amdgcn_exp2f(tt[e] * -1.4426950408889634f));  \
    gv[i] = u32x2{cvtpk(t[0], t[1]), cvtpk(t[2], t[3])};                                                  \
    PIN(gv[i]);                                                                                           \
  } while (0)
    f32x4 og[3];
    if (kh == 0) { og[0] = red[0]; og[1] = red[64]; og[2] = red[128]; G_FIN(0, 0); G_FIN(1, 1); G_FIN(2, 2); }
    else { og[0] = red[192]; og[1] = red[256]; G_FIN(3, 0); G_FIN(4, 1); }
#undef G_FIN
    SBAR();
  }
  {
    const srd_t ro = make_srd(ws + WS_O, (unsigned)(WS_G - WS_O));
    const unsigned oa = (unsigned)(((rbk * 4 + mi) * 20 + kh) * 1024);
    u32x4 af[10];
#pragma unroll
    for (int c = 0; c < 10; ++c) af[c] = bload16_dev(ro, vlin, oa + c * 2048u);
    SBAR();
#define WO_RD(D, c)                                                                                       \
  do {                                                                                                    \
    const unsigned wa_ = (c) < 4 ? wra + (unsigned)((c) * 10240) : (c) < 6 ? wra - (unsigned)KB_WR + (unsigned)(((c) - 4) * 10240) \
                                                                           : wrb + (unsigned)(((c) - 6) * 10240); \
    _Pragma("unroll") for (int t = 0; t < 5; ++t) D[t] = cat8(tr_read(LP(wa_) + t * 32), tr_read(LP(wa_) + t * 32 + 1280)); \
  } while (0)
    bf16x8 wfa[5][5], wfb[5][5];
#pragma unroll
    for (int c = 0; c < 5; ++c) WO_RD(wfa[c], c);
    SBAR();
#pragma unroll
    for (int j = 0; j < 5; ++j) acc[j] = f32x4{0.f, 0.f, 0.f, 0.f};
    STAMP(1, 6);
    for (unsigned spins = 0; spins < (1u << 15); ++spins) {
      unsigned mx = 0u;
#pragma unroll
      for (int c = 0; c < 10; ++c) mx = max(max(max(mx, af[c][0]), af[c][1]), max(af[c][2], af[c][3]));
      if (!__any(mx == 0xffffffffu)) break;
      __builtin_amdgcn_s_sleep(4);
      asm volatile("" ::: "memory");
#pragma unroll
      for (int c = 0; c < 10; ++c) af[c] = bload16_dev(ro, vlin, oa + c * 2048u);
    }
    STAMP(1, 7);
    LBAR(0);
#pragma unroll
    for (int c = 5; c < 8; ++c) WO_RD(wfb[c - 5], c);
    SBAR();
#pragma unroll
    for (int c = 0; c < 5; ++c)
#pragma unroll
      for (int t = 0; t < 5; ++t) acc[t] = MFMA16(wfa[c][t], __builtin_bit_cast(bf16x8, af[c]), acc[t]);
#pragma unroll
    for (int c = 8; c < 10; ++c) WO_RD(wfb[c - 5], c);
#pragma unroll
    for (int c = 5; c < 10; ++c)
#pragma unroll
      for (int t = 0; t < 5; ++t) acc[t] = MFMA16(wfb[c - 5][t], __builtin_bit_cast(bf16x8, af[c]), acc[t]);
#undef WO_RD
  }
  if (kh == 1) { red[0] = acc[0]; red[64] = acc[1]; red[128] = acc[2]; } else { red[192] = acc[3]; red[256] = acc[4]; }
  LBAR(0);
#define O_FIN(j, i)                                                                                       \
  do {                                                                                                    \
    const f32x4 sv = acc[j] + red[(j) * 64];                                                              \
    const float r0 = bf_lo(xv[i][0]) + sv[0] * bf_lo(gv[i][0]), r1 = bf_hi(xv[i][0]) + sv[1] * bf_hi(gv[i][0]); \
    const float r2 = bf_lo(xv[i][1]) + sv[2] * bf_lo(gv[i][1]), r3 = bf_hi(xv[i][1]) + sv[3] * bf_hi(gv[i][1]); \
    *(LDS_AS u32x2*)(stg + (j) * 32) = u32x2{cvtpk(r0, r1), cvtpk(r2, r3)};                               \
  } while (0)
  lds_ptr stg = lds + AT_SLOT + (mi * 16 + (lane & 15)) * 176 + gq * 8;
  if (kh == 0) { O_FIN(0, 0); O_FIN(1, 1); O_FIN(2, 2); } else { O_FIN(3, 0); O_FIN(4, 1); }
#undef O_FIN
  LBAR(0);
  {
#pragma unroll
    for (int i = 0; i < 2; ++i) {
      const int p = i * 512 + tid, row = (p * 6554) >> 16, c16 = p - row * 10;
      if (i == 0 || wid < 2) *(u32x4*)(out + (size_t)(rbk * 64) * 1280 + cbk * 160 + row * 1280 + c16 * 16) = (*(const LDS_AS u32x4*)(lds + AT_SLOT + row * 176 + c16 * 16));
    }
  }
  STAMP(1, 8);
}

extern "C" void kernel_launch(void* const* d_in, const int* in_sizes, int n_in, void* d_out, int out_size, void* d_ws,
                              size_t ws_size, hipStream_t stream) {
  (void)in_sizes; (void)n_in; (void)out_size; (void)ws_size;
  const char* x = (const char*)d_in[0];
  const char* wqkv = (const char*)d_in[1];
  const char* wo = (const char*)d_in[2];
  const char* w1 = (const char*)d_in[4];
  const unsigned short* b1 = (const unsigned short*)d_in[5];
  const char* w2 = (const char*)d_in[6];
  const unsigned short* b2 = (const unsigned short*)d_in[7];
  char* ws = (char*)d_ws;
  (void)hipFuncSetAttribute((const void*)ka_proj, hipFuncAttributeMaxDynamicSharedMemorySize, KA_LDS);
  (void)hipFuncSetAttribute((const void*)kb_main, hipFuncAttributeMaxDynamicSharedMemorySize, KB_LDS);
  ka_proj<<<256, 512, KA_LDS, stream>>>(x, wqkv, w1, b1, w2, wo, ws);
  kb_main<<<256, 512, KB_LDS, stream>>>(x, w2, b2, wo, ws, (char*)d_out);
}
```

```cpp
#include <hip/hip_runtime.h>
#include <hip/hip_bf16.h>
#include <stdint.h>

typedef __attribute__((ext_vector_type(8))) short bf16x8;
typedef __attribute__((ext_vector_type(4))) short bf16x4;
typedef __attribute__((ext_vector_type(16))) float f32x16;
typedef __attribute__((ext_vector_type(4))) float f32x4;
typedef __attribute__((ext_vector_type(2))) float f32x2;
typedef __attribute__((ext_vector_type(4))) unsigned u32x4;
typedef __attribute__((ext_vector_type(2))) unsigned u32x2;
typedef __attribute__((ext_vector_type(2))) __bf16 bf2;
typedef __attribute__((address_space(3))) char* lds_ptr;
#define LDS_AS __attribute__((address_space(3)))
#define DI __device__ __forceinline__

#define MFMA32(a, b, c) __builtin_amdgcn_mfma_f32_32x32x16_bf16((a), (b), (c), 0, 0, 0)
#define MFMA32K8(a, b, c) __builtin_amdgcn_mfma_f32_32x32x8bf16_1k((a), (b), (c), 0, 0, 0)
#define MFMA16(a, b, c) __builtin_amdgcn_mfma_f32_16x16x32_bf16((a), (b), (c), 0, 0, 0)
#define SBAR() __builtin_amdgcn_sched_barrier(0)
#define PIN(x) asm volatile("" : "+v"(x))
#define WAIT_BAR(N) asm volatile("s_waitcnt vmcnt(" #N ") lgkmcnt(0)\n\ts_barrier" ::: "memory")

constexpr int D_ = 640, NH = 32;
constexpr float LN_EPS = 1e-5f;
constexpr float QSCALE = 0.22360679774997896f * 1.4426950408889634f;

constexpr size_t WS_Q = 0;
constexpr size_t WS_K = WS_Q + (size_t)NH * 64 * 1280;
constexpr size_t WS_V = WS_K + (size_t)NH * 64 * 1280;
constexpr size_t WS_H = WS_V + (size_t)NH * 64 * 1280;
constexpr size_t WS_O = WS_H + (size_t)128 * 48 * 1024;
constexpr size_t WS_G = WS_O + (size_t)128 * 20 * 1024;

#define STAMP(kern, i) do { } while (0)
#define STAMPC(kern, i) do { } while (0)
#define STAMP2(kern, i) do { } while (0)

DI void st16s(const void* sb, unsigned off, u32x4 v) { asm volatile("global_store_dwordx4 %0, %1, %2 sc1\n\ts_nop 1" :: "v"(off), "v"(v), "s"(sb) : "memory"); }
DI void st8s(const void* sb, unsigned off, u32x2 v) { asm volatile("global_store_dwordx2 %0, %1, %2 sc1" :: "v"(off), "v"(v), "s"(sb) : "memory"); }
DI unsigned cvtpk(float lo, float hi) {
  f32x2 f = {lo, hi};
  return __builtin_bit_cast(unsigned, __builtin_convertvector(f, bf2));
}
DI float bf_lo(unsigned u) { return __uint_as_float(u << 16); }
DI float bf_hi(unsigned u) { return __uint_as_float(u & 0xffff0000u); }
typedef short v4i16_t __attribute__((ext_vector_type(4)));
DI bf16x4 tr_read(lds_ptr p) {
  return __builtin_bit_cast(bf16x4, __builtin_amdgcn_ds_read_tr16_b64_v4i16((LDS_AS v4i16_t*)p));
}
DI bf16x8 cat8(bf16x4 a, bf16x4 b) { return __builtin_shufflevector(a, b, 0, 1, 2, 3, 4, 5, 6, 7); }

constexpr int KA_BUF = 45056;
constexpr int KA_STAT = 3 * KA_BUF;
constexpr int KA_SCS = KA_STAT + 2048, KA_SB1 = KA_SCS + 3584, KA_SMR = KA_SB1 + 384;
constexpr int KA_LDS = KA_SMR + 2048;
#define LBAR(N) asm volatile("s_waitcnt lgkmcnt(" #N ")\n\ts_barrier" ::: "memory")
#define LP(a) ((lds_ptr)(uintptr_t)(a))
#define SET_M0(v) asm volatile("s_mov_b32 m0, %0\n\ts_nop 0" :: "s"(v) : "memory")
#define NOP_ do { } while (0)
#define MFMA4(a, b, c) __builtin_amdgcn_mfma_f32_4x4x4bf16_1k((a), (b), (c), 0, 0, 0)

typedef __amdgpu_buffer_rsrc_t srd_t;
DI srd_t make_srd(const void* p, unsigned bytes) { return __builtin_amdgcn_make_buffer_rsrc((void*)p, (short)0, (int)bytes, 0x00020000); }
DI float x32_add(float v) {
  const auto rr = __builtin_amdgcn_permlane32_swap(__float_as_uint(v), __float_as_uint(v), false, false);
  return __uint_as_float(rr[0]) + __uint_as_float(rr[1]);
}
DI bf16x4 lo4(bf16x8 v) { return __builtin_shufflevector(v, v, 0, 1, 2, 3); }
DI bf16x4 hi4(bf16x8 v) { return __builtin_shufflevector(v, v, 4, 5, 6, 7); }

template <int TYPE, int nh>
DI void ka_body(lds_ptr lds, const char* __restrict__ x, const char* __restrict__ wqkv, const char* __restrict__ wh1,
                const unsigned short* __restrict__ b1, const char* __restrict__ w2, const char* __restrict__ wo,
                const int n0, const int f0, const int rb, const int cb, char* __restrict__ ws) {
  constexpr int NHT = nh ? 1 : 2;
  const int tid = threadIdx.x, lane = tid & 63;
  const int w = __builtin_amdgcn_readfirstlane(tid >> 6);
  const int mi = w & 3;
  const int r = lane & 31, h = lane >> 5;
  const unsigned lds0 = (unsigned)(uintptr_t)lds;
  STAMP(0, 0);
  const unsigned vx0 = (unsigned)((lane >> 3) * 1280 + (((lane & 7) ^ (lane >> 4)) << 4));
  const unsigned vx1 = (unsigned)((8 + (lane >> 3)) * 1280 + (((lane & 7) ^ (4 + (lane >> 4))) << 4) - 1024);
  const unsigned vw = (unsigned)((lane >> 3) * 3840 + ((((((lane >> 2) ^ (lane >> 4)) & 1) << 2) | (lane & 3)) << 4));
  const int p0 = w * 96 + lane, p1 = w * 96 + 64 + r;
  const int r0 = (p0 * 5462) >> 16, r1 = (p1 * 5462) >> 16;
  const unsigned vh0 = (unsigned)(r0 * 3072 + (p0 - r0 * 12) * 16), vh1 = (unsigned)(r1 * 3072 + (p1 - r1 * 12) * 16 - 1024);
  const char* xs = x + (size_t)(rb * 128 + 16 * w) * 1280;
  const char* wsrc = wqkv + ((size_t)(8 * w) * 1920 + n0) * 2;
  const char* hsrc = wh1 + f0 * 2;
  const unsigned mx = lds0 + (unsigned)w * 2048u, mw = lds0 + 16384u + (unsigned)w * 1024u, mh = lds0 + 32768u + (unsigned)w * 1536u;
#define KA_DX(c, buf) do { SET_M0(mx + (unsigned)(buf)); asm volatile("global_load_lds_dwordx4 %0, %2\n\tglobal_load_lds_dwordx4 %1, %2 offset:1024" :: "v"(vx0), "v"(vx1), "s"(xs + (c) * 128) : "memory"); } while (0)
#define KA_DW(c, buf, hf) do { SET_M0(mw + (unsigned)(buf) + (hf) * 8192u); asm volatile("global_load_lds_dwordx4 %0, %1" :: "v"(vw), "s"(wsrc + (size_t)(c) * (64 * 3840) + (hf) * 128) : "memory"); } while (0)
#define KA_DH(c, buf) do { SET_M0(mh + (unsigned)(buf)); asm volatile("global_load_lds_dwordx4 %0, %2\n\ts_mov_b32 exec_hi, 0\n\tglobal_load_lds_dwordx4 %1, %2 offset:1024\n\ts_mov_b32 exec_hi, -1" :: "v"(vh0), "v"(vh1), "s"(hsrc + (size_t)(c) * (64 * 3072)) : "memory"); } while (0)
#define KA_ISSUE(c, buf) do { KA_DX(c, buf); KA_DW(c, buf, 0); KA_DW(c, buf, 1); KA_DH(c, buf); } while (0)
  unsigned braw = 0, warm = 0;
  if (nh == 0 && tid < 96) braw = b1[f0 + tid];
  SBAR();
  KA_ISSUE(0, 0);
  KA_ISSUE(1, KA_BUF);

  unsigned xa[4], wa0[4], ha[4];
  {
    const unsigned wb = lds0 + (unsigned)(16384 + nh * 8192 + (8 * h + ((lane & 15) >> 2)) * 128 + 64 * ((lane >> 3) & 1) +
                                          (16 * ((lane >> 4) & 1) + 4 * (lane & 3)) * 2);
    const unsigned hb = lds0 + (unsigned)(32768 + (8 * h + ((lane & 15) >> 2)) * 192 + nh * 128 + (16 * ((lane >> 4) & 1) + 4 * (lane & 3)) * 2);
#pragma unroll
    for (int i = 0; i < 4; ++i) {
      const int ks = i ^ mi;
      xa[i] = lds0 + (unsigned)((mi * 32 + r) * 128 + (((2 * ks + h) ^ ((r >> 1) & 7)) << 4));
      wa0[i] = wb + (unsigned)(ks * 2048);
      ha[i] = hb + (unsigned)(ks * 3072);
    }
  }
  const int tq = rb * 4 + mi;
  const bool isq = n0 < 640;
  unsigned so[TYPE == 1 ? 2 : 4], split = 0u;
  if (TYPE == 1) {
#pragma unroll
    for (int j = 0; j < 2; ++j) {
      const int nn = n0 - 1280 + nh * 64 + j * 32 + r;
      const int head = (nn * 3277) >> 16;
      so[j] = (unsigned)(head * (64 * 1280) + (h * 20 + nn - head * 20) * 16);
      PIN(so[j]);
    }
  } else {
#pragma unroll
    for (int j = 0; j < 4; ++j) {
      const int G = (((isq ? n0 : n0 - 640) + nh * 64) >> 2) + 4 * j + 2 * h;
      const int head = (G * 205) >> 10;
      const int dg = G - head * 5;
      so[j] = (unsigned)(head * (64 * 1280) + (dg < 4 ? (dg >> 1) * 512 + r * 16 : 1024 + r * 8));
      if (dg == 4) split |= 1u << j;
      PIN(so[j]);
    }
    PIN(split);
  }
  f32x16 acc[2], ach[NHT];
  f32x4 cq[2], chh[NHT], rsum = {0.f, 0.f, 0.f, 0.f}, rsq = rsum;
#pragma unroll
  for (int i = 0; i < 16; ++i) {
    acc[0][i] = 0.f; acc[1][i] = 0.f;
#pragma unroll
    for (int j = 0; j < NHT; ++j) ach[j][i] = 0.f;
  }
  cq[0] = rsum; cq[1] = rsum;
#pragma unroll
  for (int j = 0; j < NHT; ++j) chh[j] = rsum;
  bf16x8 sel;
  {
    const short sv = (lane & 15) == ((lane >> 4) & 1) ? (short)0x3f80 : (short)0;
    sel = bf16x8{sv, sv, sv, sv, sv, sv, sv, sv};
    PIN(sel);
  }
  bf16x8 xf[3], w0[3], w1[3], wh[3][NHT];
  unsigned wa1[4];
#pragma unroll
  for (int i = 0; i < 4; ++i) wa1[i] = wa0[i] ^ 64u;
#define KA_READ(B, i)                                                                                           \
  do {                                                                                                          \
    constexpr int s_ = ((B) + (i)) % 3;                                                                         \
    xf[s_] = *(const LDS_AS bf16x8*)(LP(xa[i]) + BUFOFF(B));                                                    \
    w0[s_] = cat8(tr_read(LP(wa0[i]) + BUFOFF(B)), tr_read(LP(wa0[i]) + BUFOFF(B) + 512));                      \
    w1[s_] = cat8(tr_read(LP(wa1[i]) + BUFOFF(B)), tr_read(LP(wa1[i]) + BUFOFF(B) + 512));                      \
    _Pragma("unroll") for (int j_ = 0; j_ < NHT; ++j_)                                                          \
      wh[s_][j_] = cat8(tr_read(LP(ha[i]) + BUFOFF(B) + j_ * 64), tr_read(LP(ha[i]) + BUFOFF(B) + j_ * 64 + 768)); \
  } while (0)
#define SGB(m, n) __builtin_amdgcn_sched_group_barrier((m), (n), 0)
#define KA_STEP(B, i, RD) KA_STEPX(B, i, RD, true)
#define KA_STEPX(B, i, RD, ROWS)                                                                                \
  do {                                                                                                          \
    constexpr int b_ = ((B) + (i)) % 3;                                                                         \
    if constexpr (NHT == 2) asm volatile("" : "+v"(xf[b_]), "+v"(w0[b_]), "+v"(w1[b_]), "+v"(wh[b_][0]), "+v"(wh[b_][NHT - 1])); \
    else asm volatile("" : "+v"(xf[b_]), "+v"(w0[b_]), "+v"(w1[b_]), "+v"(wh[b_][0]));                          \
    SBAR();                                                                                                     \
    RD;                                                                                                         \
    if (TYPE == 1) { acc[0] = MFMA32(xf[b_], w0[b_], acc[0]); acc[1] = MFMA32(xf[b_], w1[b_], acc[1]); }        \
    else { acc[0] = MFMA32(w0[b_], xf[b_], acc[0]); acc[1] = MFMA32(w1[b_], xf[b_], acc[1]); }                  \
    _Pragma("unroll") for (int j_ = 0; j_ < NHT; ++j_) ach[j_] = MFMA32(wh[b_][j_], xf[b_], ach[j_]);           \
    SGB(0x8, 1); SGB(0x100, 3); SGB(0x8, 1); SGB(0x100, 2); SGB(0x8, 1); SGB(0x100, 2);                         \
    if (NHT == 2) { SGB(0x8, 1); SGB(0x100, 2); }                                                               \
    if constexpr (NHT == 2) asm volatile("" : "+v"(acc[0]), "+v"(acc[1]), "+v"(ach[0]), "+v"(ach[NHT - 1]));    \
    else asm volatile("" : "+v"(acc[0]), "+v"(acc[1]), "+v"(ach[0]));                                           \
    SBAR();                                                                                                     \
    if ((i) == 0) {                                                                              \
      cq[0] = MFMA16(w0[b_], sel, cq[0]); cq[1] = MFMA16(w1[b_], sel, cq[1]);                                   \
      _Pragma("unroll") for (int j_ = 0; j_ < NHT; ++j_) chh[j_] = MFMA16(wh[b_][j_], sel, chh[j_]);            \
      if constexpr (NHT == 2) asm volatile("" : "+v"(cq[0]), "+v"(cq[1]), "+v"(chh[0]), "+v"(chh[NHT - 1]));    \
      else asm volatile("" : "+v"(cq[0]), "+v"(cq[1]), "+v"(chh[0]));                                           \
    }                                                                                                           \
    if ((ROWS) && (nh ? (i) < 2 : (i) >= 2)) {                                                   \
      rsq = MFMA4(lo4(xf[b_]), lo4(xf[b_]), rsq); rsum = MFMA16(xf[b_], sel, rsum);                             \
      rsq2 = MFMA4(hi4(xf[b_]), hi4(xf[b_]), rsq2);                                                             \
      asm volatile("" : "+v"(rsum), "+v"(rsq), "+v"(rsq2));                                                     \
    }                                                                                                           \
    SBAR();                                                                                                     \
  } while (0)
#define BUFOFF(B) ((B) * KA_BUF)
#define KA_CHUNK(c, B, BN, WAITN, PREV, MORE)                                                                   \
  do {                                                                                                          \
    KA_STEP(B, 0, KA_READ(B, 2));                                                                               \
    if ((PREV)) { KA_DH((c) + 2, BUFOFF(((B) + 2) % 3)); SBAR(); }                               \
    KA_STEP(B, 1, KA_READ(B, 3));                                                                               \
    WAIT_BAR(WAITN);                                                                                            \
    KA_STEP(B, 2, KA_READ(BN, 0));                                                                              \
    if ((MORE)) { KA_DX((c) + 3, BUFOFF(B)); SBAR(); }                                           \
    KA_STEP(B, 3, KA_READ(BN, 1));                                                                              \
    if ((MORE)) { KA_DW((c) + 3, BUFOFF(B), 0); KA_DW((c) + 3, BUFOFF(B), 1); SBAR(); }          \
  } while (0)
  f32x4 rsq2 = rsum;
  WAIT_BAR(6);
  STAMP(0, 1);
  STAMPC(0, 14);
  KA_READ(0, 0);
  KA_READ(0, 1);
  KA_STEP(0, 0, KA_READ(0, 2));
  KA_DX(2, BUFOFF(2)); KA_DW(2, BUFOFF(2), 0); KA_DW(2, BUFOFF(2), 1); SBAR();
  KA_STEP(0, 1, KA_READ(0, 3));
  KA_DH(2, BUFOFF(2)); SBAR();
  WAIT_BAR(6);
  KA_STEP(0, 2, KA_READ(1, 0));
  KA_DX(3, BUFOFF(0)); SBAR();
  KA_STEP(0, 3, KA_READ(1, 1));
  KA_DW(3, BUFOFF(0), 0); KA_DW(3, BUFOFF(0), 1); SBAR();
#pragma unroll 1
  for (int c = 1; c < 7; c += 3) {
    KA_CHUNK(c, 1, 2, 6, true, true);
    KA_CHUNK(c + 1, 2, 0, 6, true, true);
    KA_CHUNK(c + 2, 0, 1, 6, true, true);
  }
  KA_CHUNK(7, 1, 2, 6, true, false);
  KA_CHUNK(8, 2, 0, 0, false, false);
  KA_STEP(0, 0, KA_READ(0, 2));
  KA_STEP(0, 1, KA_READ(0, 3));
  if (nh == 0) {
    asm volatile("" : "+v"(xf[2]), "+v"(xf[0]));
    rsq = MFMA4(lo4(xf[2]), lo4(xf[2]), rsq); rsum = MFMA16(xf[2], sel, rsum); rsq2 = MFMA4(hi4(xf[2]), hi4(xf[2]), rsq2);
    rsum = MFMA16(xf[0], sel, rsum); rsq = MFMA4(lo4(xf[0]), lo4(xf[0]), rsq); rsq2 = MFMA4(hi4(xf[0]), hi4(xf[0]), rsq2);
  }
  asm volatile("s_nop 7" : "+v"(rsum), "+v"(rsq), "+v"(rsq2));
  rsq += rsq2;
  STAMP(0, 3);
  STAMPC(0, 15);
  LDS_AS float* st_r = (LDS_AS float*)(lds + KA_STAT);
  LDS_AS float* st_cs = (LDS_AS float*)(lds + KA_SCS);
  LDS_AS float* st_b = (LDS_AS float*)(lds + KA_SB1);
  {
    const int d = lane & 3;
    const float sq = x32_add(d == 0 ? rsq[0] : d == 1 ? rsq[1] : d == 2 ? rsq[2] : rsq[3]);
    if (h == 0) st_r[256 + nh * 128 + mi * 32 + r] = sq;
    if ((lane & 15) < 2) {
      const int q4 = (lane & 1) * 16 + (lane >> 4) * 4;
      *(LDS_AS f32x4*)(st_r + nh * 128 + mi * 32 + q4) = rsum;
      *(LDS_AS f32x4*)(st_cs + mi * 224 + nh * 64 + q4) = cq[0];
      *(LDS_AS f32x4*)(st_cs + mi * 224 + nh * 64 + 32 + q4) = cq[1];
#pragma unroll
      for (int j = 0; j < NHT; ++j) *(LDS_AS f32x4*)(st_cs + mi * 224 + 128 + nh * 64 + j * 32 + q4) = chh[j];
    }
    if (nh == 0 && tid < 96) st_b[tid] = __uint_as_float(braw << 16);
  }
  SBAR();
  KA_STEPX(0, 2, NOP_, false);
  LBAR(0);
  float cpart = 0.f;
  if (tid < 224) cpart = (st_cs[tid] + st_cs[224 + tid]) + (st_cs[448 + tid] + st_cs[672 + tid]);
  const float sx_ = st_r[mi * 32 + r] + st_r[128 + mi * 32 + r], sq_ = st_r[256 + mi * 32 + r] + st_r[384 + mi * 32 + r];
  SBAR();
  KA_STEPX(0, 3, NOP_, false);
#undef KA_READ
#undef KA_STEP
#undef KA_STEPX
#undef KA_CHUNK
#undef KA_ISSUE
  if (tid < 224) st_cs[tid] = cpart;
  {
    const char* ob = ws + WS_O + (size_t)blockIdx.x * 10240;
    const u32x4 ff = {0xffffffffu, 0xffffffffu, 0xffffffffu, 0xffffffffu};
    st16s(ob, (unsigned)tid * 16u, ff);
    if (w < 2) st16s(ob + 8192, (unsigned)tid * 16u, ff);
  }
  if (nh == 1) {
    const int l0_ = blockIdx.x * 85 + (tid - 256 < 85 ? tid - 256 : 0);
    const char* p = l0_ < 15360 ? w2 + (size_t)l0_ * 128 : wo + (size_t)(l0_ - 15360) * 128;
    if (w < 6) asm volatile("global_load_dword %0, %1, off" : "=v"(warm) : "v"(p) : "memory");
  }
  const float mu = sx_ * (1.0f / D_);
  const float var = fmaxf(sq_ * (1.0f / D_) - mu * mu, 0.f);
  const float rs = __builtin_amdgcn_rsqf(var + LN_EPS);
  LDS_AS f32x2* st_w = (LDS_AS f32x2*)(lds + KA_SMR) + w * 32;
  if (TYPE == 1 && h == 0) st_w[r] = f32x2{mu * rs, rs};
  LBAR(0);
  STAMP(0, 5);
  if (cb < 15) {
    if (TYPE == 1) {
      char* dst = ws + WS_V + (size_t)tq * 1280;
      const float cs0 = st_cs[nh * 64 + r], cs1 = st_cs[nh * 64 + 32 + r];
#pragma unroll
      for (int s = 0; s < 2; ++s) {
        f32x2 mr[8];
#pragma unroll
        for (int e = 0; e < 8; ++e) mr[e] = st_w[16 * s + 8 * (e >> 2) + 4 * h + (e & 3)];
        u32x4 o0, o1;
#pragma unroll
        for (int e = 0; e < 4; ++e) {
          const int i = 8 * s + 2 * e;
          o0[e] = cvtpk(acc[0][i] * mr[2 * e][1] - mr[2 * e][0] * cs0, acc[0][i + 1] * mr[2 * e + 1][1] - mr[2 * e + 1][0] * cs0);
          o1[e] = cvtpk(acc[1][i] * mr[2 * e][1] - mr[2 * e][0] * cs1, acc[1][i + 1] * mr[2 * e + 1][1] - mr[2 * e + 1][0] * cs1);
        }
        st16s(dst + s * 640, so[0], o0);
        st16s(dst + s * 640, so[1], o1);
      }
    } else {
      const float sc = isq ? rs * QSCALE : rs, ms = mu * sc;
      char* base = ws + (isq ? WS_Q : WS_K) + (size_t)tq * 1280;
#pragma unroll
      for (int j = 0; j < 2; ++j)
#pragma unroll
        for (int u = 0; u < 2; ++u) {
          u32x2 o[2];
#pragma unroll
          for (int gg = 0; gg < 2; ++gg) {
            const int g = 2 * u + gg;
            const f32x4 cs = *(const LDS_AS f32x4*)(st_cs + nh * 64 + j * 32 + 8 * g + 4 * h);
            o[gg] = u32x2{cvtpk(acc[j][4 * g] * sc - ms * cs[0], acc[j][4 * g + 1] * sc - ms * cs[1]),
                          cvtpk(acc[j][4 * g + 2] * sc - ms * cs[2], acc[j][4 * g + 3] * sc - ms * cs[3])};
          }
          const auto a = __builtin_amdgcn_permlane32_swap(o[0][0], o[1][0], false, false);
          const auto b = __builtin_amdgcn_permlane32_swap(o[0][1], o[1][1], false, false);
          const unsigned off = so[j * 2 + u];
          if ((split >> (j * 2 + u)) & 1u) {
            st8s(base, off, u32x2{a[0], b[0]});
            st8s(base + 64 * 1280, off, u32x2{a[1], b[1]});
          } else {
            st16s(base, off, u32x4{a[0], b[0], a[1], b[1]});
          }
        }
    }
  }
  {
    const float sc = rs * __builtin_amdgcn_rsqf(var * rs * rs + LN_EPS), ms = mu * sc;
    constexpr float GK1 = -2.0f * 1.4426950408889634f * 0.7978845608028654f, GK2 = GK1 * 0.044715f;
    const char* dst = ws + WS_H + ((size_t)(rb * 8 + mi * 2) * 48 + (f0 >> 5) + nh * 2) * 1024;
    const unsigned ho = (unsigned)((r >> 4) * (48 * 1024) + (h * 16 + (r & 15)) * 16);
    const f32x2 sc2 = {sc, sc}, ms2 = {ms, ms}, k1 = {GK1, GK1}, k2 = {GK2, GK2}, one2 = {1.0f, 1.0f};
#pragma unroll
    for (int j = 0; j < NHT; ++j)
#pragma unroll
      for (int u = 0; u < 2; ++u) {
        u32x4 o;
#pragma unroll
        for (int gg = 0; gg < 2; ++gg) {
          const int g = 2 * u + gg;
          const f32x4 cs = *(const LDS_AS f32x4*)(st_cs + 128 + nh * 64 + j * 32 + 8 * g + 4 * h);
          const f32x4 bb = *(const LDS_AS f32x4*)(st_b + nh * 64 + j * 32 + 8 * g + 4 * h);
#pragma unroll
          for (int e = 0; e < 2; ++e) {
            const f32x2 a2 = {ach[j][4 * g + 2 * e], ach[j][4 * g + 2 * e + 1]};
            const f32x2 c2 = {cs[2 * e], cs[2 * e + 1]}, b2_ = {bb[2 * e], bb[2 * e + 1]};
            const f32x2 t = a2 * sc2 + (b2_ - ms2 * c2);
            const f32x2 arg = t * ((t * t) * k2 + k1);
            const f32x2 den = f32x2{__builtin_amdgcn_exp2f(arg[0]), __builtin_amdgcn_exp2f(arg[1])} + one2;
            const f32x2 v = t * f32x2{__builtin_amdgcn_rcpf(den[0]), __builtin_amdgcn_rcpf(den[1])};
            o[2 * gg + e] = cvtpk(v[0], v[1]);
          }
        }
        st16s(dst + j * 1024 + u * 512, ho, o);
      }
  }
  STAMP(0, 4);
  asm volatile("s_waitcnt vmcnt(0)" : "+v"(warm) :: "memory");
}

__global__ __launch_bounds__(512, 2) void ka_proj(const char* __restrict__ x, const char* __restrict__ wqkv,
                                                  const char* __restrict__ w1, const unsigned short* __restrict__ b1,
                                                  const char* __restrict__ w2, const char* __restrict__ wo,
                                                  char* __restrict__ ws) {
  extern __shared__ __attribute__((aligned(16))) char smem[];
  lds_ptr lds = (lds_ptr)smem;
  const int bid = blockIdx.x, xg = bid & 7, idx = bid >> 3;
  const int rb = (xg & 1) * 8 + (idx & 7), cb = (xg >> 1) * 4 + (idx >> 3);
  const int n0 = (cb < 15 ? cb : 14) * 128, f0 = cb * 96;
  const int nh = __builtin_amdgcn_readfirstlane(threadIdx.x >> 8);
  if (cb < 10) {
    if (nh) ka_body<0, 1>(lds, x, wqkv, w1, b1, w2, wo, n0, f0, rb, cb, ws);
    else ka_body<0, 0>(lds, x, wqkv, w1, b1, w2, wo, n0, f0, rb, cb, ws);
  } else {
    if (nh) ka_body<1, 1>(lds, x, wqkv, w1, b1, w2, wo, n0, f0, rb, cb, ws);
    else ka_body<1, 0>(lds, x, wqkv, w1, b1, w2, wo, n0, f0, rb, cb, ws);
  }
}

DI u32x4 bload16_dev(srd_t r, unsigned voff, unsigned soff) { return __builtin_amdgcn_raw_buffer_load_b128(r, (int)voff, (int)soff, 16); }

constexpr int AT_TILE = 3584;
constexpr int AT_SLOT = 28672;
constexpr int KB_WR = 2 * AT_SLOT;
constexpr int KB_GRP = 40960;
constexpr int KB_RED = KB_WR + 2 * KB_GRP;
constexpr int KB_LDS = KB_RED + 20480;

__global__ __launch_bounds__(512, 2) void kb_main(const char* __restrict__ x, const char* __restrict__ w2,
                                                  const unsigned short* __restrict__ b2, const char* __restrict__ wo,
                                                  char* __restrict__ ws, char* __restrict__ out) {
  extern __shared__ __attribute__((aligned(16))) char smem[];
  lds_ptr lds = (lds_ptr)smem;
  const int tid = threadIdx.x, lane = tid & 63;
  const int wid = __builtin_amdgcn_readfirstlane(tid >> 6);
  const int r = lane & 31, h = lane >> 5;
  const int xg = blockIdx.x & 7, idx = blockIdx.x >> 3;
  const int head = xg * 4 + (idx >> 3), qb = idx & 7;
  const int rbk = xg * 4 + (idx & 3), cbk = idx >> 2;
  const int mi = wid & 3, kh = wid >> 2, gq = lane >> 4;
  const unsigned lds0 = (unsigned)(uintptr_t)lds;
  const char* Kh = ws + WS_K + (size_t)head * (64 * 1280);
  const char* Vh = ws + WS_V + (size_t)head * (64 * 1280);
  const char* Qt = ws + WS_Q + (size_t)(head * 64 + qb * 8 + wid) * 1280;
  STAMP(1, 0);

  const unsigned vlin = (unsigned)lane * 16u;
  const unsigned vv = (unsigned)(h * 20 + r) * 16u;
  const unsigned vv2 = vv + (unsigned)(WS_V - WS_K) - 1280u - 1536u, vv3 = vv2 + 640u - 1024u;
  const unsigned long long mk16 = 0xffffull, mk20 = 0x000fffff000fffffull;
#define KV_M0(c) SET_M0(lds0 + (unsigned)(((c) & 1) * AT_SLOT + wid * AT_TILE))
#define KV_SRC(c) (Kh + (size_t)((c) * 8 + wid + 1) * 1280)
#define KV_P0(c) asm volatile("global_load_lds_dwordx4 %0, %1" :: "v"(vlin), "s"(KV_SRC(c)) : "memory")
#define KV_P1(c) asm volatile("s_mov_b64 exec, %2\n\tglobal_load_lds_dwordx4 %0, %1 offset:1024\n\ts_mov_b64 exec, -1" :: "v"(vlin), "s"(KV_SRC(c)), "s"(mk16) : "memory")
#define KV_P23(c) asm volatile("s_mov_b64 exec, %3\n\tglobal_load_lds_dwordx4 %0, %2 offset:1536\n\tglobal_load_lds_dwordx4 %1, %2 offset:2560\n\ts_mov_b64 exec, -1" :: "v"(vv2), "v"(vv3), "s"(KV_SRC(c)), "s"(mk20) : "memory")
  bf16x8 qa, ka0;
  u32x2 qbv, kb0;
  {
    const unsigned v8 = 1024u + (unsigned)r * 8u;
    asm volatile("global_load_dwordx4 %0, %4, %6\n\tglobal_load_dwordx2 %1, %5, %6\n\t"
                 "global_load_dwordx4 %2, %4, %7\n\tglobal_load_dwordx2 %3, %5, %7"
                 : "=&v"(qa), "=&v"(qbv), "=&v"(ka0), "=&v"(kb0) : "v"(vlin), "v"(v8), "s"(Qt), "s"(Kh) : "memory");
  }
  KV_M0(0); KV_P0(0); KV_P1(0); KV_P23(0);
#pragma unroll
  for (int sl = 0; sl < 2; ++sl) {
    lds_ptr tp = lds + sl * AT_SLOT + wid * AT_TILE;
    if (lane < 32) *(LDS_AS u32x2*)(tp + 1024 + 256 + lane * 8) = u32x2{0x00003f80u, 0u};
    if (lane < 48) {
      const int blk4 = (lane * 43) >> 9, dd = 20 + lane - blk4 * 12;
      const unsigned val = dd == 20 ? 0x3f803f80u : 0u;
      *(LDS_AS u32x4*)(tp + 1536 + blk4 * 512 + dd * 16) = u32x4{val, val, val, val};
    }
  }
  unsigned vo[5];
#pragma unroll
  for (int i = 0; i < 5; ++i) {
    const int p = i * 64 + lane, row = (p * 6554) >> 16;
    vo[i] = (unsigned)(row * 1280 + (p - row * 10) * 16 - (i & 3) * 1024);
  }
#define W_SRC(W, grp) ((W) + cbk * 160 + (size_t)((grp) * 256 + wid * 32) * 1280)
#define W_M0(reg, hi) SET_M0(lds0 + (unsigned)(KB_WR + (reg) * KB_GRP + wid * 5120 + (hi) * 4096))
#define W_P(W, i, grp) asm volatile("global_load_lds_dwordx4 %0, %1 offset:%2" :: "v"(vo[i]), "s"(W_SRC(W, grp)), "i"(((i) & 3) * 1024) : "memory")
  const char* hsrc = ws + WS_H + ((size_t)(rbk * 4 + mi) * 48 + kh) * 1024 + 4096;
#define H_LOAD(D, j, grp) asm volatile("global_load_dwordx4 %0, %1, %2 offset:%3" : "=v"(D[j]) : "v"(vlin), "s"(hsrc + (grp) * 8192), "i"((j) * 2048 - 4096) : "memory")
  const unsigned wra = lds0 + (unsigned)(KB_WR + (kh * 32 + 4 * ((gq & 1) + 4 * (gq >> 1)) + ((lane & 15) >> 2)) * 160 + (lane & 3) * 8);
  const unsigned wrb = wra + KB_GRP;

  unsigned oo[3];
#pragma unroll
  for (int g = 0; g < 3; ++g) {
    const int Gc = head * 5 + 2 * g + h, G8 = Gc & 7;
    oo[g] = (unsigned)((r >> 4) * (20 * 1024) + (r & 15) * 16 + (Gc >> 3) * 1024 + ((G8 >> 2) * 2 + (G8 & 1)) * 256 + ((G8 >> 1) & 1) * 8);
    PIN(oo[g]);
  }
  bf16x8 hA[4], hB[4];
  asm volatile("s_waitcnt vmcnt(4)" : "+v"(qa), "+v"(qbv), "+v"(ka0), "+v"(kb0) :: "memory");
  f32x16 o;
#pragma unroll
  for (int i = 0; i < 16; ++i) o[i] = 0.f;
  const f32x16 zero = o;
  if (h == 1) { qbv[0] = 0u; qbv[1] = 0u; kb0[0] = 0x00003f80u; kb0[1] = 0u; }
  const f32x16 s16 = MFMA32(ka0, qa, zero);
  f32x16 sA = MFMA32K8(__builtin_bit_cast(bf16x4, kb0), __builtin_bit_cast(bf16x4, qbv), s16);
  {
    float m = fmaxf(fmaxf(sA[0], sA[1]), sA[2]);
#pragma unroll
    for (int i = 3; i < 15; i += 2) m = fmaxf(fmaxf(m, sA[i]), sA[i + 1]);
    m = fmaxf(m, sA[15]);
    const auto mm = __builtin_amdgcn_permlane32_swap(__float_as_uint(m), __float_as_uint(m), false, false);
    m = fmaxf(__uint_as_float(mm[0]), __uint_as_float(mm[1]));
    if (h == 1) qbv[0] = cvtpk(-m, 0.f);
  }
  const bf16x4 qb4 = __builtin_bit_cast(bf16x4, qbv);
  sA = MFMA32K8(__builtin_bit_cast(bf16x4, kb0), qb4, s16);
  f32x16 sB;
  f32x4 acc[5];
#pragma unroll
  for (int j = 0; j < 5; ++j) acc[j] = f32x4{0.f, 0.f, 0.f, 0.f};
  WAIT_BAR(0);
  STAMP(1, 1);
  STAMPC(1, 14);
  STAMP2(2, 0);

  unsigned a16 = lds0 + vlin, a8 = lds0 + (unsigned)lane * 8u;
  int sdelta = AT_SLOT;
  bf16x8 kaA, v0A, v1A, kaB, v0B, v1B;
  bf16x4 kbA, kbB;
  bf16x4 wl[5], wu[5];
  u32x4 p0, p1 = {0u, 0u, 0u, 0u};
  v1B = __builtin_bit_cast(bf16x8, p1);
#define RD_KA(X, j) ka##X = *(const LDS_AS bf16x8*)(LP(a16) + (j) * AT_TILE)
#define RD_KB(X, j) kb##X = *(const LDS_AS bf16x4*)(LP(a8) + (j) * AT_TILE + 1024)
#define RD_V0(X, j) v0##X = *(const LDS_AS bf16x8*)(LP(a16) + (j) * AT_TILE + 1536)
#define RD_V1(X, j) v1##X = *(const LDS_AS bf16x8*)(LP(a16) + (j) * AT_TILE + 2560)
#define W_LO(WA, j, t) wl[t] = tr_read(LP(WA) + (j) * 10240 + (t) * 32)
#define W_HI(WA, j, t) wu[t] = tr_read(LP(WA) + (j) * 10240 + (t) * 32 + 1280)
#define G_MM(HS, j, t) acc[t] = MFMA16(cat8(wl[t], wu[t]), HS[j], acc[t])
#define EX2(S, i) do { S[i] = __builtin_amdgcn_exp2f(S[i]); S[i + 1] = __builtin_amdgcn_exp2f(S[i + 1]); } while (0)
#define MID() do { __builtin_amdgcn_sched_group_barrier(0x400, 2, 0); __builtin_amdgcn_sched_group_barrier(0x008, 1, 0); \
                    __builtin_amdgcn_sched_group_barrier(0x400, 2, 0); __builtin_amdgcn_sched_group_barrier(0x002, 2, 0); } while (0)
#define AT_STEP(SC, SN, X, Y, JN, PRE, WP, A0, A1, A2, A3, A4, A5, V0_, V1_, V2_, M1, M2, M3)            \
  do {                                                                                                   \
    PRE;                                                                                                 \
    asm volatile("" : "+v"(ka##X), "+v"(kb##X), "+v"(v0##X), "+v"(v1##X)); WP; SBAR();                   \
    EX2(SC, 0); PIN(SC); SBAR();                                                                         \
    o = MFMA32(v1##Y, __builtin_bit_cast(bf16x8, p1), o); SBAR();                                        \
    RD_KA(Y, JN); RD_KB(Y, JN); RD_V0(Y, JN); RD_V1(Y, JN); A0; A1; A2; A3; A4; A5; V0_; V1_; SBAR(); \
    M1; EX2(SC, 2); EX2(SC, 4); p0[0] = cvtpk(SC[0], SC[1]); p0[1] = cvtpk(SC[2], SC[3]); MID();         \
    asm volatile("" : "+v"(p0), "+v"(SC), "+v"(acc[0]), "+v"(acc[3]), "+v"(o)); SBAR();                  \
    SN = MFMA32(ka##X, qa, zero); SBAR();                                                                \
    M2; EX2(SC, 6); EX2(SC, 8); p0[2] = cvtpk(SC[4], SC[5]); p0[3] = cvtpk(SC[6], SC[7]); MID();         \
    asm volatile("" : "+v"(p0), "+v"(SC), "+v"(acc[1])); SBAR();                                         \
    SN = MFMA32K8(kb##X, qb4, SN); SBAR();                                                               \
    V2_; SBAR();                                                                                         \
    M3; EX2(SC, 10); EX2(SC, 12); p1[0] = cvtpk(SC[8], SC[9]); p1[1] = cvtpk(SC[10], SC[11]); MID();     \
    asm volatile("" : "+v"(p1), "+v"(SC), "+v"(acc[2]), "+v"(acc[4])); SBAR();                           \
    o = MFMA32(v0##X, __builtin_bit_cast(bf16x8, p0), o); SBAR();                                        \
    EX2(SC, 14); p1[2] = cvtpk(SC[12], SC[13]); p1[3] = cvtpk(SC[14], SC[15]);                           \
    asm volatile("" : "+v"(p1), "+v"(o)); SBAR();                                                        \
  } while (0)
#define WP_E asm volatile("" : "+v"(wl[0]), "+v"(wu[0]), "+v"(wl[1]), "+v"(wu[1]), "+v"(wl[2]), "+v"(wu[2]))
#define WP_O asm volatile("" : "+v"(wl[3]), "+v"(wu[3]), "+v"(wl[4]), "+v"(wu[4]))
#define FLIP() do { a16 += (unsigned)sdelta; a8 += (unsigned)sdelta; sdelta = -sdelta; } while (0)
#define SYNC_H(HN) asm volatile("s_waitcnt vmcnt(3) lgkmcnt(0)\n\ts_barrier" : "+v"(HN[0]) :: "memory")
#define WAIT_H(HC, j, N) asm volatile("s_waitcnt vmcnt(" #N ")" : "+v"(HC[j]) :: "memory")
#define STEP_E(JN, PRE, WC, HC, j, VA, VB)                                                               \
  AT_STEP(sA, sB, A, B, JN, PRE, WP_E, W_LO(WC, j, 3), W_HI(WC, j, 3), W_LO(WC, j, 4), W_HI(WC, j, 4), NOP_, NOP_, NOP_, VA, VB, \
          G_MM(HC, j, 0), G_MM(HC, j, 1), G_MM(HC, j, 2))
#define STEP_O(JN, PRE, WA, jn, HC, j, VA, VB)                                                           \
  AT_STEP(sB, sA, B, A, JN, PRE, WP_O, W_LO(WA, jn, 0), W_HI(WA, jn, 0), W_LO(WA, jn, 1), W_HI(WA, jn, 1), W_LO(WA, jn, 2), \
          W_HI(WA, jn, 2), NOP_, VA, VB, G_MM(HC, j, 3), NOP_, G_MM(HC, j, 4))
#define CHUNK_G(c, WC, HC, WN, HN, RN, NW, NG, HG)                                                       \
  do {                                                                                                   \
    STEP_E(1, NOP_, WC, HC, 0, W_M0(RN, 0); W_P(NW, 0, NG), W_P(NW, 1, NG));                             \
    STEP_O(2, NOP_, WC, 1, HC, 0, W_P(NW, 2, NG), W_P(NW, 3, NG));                                       \
    STEP_E(3, WAIT_H(HC, 1, 6), WC, HC, 1, W_M0(RN, 1); W_P(NW, 4, NG), KV_M0((c) + 1); KV_P0((c) + 1)); \
    STEP_O(4, NOP_, WC, 2, HC, 1, KV_P1((c) + 1), KV_P23((c) + 1));                                      \
    STEP_E(5, WAIT_H(HC, 2, 10), WC, HC, 2, H_LOAD(HN, 0, HG), H_LOAD(HN, 1, HG));                       \
    STEP_O(6, NOP_, WC, 3, HC, 2, H_LOAD(HN, 2, HG), H_LOAD(HN, 3, HG));                                 \
    STEP_E(7, WAIT_H(HC, 3, 13), WC, HC, 3, NOP_, NOP_);                                                 \
    STEP_O(0, SYNC_H(HN); STAMP2(2, 1 + (c)); FLIP(), WN, 0, HC, 3, NOP_, NOP_);                         \
  } while (0)
#define PLAIN2_E(JN, VA, VB) AT_STEP(sA, sB, A, B, JN, NOP_, NOP_, NOP_, NOP_, NOP_, NOP_, NOP_, NOP_, NOP_, VA, VB, NOP_, NOP_, NOP_)
#define PLAIN2_O(JN, VA, VB) AT_STEP(sB, sA, B, A, JN, NOP_, NOP_, NOP_, NOP_, NOP_, NOP_, NOP_, NOP_, NOP_, VA, VB, NOP_, NOP_, NOP_)
#define PLAIN_E(JN, X0, X1, X2, X3) AT_STEP(sA, sB, A, B, JN, NOP_, NOP_, X0, X1, X2, X3, NOP_, NOP_, NOP_, NOP_, NOP_, NOP_, NOP_, NOP_)
#define PLAIN_O(JN, X0, X1, X2, X3) AT_STEP(sB, sA, B, A, JN, NOP_, NOP_, X0, X1, X2, X3, NOP_, NOP_, NOP_, NOP_, NOP_, NOP_, NOP_, NOP_)

  RD_KA(A, 0); RD_KB(A, 0); RD_V0(A, 0); RD_V1(A, 0);
  PLAIN2_E(1, W_M0(0, 0); W_P(w2, 0, 0), W_P(w2, 1, 0));
  PLAIN2_O(2, W_P(w2, 2, 0), W_P(w2, 3, 0));
  PLAIN2_E(3, W_M0(0, 1); W_P(w2, 4, 0), KV_M0(1); KV_P0(1));
  PLAIN2_O(4, KV_P1(1), KV_P23(1));
  PLAIN2_E(5, H_LOAD(hA, 0, 0), H_LOAD(hA, 1, 0));
  PLAIN2_O(6, H_LOAD(hA, 2, 0), H_LOAD(hA, 3, 0));
  PLAIN_E(7, NOP_, NOP_, NOP_, NOP_);
  AT_STEP(sB, sA, B, A, 0, SYNC_H(hA); STAMP2(2, 1); FLIP(), NOP_, W_LO(wra, 0, 0), W_HI(wra, 0, 0), W_LO(wra, 0, 1), W_HI(wra, 0, 1),
          W_LO(wra, 0, 2), W_HI(wra, 0, 2), NOP_, NOP_, NOP_, NOP_, NOP_, NOP_);
#pragma unroll 1
  for (int c = 1; c < 5; c += 2) {
    CHUNK_G(c, wra, hA, wrb, hB, 1, w2, c, c);
    CHUNK_G(c + 1, wrb, hB, wra, hA, 0, w2, c + 1, c + 1);
  }
  CHUNK_G(5, wra, hA, wrb, hB, 1, w2, 5, 5);
  const int tok = rbk * 64 + mi * 16 + (lane & 15);
  const unsigned xo = (unsigned)(tok * 1280 + (cbk * 80 + kh * 48 + 4 * gq) * 2);
  const int j2 = kh ? 1 : 2;
  LDS_AS f32x4* red = (LDS_AS f32x4*)(lds + KB_RED) + (mi * 5) * 64 + lane;
  u32x2 gv[3], xv[3];
  PLAIN2_E(1, KV_M0(7); KV_P0(7), KV_P1(7));
  PLAIN2_O(2, KV_P23(7), W_M0(0, 0); W_P(wo, 0, 0));
  PLAIN2_E(3, W_P(wo, 1, 0), W_P(wo, 2, 0));
  PLAIN2_O(4, W_P(wo, 3, 0), W_M0(0, 1); W_P(wo, 4, 0));
  PLAIN2_E(5, NOP_, NOP_);
  PLAIN2_O(6, NOP_, NOP_);
  PLAIN2_E(7, NOP_, NOP_);
  AT_STEP(sB, sA, B, A, 0,
          asm volatile("s_waitcnt vmcnt(0) lgkmcnt(0)\n\ts_barrier" : "+v"(hB[1]), "+v"(hB[2]), "+v"(hB[3]) :: "memory"); STAMP2(2, 7); FLIP(),
          NOP_, NOP_, NOP_, NOP_, NOP_, NOP_, NOP_, NOP_, NOP_, NOP_, NOP_, NOP_, NOP_);
  PLAIN2_E(1, _Pragma("unroll") for (int j = 0; j < 3; ++j) gv[j] = *(const u32x2*)(b2 + cbk * 80 + kh * 48 + (j < 2 ? j : j2) * 16 + 4 * gq), NOP_);
  PLAIN2_O(2, _Pragma("unroll") for (int j = 0; j < 3; ++j) xv[j] = *(const u32x2*)(x + xo + (j < 2 ? j : j2) * 32), NOP_);
  PLAIN2_E(3, NOP_, NOP_);
  PLAIN2_O(4, NOP_, NOP_);
  PLAIN2_E(5, _Pragma("unroll") for (int j = 0; j < 3; ++j) { PIN(gv[j]); PIN(xv[j]); }
           SET_M0(lds0 + (unsigned)(wid * 2560));
           asm volatile("global_load_lds_dwordx4 %0, %3\n\tglobal_load_lds_dwordx4 %1, %3 offset:1024\n\t"
                        "s_mov_b32 exec_hi, 0\n\tglobal_load_lds_dwordx4 %2, %3 offset:2048\n\ts_mov_b32 exec_hi, -1"
                        :: "v"(vo[0]), "v"(vo[1]), "v"(vo[2]), "s"(wo + cbk * 160 + (size_t)(256 + wid * 16) * 1280) : "memory"), NOP_);
  PLAIN2_O(6, NOP_, NOP_);
  PLAIN2_E(7, NOP_, NOP_);
  EX2(sB, 0); PIN(sB); SBAR();
  o = MFMA32(v1A, __builtin_bit_cast(bf16x8, p1), o); SBAR();
  EX2(sB, 2); EX2(sB, 4); EX2(sB, 6);
  p0 = u32x4{cvtpk(sB[0], sB[1]), cvtpk(sB[2], sB[3]), cvtpk(sB[4], sB[5]), cvtpk(sB[6], sB[7])};
  EX2(sB, 8); asm volatile("" : "+v"(p0), "+v"(sB)); SBAR();
  o = MFMA32(v0B, __builtin_bit_cast(bf16x8, p0), o); SBAR();
  EX2(sB, 10); EX2(sB, 12); EX2(sB, 14);
  p1 = u32x4{cvtpk(sB[8], sB[9]), cvtpk(sB[10], sB[11]), cvtpk(sB[12], sB[13]), cvtpk(sB[14], sB[15])};
  o = MFMA32(v1B, __builtin_bit_cast(bf16x8, p1), o);

  STAMP(1, 3);
  STAMPC(1, 15);
  STAMP2(2, 8);
  float l = __uint_as_float(__builtin_amdgcn_permlane32_swap(__float_as_uint(o[8]), __float_as_uint(o[8]), false, false)[1]);
  float big = fmaxf(fmaxf(__builtin_fabsf(o[0]), __builtin_fabsf(o[1])), l);
#pragma unroll
  for (int i = 2; i < 12; i += 2) big = fmaxf(fmaxf(big, __builtin_fabsf(o[i])), __builtin_fabsf(o[i + 1]));
  bool unsafe = !(big < 1.0e38f) || !(l > 0.25f);
#pragma unroll
  for (int i = 0; i < 12; ++i) unsafe |= o[i] != o[i];
  if (__builtin_expect(__any(unsafe), 0)) {
    if (h == 1) { qbv[0] = 0u; }
    const bf16x4 qz = __builtin_bit_cast(bf16x4, qbv);
    float m = -3.0e38f;
#pragma unroll
    for (int i = 0; i < 16; ++i) o[i] = 0.f;
    const unsigned onev = r == 20 ? 0x3f803f80u : 0u;
#pragma unroll 1
    for (int t = 0; t < 64; ++t) {
      const bf16x8 ka = *(const bf16x8*)(Kh + t * 1280 + lane * 16);
      u32x2 kb = *(const u32x2*)(Kh + t * 1280 + 1024 + r * 8);
      u32x4 v0 = {onev, onev, onev, onev}, v1 = v0;
      if (r < 20) {
        v0 = *(const u32x4*)(Vh + t * 1280 + vv);
        v1 = *(const u32x4*)(Vh + t * 1280 + 640 + vv);
      }
      f32x16 s = MFMA32(ka, qa, zero);
      s = MFMA32K8(__builtin_bit_cast(bf16x4, kb), qz, s);
      float tm = s[0];
#pragma unroll
      for (int i = 1; i < 16; ++i) tm = fmaxf(tm, s[i]);
      tm = fmaxf(tm, __shfl_xor(tm, 32));
      const float mn = fmaxf(m, tm);
      const float al = __builtin_amdgcn_exp2f(m - mn);
      m = mn;
      u32x4 q0, q1;
#pragma unroll
      for (int i = 0; i < 16; ++i) { o[i] *= al; s[i] = __builtin_amdgcn_exp2f(s[i] - mn); }
#pragma unroll
      for (int e = 0; e < 4; ++e) { q0[e] = cvtpk(s[2 * e], s[2 * e + 1]); q1[e] = cvtpk(s[8 + 2 * e], s[9 + 2 * e]); }
      o = MFMA32(__builtin_bit_cast(bf16x8, v0), __builtin_bit_cast(bf16x8, q0), o);
      o = MFMA32(__builtin_bit_cast(bf16x8, v1), __builtin_bit_cast(bf16x8, q1), o);
    }
    l = __shfl(o[8], 32 + r);
  }
  {
    const float inv = __builtin_amdgcn_rcpf(l);
    const char* ob = ws + WS_O + (size_t)(qb * 16 + wid * 2) * (20 * 1024);
#pragma unroll
    for (int g = 0; g < 3; ++g)
      if (g < 2 || h == 0)
        st8s(ob, oo[g], u32x2{min(cvtpk(o[4 * g] * inv, o[4 * g + 1] * inv), 0xfffffffeu),
                              min(cvtpk(o[4 * g + 2] * inv, o[4 * g + 3] * inv), 0xfffffffeu)});
  }
  STAMP(1, 4);
  {
    bf16x8 wf[2][5];
#pragma unroll
    for (int t = 0; t < 5; ++t) wf[0][t] = cat8(tr_read(LP(wrb) + t * 32), tr_read(LP(wrb) + t * 32 + 1280));
#pragma unroll
    for (int j = 0; j < 4; ++j) {
      SBAR();
      if (j < 3) {
#pragma unroll
        for (int t = 0; t < 5; ++t)
          wf[(j + 1) & 1][t] = cat8(tr_read(LP(wrb) + (j + 1) * 10240 + t * 32), tr_read(LP(wrb) + (j + 1) * 10240 + t * 32 + 1280));
      }
      SBAR();
#pragma unroll
      for (int t = 0; t < 5; ++t) acc[t] = MFMA16(wf[j & 1][t], hB[j], acc[t]);
      asm volatile("" : "+v"(acc[0]), "+v"(acc[1]), "+v"(acc[2]), "+v"(acc[3]), "+v"(acc[4]));
    }
    asm volatile("s_nop 7" : "+v"(acc[0]), "+v"(acc[1]), "+v"(acc[2]), "+v"(acc[3]), "+v"(acc[4]));
    if (kh == 1) { red[0] = acc[0]; red[64] = acc[1]; red[128] = acc[2]; } else { red[192] = acc[3]; red[256] = acc[4]; }
    asm volatile("s_waitcnt vmcnt(3) lgkmcnt(0)\n\ts_barrier" ::: "memory");
    const char* s_ = wo + cbk * 160 + (size_t)(384 + wid * 32) * 1280;
    W_M0(1, 0);
    asm volatile("global_load_lds_dwordx4 %0, %4\n\tglobal_load_lds_dwordx4 %1, %4 offset:1024\n\t"
                 "global_load_lds_dwordx4 %2, %4 offset:2048\n\tglobal_load_lds_dwordx4 %3, %4 offset:3072"
                 :: "v"(vo[0]), "v"(vo[1]), "v"(vo[2]), "v"(vo[3]), "s"(s_) : "memory");
    W_M0(1, 1);
    asm volatile("global_load_lds_dwordx4 %0, %1" :: "v"(vo[4]), "s"(s_) : "memory");
#define G_FIN(j, i)                                                                                       \
  do {                                                                                                    \
    const f32x4 tt = acc[j] + og[i] + f32x4{bf_lo(gv[i][0]), bf_hi(gv[i][0]), bf_lo(gv[i][1]), bf_hi(gv[i][1])}; \
    float t[4];                                                                                           \
    _Pragma("unroll") for (int e = 0; e < 4; ++e)                                                         \
      t[e] = tt[e] * __builtin_amdgcn_rcpf(1.0f + __builtin_amdgcn_exp2f(tt[e] * -1.4426950408889634f));  \
    gv[i] = u32x2{cvtpk(t[0], t[1]), cvtpk(t[2], t[3])};                                                  \
    PIN(gv[i]);                                                                                           \
  } while (0)
    f32x4 og[3];
    if (kh == 0) { og[0] = red[0]; og[1] = red[64]; og[2] = red[128]; G_FIN(0, 0); G_FIN(1, 1); G_FIN(2, 2); }
    else { og[0] = red[192]; og[1] = red[256]; G_FIN(3, 0); G_FIN(4, 1); }
#undef G_FIN
    SBAR();
  }
  {
    const srd_t ro = make_srd(ws + WS_O, (unsigned)(WS_G - WS_O));
    const unsigned oa = (unsigned)(((rbk * 4 + mi) * 20 + kh) * 1024);
    u32x4 af[10];
#pragma unroll
    for (int c = 0; c < 10; ++c) af[c] = bload16_dev(ro, vlin, oa + c * 2048u);
    SBAR();
#define WO_RD(D, c)                                                                                       \
  do {                                                                                                    \
    const unsigned wa_ = (c) < 4 ? wra + (unsigned)((c) * 10240) : (c) < 6 ? wra - (unsigned)KB_WR + (unsigned)(((c) - 4) * 10240) \
                                                                           : wrb + (unsigned)(((c) - 6) * 10240); \
    _Pragma("unroll") for (int t = 0; t < 5; ++t) D[t] = cat8(tr_read(LP(wa_) + t * 32), tr_read(LP(wa_) + t * 32 + 1280)); \
  } while (0)
    bf16x8 wfa[5][5], wfb[5][5];
#pragma unroll
    for (int c = 0; c < 5; ++c) WO_RD(wfa[c], c);
    SBAR();
#pragma unroll
    for (int j = 0; j < 5; ++j) acc[j] = f32x4{0.f, 0.f, 0.f, 0.f};
    STAMP(1, 6);
    for (unsigned spins = 0; spins < (1u << 15); ++spins) {
      unsigned mx = 0u;
#pragma unroll
      for (int c = 0; c < 10; ++c) mx = max(max(max(mx, af[c][0]), af[c][1]), max(af[c][2], af[c][3]));
      if (!__any(mx == 0xffffffffu)) break;
      __builtin_amdgcn_s_sleep(4);
      asm volatile("" ::: "memory");
#pragma unroll
      for (int c = 0; c < 10; ++c) af[c] = bload16_dev(ro, vlin, oa + c * 2048u);
    }
    STAMP(1, 7);
    LBAR(0);
#pragma unroll
    for (int c = 5; c < 8; ++c) WO_RD(wfb[c - 5], c);
    SBAR();
#pragma unroll
    for (int c = 0; c < 5; ++c)
#pragma unroll
      for (int t = 0; t < 5; ++t) acc[t] = MFMA16(wfa[c][t], __builtin_bit_cast(bf16x8, af[c]), acc[t]);
#pragma unroll
    for (int c = 8; c < 10; ++c) WO_RD(wfb[c - 5], c);
#pragma unroll
    for (int c = 5; c < 10; ++c)
#pragma unroll
      for (int t = 0; t < 5; ++t) acc[t] = MFMA16(wfb[c - 5][t], __builtin_bit_cast(bf16x8, af[c]), acc[t]);
#undef WO_RD
  }
  if (kh == 1) { red[0] = acc[0]; red[64] = acc[1]; red[128] = acc[2]; } else { red[192] = acc[3]; red[256] = acc[4]; }
  LBAR(0);
#define O_FIN(j, i)                                                                                       \
  do {                                                                                                    \
    const f32x4 sv = acc[j] + red[(j) * 64];                                                              \
    const float r0 = bf_lo(xv[i][0]) + sv[0] * bf_lo(gv[i][0]), r1 = bf_hi(xv[i][0]) + sv[1] * bf_hi(gv[i][0]); \
    const float r2 = bf_lo(xv[i][1]) + sv[2] * bf_lo(gv[i][1]), r3 = bf_hi(xv[i][1]) + sv[3] * bf_hi(gv[i][1]); \
    *(LDS_AS u32x2*)(stg + (j) * 32) = u32x2{cvtpk(r0, r1), cvtpk(r2, r3)};                               \
  } while (0)
  lds_ptr stg = lds + AT_SLOT + (mi * 16 + (lane & 15)) * 176 + gq * 8;
  if (kh == 0) { O_FIN(0, 0); O_FIN(1, 1); O_FIN(2, 2); } else { O_FIN(3, 0); O_FIN(4, 1); }
#undef O_FIN
  LBAR(0);
  {
    const char* ob = out + (size_t)(rbk * 64) * 1280 + cbk * 160;
#pragma unroll
    for (int i = 0; i < 2; ++i) {
      const int p = i * 512 + tid, row = (p * 6554) >> 16, c16 = p - row * 10;
      if (i == 0 || wid < 2) st16s(ob, (unsigned)(row * 1280 + c16 * 16), *(const LDS_AS u32x4*)(lds + AT_SLOT + row * 176 + c16 * 16));
    }
  }
  STAMP(1, 8);
}

extern "C" void kernel_launch(void* const* d_in, const int* in_sizes, int n_in, void* d_out, int out_size, void* d_ws,
                              size_t ws_size, hipStream_t stream) {
  (void)in_sizes; (void)n_in; (void)out_size; (void)ws_size;
  const char* x = (const char*)d_in[0];
  const char* wqkv = (const char*)d_in[1];
  const char* wo = (const char*)d_in[2];
  const char* w1 = (const char*)d_in[4];
  const unsigned short* b1 = (const unsigned short*)d_in[5];
  const char* w2 = (const char*)d_in[6];
  const unsigned short* b2 = (const unsigned short*)d_in[7];
  char* ws = (char*)d_ws;
  (void)hipFuncSetAttribute((const void*)ka_proj, hipFuncAttributeMaxDynamicSharedMemorySize, KA_LDS);
  (void)hipFuncSetAttribute((const void*)kb_main, hipFuncAttributeMaxDynamicSharedMemorySize, KB_LDS);
  ka_proj<<<256, 512, KA_LDS, stream>>>(x, wqkv, w1, b1, w2, wo, ws);
  kb_main<<<256, 512, KB_LDS, stream>>>(x, w2, b2, wo, ws, (char*)d_out);
}
```

```cpp
#include <hip/hip_runtime.h>
#include <hip/hip_bf16.h>
#include <stdint.h>

typedef __attribute__((ext_vector_type(8))) short bf16x8;
typedef __attribute__((ext_vector_type(4))) short bf16x4;
typedef __attribute__((ext_vector_type(16))) float f32x16;
typedef __attribute__((ext_vector_type(4))) float f32x4;
typedef __attribute__((ext_vector_type(2))) float f32x2;
typedef __attribute__((ext_vector_type(4))) unsigned u32x4;
typedef __attribute__((ext_vector_type(2))) unsigned u32x2;
typedef __attribute__((ext_vector_type(2))) __bf16 bf2;
typedef __attribute__((address_space(3))) char* lds_ptr;
#define LDS_AS __attribute__((address_space(3)))
#define DI __device__ __forceinline__

#define MFMA32(a, b, c) __builtin_amdgcn_mfma_f32_32x32x16_bf16((a), (b), (c), 0, 0, 0)
#define MFMA32K8(a, b, c) __builtin_amdgcn_mfma_f32_32x32x8bf16_1k((a), (b), (c), 0, 0, 0)
#define MFMA16(a, b, c) __builtin_amdgcn_mfma_f32_16x16x32_bf16((a), (b), (c), 0, 0, 0)
#define SBAR() __builtin_amdgcn_sched_barrier(0)
#define PIN(x) asm volatile("" : "+v"(x))
#define WAIT_BAR(N) asm volatile("s_waitcnt vmcnt(" #N ") lgkmcnt(0)\n\ts_barrier" ::: "memory")

constexpr int D_ = 640, NH = 32;
constexpr float LN_EPS = 1e-5f;
constexpr float QSCALE = 0.22360679774997896f * 1.4426950408889634f;

constexpr size_t WS_Q = 0;
constexpr size_t WS_K = WS_Q + (size_t)NH * 64 * 1280;
constexpr size_t WS_V = WS_K + (size_t)NH * 64 * 1280;
constexpr size_t WS_H = WS_V + (size_t)NH * 64 * 1280;
constexpr size_t WS_O = WS_H + (size_t)128 * 48 * 1024;
constexpr size_t WS_G = WS_O + (size_t)128 * 20 * 1024;

#define STAMP(kern, i) do { } while (0)
#define STAMPC(kern, i) do { } while (0)
#define STAMP2(kern, i) do { } while (0)

DI void st16s(const void* sb, unsigned off, u32x4 v) { asm volatile("global_store_dwordx4 %0, %1, %2 sc1\n\ts_nop 1" :: "v"(off), "v"(v), "s"(sb) : "memory"); }
DI void st8s(const void* sb, unsigned off, u32x2 v) { asm volatile("global_store_dwordx2 %0, %1, %2 sc1" :: "v"(off), "v"(v), "s"(sb) : "memory"); }
DI unsigned cvtpk(float lo, float hi) {
  f32x2 f = {lo, hi};
  return __builtin_bit_cast(unsigned, __builtin_convertvector(f, bf2));
}
DI float bf_lo(unsigned u) { return __uint_as_float(u << 16); }
DI float bf_hi(unsigned u) { return __uint_as_float(u & 0xffff0000u); }
typedef short v4i16_t __attribute__((ext_vector_type(4)));
DI bf16x4 tr_read(lds_ptr p) {
  return __builtin_bit_cast(bf16x4, __builtin_amdgcn_ds_read_tr16_b64_v4i16((LDS_AS v4i16_t*)p));
}
DI bf16x8 cat8(bf16x4 a, bf16x4 b) { return __builtin_shufflevector(a, b, 0, 1, 2, 3, 4, 5, 6, 7); }

constexpr int KA_BUF = 45056;
constexpr int KA_STAT = 3 * KA_BUF;
constexpr int KA_SCS = KA_STAT + 2048, KA_SB1 = KA_SCS + 3584, KA_SMR = KA_SB1 + 384;
constexpr int KA_LDS = KA_SMR + 2048;
#define LBAR(N) asm volatile("s_waitcnt lgkmcnt(" #N ")\n\ts_barrier" ::: "memory")
#define LP(a) ((lds_ptr)(uintptr_t)(a))
#define SET_M0(v) asm volatile("s_mov_b32 m0, %0\n\ts_nop 0" :: "s"(v) : "memory")
#define NOP_ do { } while (0)
#define MFMA4(a, b, c) __builtin_amdgcn_mfma_f32_4x4x4bf16_1k((a), (b), (c), 0, 0, 0)

typedef __amdgpu_buffer_rsrc_t srd_t;
DI srd_t make_srd(const void* p, unsigned bytes) { return __builtin_amdgcn_make_buffer_rsrc((void*)p, (short)0, (int)bytes, 0x00020000); }
DI float x32_add(float v) {
  const auto rr = __builtin_amdgcn_permlane32_swap(__float_as_uint(v), __float_as_uint(v), false, false);
  return __uint_as_float(rr[0]) + __uint_as_float(rr[1]);
}
DI bf16x4 lo4(bf16x8 v) { return __builtin_shufflevector(v, v, 0, 1, 2, 3); }
DI bf16x4 hi4(bf16x8 v) { return __builtin_shufflevector(v, v, 4, 5, 6, 7); }

template <int TYPE, int nh>
DI void ka_body(lds_ptr lds, const char* __restrict__ x, const char* __restrict__ wqkv, const char* __restrict__ wh1,
                const unsigned short* __restrict__ b1, const char* __restrict__ w2, const char* __restrict__ wo,
                const int n0, const int f0, const int rb, const int cb, char* __restrict__ ws) {
  constexpr int NHT = nh ? 1 : 2;
  const int tid = threadIdx.x, lane = tid & 63;
  const int w = __builtin_amdgcn_readfirstlane(tid >> 6);
  const int mi = w & 3;
  const int r = lane & 31, h = lane >> 5;
  const unsigned lds0 = (unsigned)(uintptr_t)lds;
  STAMP(0, 0);
  const unsigned vx0 = (unsigned)((lane >> 3) * 1280 + (((lane & 7) ^ (lane >> 4)) << 4));
  const unsigned vx1 = (unsigned)((8 + (lane >> 3)) * 1280 + (((lane & 7) ^ (4 + (lane >> 4))) << 4) - 1024);
  const unsigned vw = (unsigned)((lane >> 3) * 3840 + ((((((lane >> 2) ^ (lane >> 4)) & 1) << 2) | (lane & 3)) << 4));
  const int p0 = w * 96 + lane, p1 = w * 96 + 64 + r;
  const int r0 = (p0 * 5462) >> 16, r1 = (p1 * 5462) >> 16;
  const unsigned vh0 = (unsigned)(r0 * 3072 + (p0 - r0 * 12) * 16), vh1 = (unsigned)(r1 * 3072 + (p1 - r1 * 12) * 16 - 1024);
  const char* xs = x + (size_t)(rb * 128 + 16 * w) * 1280;
  const char* wsrc = wqkv + ((size_t)(8 * w) * 1920 + n0) * 2;
  const char* hsrc = wh1 + f0 * 2;
  const unsigned mx = lds0 + (unsigned)w * 2048u, mw = lds0 + 16384u + (unsigned)w * 1024u, mh = lds0 + 32768u + (unsigned)w * 1536u;
#define KA_DX(c, buf) do { SET_M0(mx + (unsigned)(buf)); asm volatile("global_load_lds_dwordx4 %0, %2\n\tglobal_load_lds_dwordx4 %1, %2 offset:1024" :: "v"(vx0), "v"(vx1), "s"(xs + (c) * 128) : "memory"); } while (0)
#define KA_DW(c, buf, hf) do { SET_M0(mw + (unsigned)(buf) + (hf) * 8192u); asm volatile("global_load_lds_dwordx4 %0, %1" :: "v"(vw), "s"(wsrc + (size_t)(c) * (64 * 3840) + (hf) * 128) : "memory"); } while (0)
#define KA_DH(c, buf) do { SET_M0(mh + (unsigned)(buf)); asm volatile("global_load_lds_dwordx4 %0, %2\n\ts_mov_b32 exec_hi, 0\n\tglobal_load_lds_dwordx4 %1, %2 offset:1024\n\ts_mov_b32 exec_hi, -1" :: "v"(vh0), "v"(vh1), "s"(hsrc + (size_t)(c) * (64 * 3072)) : "memory"); } while (0)
#define KA_ISSUE(c, buf) do { KA_DX(c, buf); KA_DW(c, buf, 0); KA_DW(c, buf, 1); KA_DH(c, buf); } while (0)
  unsigned braw = 0, warm = 0;
  if (nh == 0 && tid < 96) braw = b1[f0 + tid];
  SBAR();
  KA_ISSUE(0, 0);
  KA_ISSUE(1, KA_BUF);

  unsigned xa[4], wa0[4], ha[4];
  {
    const unsigned wb = lds0 + (unsigned)(16384 + nh * 8192 + (8 * h + ((lane & 15) >> 2)) * 128 + 64 * ((lane >> 3) & 1) +
                                          (16 * ((lane >> 4) & 1) + 4 * (lane & 3)) * 2);
    const unsigned hb = lds0 + (unsigned)(32768 + (8 * h + ((lane & 15) >> 2)) * 192 + nh * 128 + (16 * ((lane >> 4) & 1) + 4 * (lane & 3)) * 2);
#pragma unroll
    for (int i = 0; i < 4; ++i) {
      const int ks = i ^ mi;
      xa[i] = lds0 + (unsigned)((mi * 32 + r) * 128 + (((2 * ks + h) ^ ((r >> 1) & 7)) << 4));
      wa0[i] = wb + (unsigned)(ks * 2048);
      ha[i] = hb + (unsigned)(ks * 3072);
    }
  }
  const int tq = rb * 4 + mi;
  const bool isq = n0 < 640;
  unsigned so[TYPE == 1 ? 2 : 4], split = 0u;
  if (TYPE == 1) {
#pragma unroll
    for (int j = 0; j < 2; ++j) {
      const int nn = n0 - 1280 + nh * 64 + j * 32 + r;
      const int head = (nn * 3277) >> 16;
      so[j] = (unsigned)(head * (64 * 1280) + (h * 20 + nn - head * 20) * 16);
      PIN(so[j]);
    }
  } else {
#pragma unroll
    for (int j = 0; j < 4; ++j) {
      const int G = (((isq ? n0 : n0 - 640) + nh * 64) >> 2) + 4 * j + 2 * h;
      const int head = (G * 205) >> 10;
      const int dg = G - head * 5;
      so[j] = (unsigned)(head * (64 * 1280) + (dg < 4 ? (dg >> 1) * 512 + r * 16 : 1024 + r * 8));
      if (dg == 4) split |= 1u << j;
      PIN(so[j]);
    }
    PIN(split);
  }
  f32x16 acc[2], ach[NHT];
  f32x4 cq[2], chh[NHT], rsum = {0.f, 0.f, 0.f, 0.f}, rsq = rsum;
#pragma unroll
  for (int i = 0; i < 16; ++i) {
    acc[0][i] = 0.f; acc[1][i] = 0.f;
#pragma unroll
    for (int j = 0; j < NHT; ++j) ach[j][i] = 0.f;
  }
  cq[0] = rsum; cq[1] = rsum;
#pragma unroll
  for (int j = 0; j < NHT; ++j) chh[j] = rsum;
  bf16x8 sel;
  {
    const short sv = (lane & 15) == ((lane >> 4) & 1) ? (short)0x3f80 : (short)0;
    sel = bf16x8{sv, sv, sv, sv, sv, sv, sv, sv};
    PIN(sel);
  }
  bf16x8 xf[3], w0[3], w1[3], wh[3][NHT];
  unsigned wa1[4];
#pragma unroll
  for (int i = 0; i < 4; ++i) wa1[i] = wa0[i] ^ 64u;
#define KA_READ(B, i)                                                                                           \
  do {                                                                                                          \
    constexpr int s_ = ((B) + (i)) % 3;                                                                         \
    xf[s_] = *(const LDS_AS bf16x8*)(LP(xa[i]) + BUFOFF(B));                                                    \
    w0[s_] = cat8(tr_read(LP(wa0[i]) + BUFOFF(B)), tr_read(LP(wa0[i]) + BUFOFF(B) + 512));                      \
    w1[s_] = cat8(tr_read(LP(wa1[i]) + BUFOFF(B)), tr_read(LP(wa1[i]) + BUFOFF(B) + 512));                      \
    _Pragma("unroll") for (int j_ = 0; j_ < NHT; ++j_)                                                          \
      wh[s_][j_] = cat8(tr_read(LP(ha[i]) + BUFOFF(B) + j_ * 64), tr_read(LP(ha[i]) + BUFOFF(B) + j_ * 64 + 768)); \
  } while (0)
#define SGB(m, n) __builtin_amdgcn_sched_group_barrier((m), (n), 0)
#define KA_STEP(B, i, RD) KA_STEPX(B, i, RD, true)
#define KA_STEPX(B, i, RD, ROWS)                                                                                \
  do {                                                                                                          \
    constexpr int b_ = ((B) + (i)) % 3;                                                                         \
    if constexpr (NHT == 2) asm volatile("" : "+v"(xf[b_]), "+v"(w0[b_]), "+v"(w1[b_]), "+v"(wh[b_][0]), "+v"(wh[b_][NHT - 1])); \
    else asm volatile("" : "+v"(xf[b_]), "+v"(w0[b_]), "+v"(w1[b_]), "+v"(wh[b_][0]));                          \
    SBAR();                                                                                                     \
    RD;                                                                                                         \
    if (TYPE == 1) { acc[0] = MFMA32(xf[b_], w0[b_], acc[0]); acc[1] = MFMA32(xf[b_], w1[b_], acc[1]); }        \
    else { acc[0] = MFMA32(w0[b_], xf[b_], acc[0]); acc[1] = MFMA32(w1[b_], xf[b_], acc[1]); }                  \
    _Pragma("unroll") for (int j_ = 0; j_ < NHT; ++j_) ach[j_] = MFMA32(wh[b_][j_], xf[b_], ach[j_]);           \
    SGB(0x8, 1); SGB(0x100, 3); SGB(0x8, 1); SGB(0x100, 2); SGB(0x8, 1); SGB(0x100, 2);                         \
    if (NHT == 2) { SGB(0x8, 1); SGB(0x100, 2); }                                                               \
    if constexpr (NHT == 2) asm volatile("" : "+v"(acc[0]), "+v"(acc[1]), "+v"(ach[0]), "+v"(ach[NHT - 1]));    \
    else asm volatile("" : "+v"(acc[0]), "+v"(acc[1]), "+v"(ach[0]));                                           \
    SBAR();                                                                                                     \
    if ((i) == 0) {                                                                              \
      cq[0] = MFMA16(w0[b_], sel, cq[0]); cq[1] = MFMA16(w1[b_], sel, cq[1]);                                   \
      _Pragma("unroll") for (int j_ = 0; j_ < NHT; ++j_) chh[j_] = MFMA16(wh[b_][j_], sel, chh[j_]);            \
      if constexpr (NHT == 2) asm volatile("" : "+v"(cq[0]), "+v"(cq[1]), "+v"(chh[0]), "+v"(chh[NHT - 1]));    \
      else asm volatile("" : "+v"(cq[0]), "+v"(cq[1]), "+v"(chh[0]));                                           \
    }                                                                                                           \
    if ((ROWS) && (nh ? (i) < 2 : (i) >= 2)) {                                                   \
      rsq = MFMA4(lo4(xf[b_]), lo4(xf[b_]), rsq); rsum = MFMA16(xf[b_], sel, rsum);                             \
      rsq2 = MFMA4(hi4(xf[b_]), hi4(xf[b_]), rsq2);                                                             \
      asm volatile("" : "+v"(rsum), "+v"(rsq), "+v"(rsq2));                                                     \
    }                                                                                                           \
    SBAR();                                                                                                     \
  } while (0)
#define BUFOFF(B) ((B) * KA_BUF)
#define KA_CHUNK(c, B, BN, WAITN, PREV, MORE)                                                                   \
  do {                                                                                                          \
    KA_STEP(B, 0, KA_READ(B, 2));                                                                               \
    if ((PREV)) { KA_DH((c) + 2, BUFOFF(((B) + 2) % 3)); SBAR(); }                               \
    KA_STEP(B, 1, KA_READ(B, 3));                                                                               \
    WAIT_BAR(WAITN);                                                                                            \
    KA_STEP(B, 2, KA_READ(BN, 0));                                                                              \
    if ((MORE)) { KA_DX((c) + 3, BUFOFF(B)); SBAR(); }                                           \
    KA_STEP(B, 3, KA_READ(BN, 1));                                                                              \
    if ((MORE)) { KA_DW((c) + 3, BUFOFF(B), 0); KA_DW((c) + 3, BUFOFF(B), 1); SBAR(); }          \
  } while (0)
  f32x4 rsq2 = rsum;
  WAIT_BAR(6);
  STAMP(0, 1);
  STAMPC(0, 14);
  KA_READ(0, 0);
  KA_READ(0, 1);
  KA_STEP(0, 0, KA_READ(0, 2));
  KA_DX(2, BUFOFF(2)); KA_DW(2, BUFOFF(2), 0); KA_DW(2, BUFOFF(2), 1); SBAR();
  KA_STEP(0, 1, KA_READ(0, 3));
  KA_DH(2, BUFOFF(2)); SBAR();
  WAIT_BAR(6);
  KA_STEP(0, 2, KA_READ(1, 0));
  KA_DX(3, BUFOFF(0)); SBAR();
  KA_STEP(0, 3, KA_READ(1, 1));
  KA_DW(3, BUFOFF(0), 0); KA_DW(3, BUFOFF(0), 1); SBAR();
#pragma unroll 1
  for (int c = 1; c < 7; c += 3) {
    KA_CHUNK(c, 1, 2, 6, true, true);
    KA_CHUNK(c + 1, 2, 0, 6, true, true);
    KA_CHUNK(c + 2, 0, 1, 6, true, true);
  }
  KA_CHUNK(7, 1, 2, 6, true, false);
  KA_CHUNK(8, 2, 0, 0, false, false);
  KA_STEP(0, 0, KA_READ(0, 2));
  KA_STEP(0, 1, KA_READ(0, 3));
  if (nh == 0) {
    asm volatile("" : "+v"(xf[2]), "+v"(xf[0]));
    rsq = MFMA4(lo4(xf[2]), lo4(xf[2]), rsq); rsum = MFMA16(xf[2], sel, rsum); rsq2 = MFMA4(hi4(xf[2]), hi4(xf[2]), rsq2);
    rsum = MFMA16(xf[0], sel, rsum); rsq = MFMA4(lo4(xf[0]), lo4(xf[0]), rsq); rsq2 = MFMA4(hi4(xf[0]), hi4(xf[0]), rsq2);
  }
  asm volatile("s_nop 7" : "+v"(rsum), "+v"(rsq), "+v"(rsq2));
  rsq += rsq2;
  STAMP(0, 3);
  STAMPC(0, 15);
  LDS_AS float* st_r = (LDS_AS float*)(lds + KA_STAT);
  LDS_AS float* st_cs = (LDS_AS float*)(lds + KA_SCS);
  LDS_AS float* st_b = (LDS_AS float*)(lds + KA_SB1);
  {
    const int d = lane & 3;
    const float sq = x32_add(d == 0 ? rsq[0] : d == 1 ? rsq[1] : d == 2 ? rsq[2] : rsq[3]);
    if (h == 0) st_r[256 + nh * 128 + mi * 32 + r] = sq;
    if ((lane & 15) < 2) {
      const int q4 = (lane & 1) * 16 + (lane >> 4) * 4;
      *(LDS_AS f32x4*)(st_r + nh * 128 + mi * 32 + q4) = rsum;
      *(LDS_AS f32x4*)(st_cs + mi * 224 + nh * 64 + q4) = cq[0];
      *(LDS_AS f32x4*)(st_cs + mi * 224 + nh * 64 + 32 + q4) = cq[1];
#pragma unroll
      for (int j = 0; j < NHT; ++j) *(LDS_AS f32x4*)(st_cs + mi * 224 + 128 + nh * 64 + j * 32 + q4) = chh[j];
    }
    if (nh == 0 && tid < 96) st_b[tid] = __uint_as_float(braw << 16);
  }
  SBAR();
  KA_STEPX(0, 2, NOP_, false);
  LBAR(0);
  float cpart = 0.f;
  if (tid < 224) cpart = (st_cs[tid] + st_cs[224 + tid]) + (st_cs[448 + tid] + st_cs[672 + tid]);
  const float sx_ = st_r[mi * 32 + r] + st_r[128 + mi * 32 + r], sq_ = st_r[256 + mi * 32 + r] + st_r[384 + mi * 32 + r];
  SBAR();
  KA_STEPX(0, 3, NOP_, false);
#undef KA_READ
#undef KA_STEP
#undef KA_STEPX
#undef KA_CHUNK
#undef KA_ISSUE
  if (tid < 224) st_cs[tid] = cpart;
  {
    const char* ob = ws + WS_O + (size_t)blockIdx.x * 10240;
    const u32x4 ff = {0xffffffffu, 0xffffffffu, 0xffffffffu, 0xffffffffu};
    st16s(ob, (unsigned)tid * 16u, ff);
    if (w < 2) st16s(ob + 8192, (unsigned)tid * 16u, ff);
  }
  if (nh == 1) {
    const int l0_ = blockIdx.x * 85 + (tid - 256 < 85 ? tid - 256 : 0);
    const char* p = l0_ < 15360 ? w2 + (size_t)l0_ * 128 : wo + (size_t)(l0_ - 15360) * 128;
    if (w < 6) asm volatile("global_load_dword %0, %1, off" : "=v"(warm) : "v"(p) : "memory");
  }
  const float mu = sx_ * (1.0f / D_);
  const float var = fmaxf(sq_ * (1.0f / D_) - mu * mu, 0.f);
  const float rs = __builtin_amdgcn_rsqf(var + LN_EPS);
  LDS_AS f32x2* st_w = (LDS_AS f32x2*)(lds + KA_SMR) + w * 32;
  if (TYPE == 1 && h == 0) st_w[r] = f32x2{mu * rs, rs};
  LBAR(0);
  STAMP(0, 5);
  if (cb < 15) {
    if (TYPE == 1) {
      char* dst = ws + WS_V + (size_t)tq * 1280;
      const float cs0 = st_cs[nh * 64 + r], cs1 = st_cs[nh * 64 + 32 + r];
#pragma unroll
      for (int s = 0; s < 2; ++s) {
        f32x2 mr[8];
#pragma unroll
        for (int e = 0; e < 8; ++e) mr[e] = st_w[16 * s + 8 * (e >> 2) + 4 * h + (e & 3)];
        u32x4 o0, o1;
#pragma unroll
        for (int e = 0; e < 4; ++e) {
          const int i = 8 * s + 2 * e;
          o0[e] = cvtpk(acc[0][i] * mr[2 * e][1] - mr[2 * e][0] * cs0, acc[0][i + 1] * mr[2 * e + 1][1] - mr[2 * e + 1][0] * cs0);
          o1[e] = cvtpk(acc[1][i] * mr[2 * e][1] - mr[2 * e][0] * cs1, acc[1][i + 1] * mr[2 * e + 1][1] - mr[2 * e + 1][0] * cs1);
        }
        st16s(dst + s * 640, so[0], o0);
        st16s(dst + s * 640, so[1], o1);
      }
    } else {
      const float sc = isq ? rs * QSCALE : rs, ms = mu * sc;
      char* base = ws + (isq ? WS_Q : WS_K) + (size_t)tq * 1280;
#pragma unroll
      for (int j = 0; j < 2; ++j)
#pragma unroll
        for (int u = 0; u < 2; ++u) {
          u32x2 o[2];
#pragma unroll
          for (int gg = 0; gg < 2; ++gg) {
            const int g = 2 * u + gg;
            const f32x4 cs = *(const LDS_AS f32x4*)(st_cs + nh * 64 + j * 32 + 8 * g + 4 * h);
            o[gg] = u32x2{cvtpk(acc[j][4 * g] * sc - ms * cs[0], acc[j][4 * g + 1] * sc - ms * cs[1]),
                          cvtpk(acc[j][4 * g + 2] * sc - ms * cs[2], acc[j][4 * g + 3] * sc - ms * cs[3])};
          }
          const auto a = __builtin_amdgcn_permlane32_swap(o[0][0], o[1][0], false, false);
          const auto b = __builtin_amdgcn_permlane32_swap(o[0][1], o[1][1], false, false);
          const unsigned off = so[j * 2 + u];
          if ((split >> (j * 2 + u)) & 1u) {
            st8s(base, off, u32x2{a[0], b[0]});
            st8s(base + 64 * 1280, off, u32x2{a[1], b[1]});
          } else {
            st16s(base, off, u32x4{a[0], b[0], a[1], b[1]});
          }
        }
    }
  }
  {
    const float sc = rs * __builtin_amdgcn_rsqf(var * rs * rs + LN_EPS), ms = mu * sc;
    constexpr float GK1 = -2.0f * 1.4426950408889634f * 0.7978845608028654f, GK2 = GK1 * 0.044715f;
    const char* dst = ws + WS_H + ((size_t)(rb * 8 + mi * 2) * 48 + (f0 >> 5) + nh * 2) * 1024;
    const unsigned ho = (unsigned)((r >> 4) * (48 * 1024) + (h * 16 + (r & 15)) * 16);
    const f32x2 sc2 = {sc, sc}, ms2 = {ms, ms}, k1 = {GK1, GK1}, k2 = {GK2, GK2}, one2 = {1.0f, 1.0f};
#pragma unroll
    for (int j = 0; j < NHT; ++j)
#pragma unroll
      for (int u = 0; u < 2; ++u) {
        u32x4 o;
#pragma unroll
        for (int gg = 0; gg < 2; ++gg) {
          const int g = 2 * u + gg;
          const f32x4 cs = *(const LDS_AS f32x4*)(st_cs + 128 + nh * 64 + j * 32 + 8 * g + 4 * h);
          const f32x4 bb = *(const LDS_AS f32x4*)(st_b + nh * 64 + j * 32 + 8 * g + 4 * h);
#pragma unroll
          for (int e = 0; e < 2; ++e) {
            const f32x2 a2 = {ach[j][4 * g + 2 * e], ach[j][4 * g + 2 * e + 1]};
            const f32x2 c2 = {cs[2 * e], cs[2 * e + 1]}, b2_ = {bb[2 * e], bb[2 * e + 1]};
            const f32x2 t = a2 * sc2 + (b2_ - ms2 * c2);
            const f32x2 arg = t * ((t * t) * k2 + k1);
            const f32x2 den = f32x2{__builtin_amdgcn_exp2f(arg[0]), __builtin_amdgcn_exp2f(arg[1])} + one2;
            const f32x2 v = t * f32x2{__builtin_amdgcn_rcpf(den[0]), __builtin_amdgcn_rcpf(den[1])};
            o[2 * gg + e] = cvtpk(v[0], v[1]);
          }
        }
        st16s(dst + j * 1024 + u * 512, ho, o);
      }
  }
  STAMP(0, 4);
  asm volatile("s_waitcnt vmcnt(0)" : "+v"(warm) :: "memory");
}

__global__ __launch_bounds__(512, 2) void ka_proj(const char* __restrict__ x, const char* __restrict__ wqkv,
                                                  const char* __restrict__ w1, const unsigned short* __restrict__ b1,
                                                  const char* __restrict__ w2, const char* __restrict__ wo,
                                                  char* __restrict__ ws) {
  extern __shared__ __attribute__((aligned(16))) char smem[];
  lds_ptr lds = (lds_ptr)smem;
  const int bid = blockIdx.x, xg = bid & 7, idx = bid >> 3;
  const int rb = (xg & 1) * 8 + (idx & 7), cb = (xg >> 1) * 4 + (idx >> 3);
  const int n0 = (cb < 15 ? cb : 14) * 128, f0 = cb * 96;
  const int nh = __builtin_amdgcn_readfirstlane(threadIdx.x >> 8);
  if (cb < 10) {
    if (nh) ka_body<0, 1>(lds, x, wqkv, w1, b1, w2, wo, n0, f0, rb, cb, ws);
    else ka_body<0, 0>(lds, x, wqkv, w1, b1, w2, wo, n0, f0, rb, cb, ws);
  } else {
    if (nh) ka_body<1, 1>(lds, x, wqkv, w1, b1, w2, wo, n0, f0, rb, cb, ws);
    else ka_body<1, 0>(lds, x, wqkv, w1, b1, w2, wo, n0, f0, rb, cb, ws);
  }
}

DI u32x4 bload16_dev(srd_t r, unsigned voff, unsigned soff) { return __builtin_amdgcn_raw_buffer_load_b128(r, (int)voff, (int)soff, 16); }

constexpr int AT_TILE = 3584;
constexpr int AT_SLOT = 28672;
constexpr int KB_WR = 2 * AT_SLOT;
constexpr int KB_GRP = 40960;
constexpr int KB_RED = KB_WR + 2 * KB_GRP;
constexpr int KB_LDS = KB_RED + 20480;

__global__ __launch_bounds__(512, 2) void kb_main(const char* __restrict__ x, const char* __restrict__ w2,
                                                  const unsigned short* __restrict__ b2, const char* __restrict__ wo,
                                                  char* __restrict__ ws, char* __restrict__ out) {
  extern __shared__ __attribute__((aligned(16))) char smem[];
  lds_ptr lds = (lds_ptr)smem;
  const int tid = threadIdx.x, lane = tid & 63;
  const int wid = __builtin_amdgcn_readfirstlane(tid >> 6);
  const int r = lane & 31, h = lane >> 5;
  const int xg = blockIdx.x & 7, idx = blockIdx.x >> 3;
  const int head = xg * 4 + (idx >> 3), qb = idx & 7;
  const int rbk = xg * 4 + (idx & 3), cbk = idx >> 2;
  const int mi = wid & 3, kh = wid >> 2, gq = lane >> 4;
  const unsigned lds0 = (unsigned)(uintptr_t)lds;
  const char* Kh = ws + WS_K + (size_t)head * (64 * 1280);
  const char* Vh = ws + WS_V + (size_t)head * (64 * 1280);
  const char* Qt = ws + WS_Q + (size_t)(head * 64 + qb * 8 + wid) * 1280;
  STAMP(1, 0);

  const unsigned vlin = (unsigned)lane * 16u;
  const unsigned vv = (unsigned)(h * 20 + r) * 16u;
  const unsigned vv2 = vv + (unsigned)(WS_V - WS_K) - 1280u - 1536u, vv3 = vv2 + 640u - 1024u;
  const unsigned long long mk16 = 0xffffull, mk20 = 0x000fffff000fffffull;
#define KV_M0(c) SET_M0(lds0 + (unsigned)(((c) & 1) * AT_SLOT + wid * AT_TILE))
#define KV_SRC(c) (Kh + (size_t)((c) * 8 + wid + 1) * 1280)
#define KV_P0(c) asm volatile("global_load_lds_dwordx4 %0, %1" :: "v"(vlin), "s"(KV_SRC(c)) : "memory")
#define KV_P1(c) asm volatile("s_mov_b64 exec, %2\n\tglobal_load_lds_dwordx4 %0, %1 offset:1024\n\ts_mov_b64 exec, -1" :: "v"(vlin), "s"(KV_SRC(c)), "s"(mk16) : "memory")
#define KV_P23(c) asm volatile("s_mov_b64 exec, %3\n\tglobal_load_lds_dwordx4 %0, %2 offset:1536\n\tglobal_load_lds_dwordx4 %1, %2 offset:2560\n\ts_mov_b64 exec, -1" :: "v"(vv2), "v"(vv3), "s"(KV_SRC(c)), "s"(mk20) : "memory")
  bf16x8 qa, ka0;
  u32x2 qbv, kb0;
  {
    const unsigned v8 = 1024u + (unsigned)r * 8u;
    asm volatile("global_load_dwordx4 %0, %4, %6\n\tglobal_load_dwordx2 %1, %5, %6\n\t"
                 "global_load_dwordx4 %2, %4, %7\n\tglobal_load_dwordx2 %3, %5, %7"
                 : "=&v"(qa), "=&v"(qbv), "=&v"(ka0), "=&v"(kb0) : "v"(vlin), "v"(v8), "s"(Qt), "s"(Kh) : "memory");
  }
  KV_M0(0); KV_P0(0); KV_P1(0); KV_P23(0);
#pragma unroll
  for (int sl = 0; sl < 2; ++sl) {
    lds_ptr tp = lds + sl * AT_SLOT + wid * AT_TILE;
    if (lane < 32) *(LDS_AS u32x2*)(tp + 1024 + 256 + lane * 8) = u32x2{0x00003f80u, 0u};
    if (lane < 48) {
      const int blk4 = (lane * 43) >> 9, dd = 20 + lane - blk4 * 12;
      const unsigned val = dd == 20 ? 0x3f803f80u : 0u;
      *(LDS_AS u32x4*)(tp + 1536 + blk4 * 512 + dd * 16) = u32x4{val, val, val, val};
    }
  }
  unsigned vo[5];
#pragma unroll
  for (int i = 0; i < 5; ++i) {
    const int p = i * 64 + lane, row = (p * 6554) >> 16;
    vo[i] = (unsigned)(row * 1280 + (p - row * 10) * 16 - (i & 3) * 1024);
  }
#define W_SRC(W, grp) ((W) + cbk * 160 + (size_t)((grp) * 256 + wid * 32) * 1280)
#define W_M0(reg, hi) SET_M0(lds0 + (unsigned)(KB_WR + (reg) * KB_GRP + wid * 5120 + (hi) * 4096))
#define W_P(W, i, grp) asm volatile("global_load_lds_dwordx4 %0, %1 offset:%2" :: "v"(vo[i]), "s"(W_SRC(W, grp)), "i"(((i) & 3) * 1024) : "memory")
  const char* hsrc = ws + WS_H + ((size_t)(rbk * 4 + mi) * 48 + kh) * 1024 + 4096;
#define H_LOAD(D, j, grp) asm volatile("global_load_dwordx4 %0, %1, %2 offset:%3" : "=v"(D[j]) : "v"(vlin), "s"(hsrc + (grp) * 8192), "i"((j) * 2048 - 4096) : "memory")
  const unsigned wra = lds0 + (unsigned)(KB_WR + (kh * 32 + 4 * ((gq & 1) + 4 * (gq >> 1)) + ((lane & 15) >> 2)) * 160 + (lane & 3) * 8);
  const unsigned wrb = wra + KB_GRP;

  unsigned oo[3];
#pragma unroll
  for (int g = 0; g < 3; ++g) {
    const int Gc = head * 5 + 2 * g + h, G8 = Gc & 7;
    oo[g] = (unsigned)((r >> 4) * (20 * 1024) + (r & 15) * 16 + (Gc >> 3) * 1024 + ((G8 >> 2) * 2 + (G8 & 1)) * 256 + ((G8 >> 1) & 1) * 8);
    PIN(oo[g]);
  }
  bf16x8 hA[4], hB[4];
  asm volatile("s_waitcnt vmcnt(4)" : "+v"(qa), "+v"(qbv), "+v"(ka0), "+v"(kb0) :: "memory");
  f32x16 o;
#pragma unroll
  for (int i = 0; i < 16; ++i) o[i] = 0.f;
  const f32x16 zero = o;
  if (h == 1) { qbv[0] = 0u; qbv[1] = 0u; kb0[0] = 0x00003f80u; kb0[1] = 0u; }
  const f32x16 s16 = MFMA32(ka0, qa, zero);
  f32x16 sA = MFMA32K8(__builtin_bit_cast(bf16x4, kb0), __builtin_bit_cast(bf16x4, qbv), s16);
  {
    float m = fmaxf(fmaxf(sA[0], sA[1]), sA[2]);
#pragma unroll
    for (int i = 3; i < 15; i += 2) m = fmaxf(fmaxf(m, sA[i]), sA[i + 1]);
    m = fmaxf(m, sA[15]);
    const auto mm = __builtin_amdgcn_permlane32_swap(__float_as_uint(m), __float_as_uint(m), false, false);
    m = fmaxf(__uint_as_float(mm[0]), __uint_as_float(mm[1]));
    if (h == 1) qbv[0] = cvtpk(-m, 0.f);
  }
  const bf16x4 qb4 = __builtin_bit_cast(bf16x4, qbv);
  sA = MFMA32K8(__builtin_bit_cast(bf16x4, kb0), qb4, s16);
  f32x16 sB;
  f32x4 acc[5];
#pragma unroll
  for (int j = 0; j < 5; ++j) acc[j] = f32x4{0.f, 0.f, 0.f, 0.f};
  WAIT_BAR(0);
  STAMP(1, 1);
  STAMPC(1, 14);
  STAMP2(2, 0);

  unsigned a16 = lds0 + vlin, a8 = lds0 + (unsigned)lane * 8u;
  int sdelta = AT_SLOT;
  bf16x8 kaA, v0A, v1A, kaB, v0B, v1B;
  bf16x4 kbA, kbB;
  bf16x4 wl[5], wu[5];
  u32x4 p0, p1 = {0u, 0u, 0u, 0u};
  v1B = __builtin_bit_cast(bf16x8, p1);
#define RD_KA(X, j) ka##X = *(const LDS_AS bf16x8*)(LP(a16) + (j) * AT_TILE)
#define RD_KB(X, j) kb##X = *(const LDS_AS bf16x4*)(LP(a8) + (j) * AT_TILE + 1024)
#define RD_V0(X, j) v0##X = *(const LDS_AS bf16x8*)(LP(a16) + (j) * AT_TILE + 1536)
#define RD_V1(X, j) v1##X = *(const LDS_AS bf16x8*)(LP(a16) + (j) * AT_TILE + 2560)
#define W_LO(WA, j, t) wl[t] = tr_read(LP(WA) + (j) * 10240 + (t) * 32)
#define W_HI(WA, j, t) wu[t] = tr_read(LP(WA) + (j) * 10240 + (t) * 32 + 1280)
#define G_MM(HS, j, t) acc[t] = MFMA16(cat8(wl[t], wu[t]), HS[j], acc[t])
#define EX2(S, i) do { S[i] = __builtin_amdgcn_exp2f(S[i]); S[i + 1] = __builtin_amdgcn_exp2f(S[i + 1]); } while (0)
#define MID() do { __builtin_amdgcn_sched_group_barrier(0x400, 2, 0); __builtin_amdgcn_sched_group_barrier(0x008, 1, 0); \
                    __builtin_amdgcn_sched_group_barrier(0x400, 2, 0); __builtin_amdgcn_sched_group_barrier(0x002, 2, 0); } while (0)
#define AT_STEP(SC, SN, X, Y, JN, PRE, WP, A0, A1, A2, A3, A4, A5, V0_, V1_, V2_, M1, M2, M3)            \
  do {                                                                                                   \
    PRE;                                                                                                 \
    asm volatile("" : "+v"(ka##X), "+v"(kb##X), "+v"(v0##X), "+v"(v1##X)); WP; SBAR();                   \
    EX2(SC, 0); PIN(SC); SBAR();                                                                         \
    o = MFMA32(v1##Y, __builtin_bit_cast(bf16x8, p1), o); SBAR();                                        \
    RD_KA(Y, JN); RD_KB(Y, JN); RD_V0(Y, JN); RD_V1(Y, JN); A0; A1; A2; A3; A4; A5; V0_; V1_; SBAR(); \
    M1; EX2(SC, 2); EX2(SC, 4); p0[0] = cvtpk(SC[0], SC[1]); p0[1] = cvtpk(SC[2], SC[3]); MID();         \
    asm volatile("" : "+v"(p0), "+v"(SC), "+v"(acc[0]), "+v"(acc[3]), "+v"(o)); SBAR();                  \
    SN = MFMA32(ka##X, qa, zero); SBAR();                                                                \
    M2; EX2(SC, 6); EX2(SC, 8); p0[2] = cvtpk(SC[4], SC[5]); p0[3] = cvtpk(SC[6], SC[7]); MID();         \
    asm volatile("" : "+v"(p0), "+v"(SC), "+v"(acc[1])); SBAR();                                         \
    SN = MFMA32K8(kb##X, qb4, SN); SBAR();                                                               \
    V2_; SBAR();                                                                                         \
    M3; EX2(SC, 10); EX2(SC, 12); p1[0] = cvtpk(SC[8], SC[9]); p1[1] = cvtpk(SC[10], SC[11]); MID();     \
    asm volatile("" : "+v"(p1), "+v"(SC), "+v"(acc[2]), "+v"(acc[4])); SBAR();                           \
    o = MFMA32(v0##X, __builtin_bit_cast(bf16x8, p0), o); SBAR();                                        \
    EX2(SC, 14); p1[2] = cvtpk(SC[12], SC[13]); p1[3] = cvtpk(SC[14], SC[15]);                           \
    asm volatile("" : "+v"(p1), "+v"(o)); SBAR();                                                        \
  } while (0)
#define WP_E asm volatile("" : "+v"(wl[0]), "+v"(wu[0]), "+v"(wl[1]), "+v"(wu[1]), "+v"(wl[2]), "+v"(wu[2]))
#define WP_O asm volatile("" : "+v"(wl[3]), "+v"(wu[3]), "+v"(wl[4]), "+v"(wu[4]))
#define FLIP() do { a16 += (unsigned)sdelta; a8 += (unsigned)sdelta; sdelta = -sdelta; } while (0)
#define SYNC_H(HN) asm volatile("s_waitcnt vmcnt(3) lgkmcnt(0)\n\ts_barrier" : "+v"(HN[0]) :: "memory")
#define WAIT_H(HC, j, N) asm volatile("s_waitcnt vmcnt(" #N ")" : "+v"(HC[j]) :: "memory")
#define STEP_E(JN, PRE, WC, HC, j, VA, VB)                                                               \
  AT_STEP(sA, sB, A, B, JN, PRE, WP_E, W_LO(WC, j, 3), W_HI(WC, j, 3), W_LO(WC, j, 4), W_HI(WC, j, 4), NOP_, NOP_, NOP_, VA, VB, \
          G_MM(HC, j, 0), G_MM(HC, j, 1), G_MM(HC, j, 2))
#define STEP_O(JN, PRE, WA, jn, HC, j, VA, VB)                                                           \
  AT_STEP(sB, sA, B, A, JN, PRE, WP_O, W_LO(WA, jn, 0), W_HI(WA, jn, 0), W_LO(WA, jn, 1), W_HI(WA, jn, 1), W_LO(WA, jn, 2), \
          W_HI(WA, jn, 2), NOP_, VA, VB, G_MM(HC, j, 3), NOP_, G_MM(HC, j, 4))
#define CHUNK_G(c, WC, HC, WN, HN, RN, NW, NG, HG)                                                       \
  do {                                                                                                   \
    STEP_E(1, NOP_, WC, HC, 0, W_M0(RN, 0); W_P(NW, 0, NG), W_P(NW, 1, NG));                             \
    STEP_O(2, NOP_, WC, 1, HC, 0, W_P(NW, 2, NG), W_P(NW, 3, NG));                                       \
    STEP_E(3, WAIT_H(HC, 1, 6), WC, HC, 1, W_M0(RN, 1); W_P(NW, 4, NG), KV_M0((c) + 1); KV_P0((c) + 1)); \
    STEP_O(4, NOP_, WC, 2, HC, 1, KV_P1((c) + 1), KV_P23((c) + 1));                                      \
    STEP_E(5, WAIT_H(HC, 2, 10), WC, HC, 2, H_LOAD(HN, 0, HG), H_LOAD(HN, 1, HG));                       \
    STEP_O(6, NOP_, WC, 3, HC, 2, H_LOAD(HN, 2, HG), H_LOAD(HN, 3, HG));                                 \
    STEP_E(7, WAIT_H(HC, 3, 13), WC, HC, 3, NOP_, NOP_);                                                 \
    STEP_O(0, SYNC_H(HN); STAMP2(2, 1 + (c)); FLIP(), WN, 0, HC, 3, NOP_, NOP_);                         \
  } while (0)
#define PLAIN2_E(JN, VA, VB) AT_STEP(sA, sB, A, B, JN, NOP_, NOP_, NOP_, NOP_, NOP_, NOP_, NOP_, NOP_, NOP_, VA, VB, NOP_, NOP_, NOP_)
#define PLAIN2_O(JN, VA, VB) AT_STEP(sB, sA, B, A, JN, NOP_, NOP_, NOP_, NOP_, NOP_, NOP_, NOP_, NOP_, NOP_, VA, VB, NOP_, NOP_, NOP_)
#define PLAIN_E(JN, X0, X1, X2, X3) AT_STEP(sA, sB, A, B, JN, NOP_, NOP_, X0, X1, X2, X3, NOP_, NOP_, NOP_, NOP_, NOP_, NOP_, NOP_, NOP_)
#define PLAIN_O(JN, X0, X1, X2, X3) AT_STEP(sB, sA, B, A, JN, NOP_, NOP_, X0, X1, X2, X3, NOP_, NOP_, NOP_, NOP_, NOP_, NOP_, NOP_, NOP_)

  RD_KA(A, 0); RD_KB(A, 0); RD_V0(A, 0); RD_V1(A, 0);
  PLAIN2_E(1, W_M0(0, 0); W_P(w2, 0, 0), W_P(w2, 1, 0));
  PLAIN2_O(2, W_P(w2, 2, 0), W_P(w2, 3, 0));
  PLAIN2_E(3, W_M0(0, 1); W_P(w2, 4, 0), KV_M0(1); KV_P0(1));
  PLAIN2_O(4, KV_P1(1), KV_P23(1));
  PLAIN2_E(5, H_LOAD(hA, 0, 0), H_LOAD(hA, 1, 0));
  PLAIN2_O(6, H_LOAD(hA, 2, 0), H_LOAD(hA, 3, 0));
  PLAIN_E(7, NOP_, NOP_, NOP_, NOP_);
  AT_STEP(sB, sA, B, A, 0, SYNC_H(hA); STAMP2(2, 1); FLIP(), NOP_, W_LO(wra, 0, 0), W_HI(wra, 0, 0), W_LO(wra, 0, 1), W_HI(wra, 0, 1),
          W_LO(wra, 0, 2), W_HI(wra, 0, 2), NOP_, NOP_, NOP_, NOP_, NOP_, NOP_);
#pragma unroll 1
  for (int c = 1; c < 5; c += 2) {
    CHUNK_G(c, wra, hA, wrb, hB, 1, w2, c, c);
    CHUNK_G(c + 1, wrb, hB, wra, hA, 0, w2, c + 1, c + 1);
  }
  CHUNK_G(5, wra, hA, wrb, hB, 1, w2, 5, 5);
  const int tok = rbk * 64 + mi * 16 + (lane & 15);
  const unsigned xo = (unsigned)(tok * 1280 + (cbk * 80 + kh * 48 + 4 * gq) * 2);
  const int j2 = kh ? 1 : 2;
  LDS_AS f32x4* red = (LDS_AS f32x4*)(lds + KB_RED) + (mi * 5) * 64 + lane;
  u32x2 gv[3], xv[3];
  PLAIN2_E(1, KV_M0(7); KV_P0(7), KV_P1(7));
  PLAIN2_O(2, KV_P23(7), W_M0(0, 0); W_P(wo, 0, 0));
  PLAIN2_E(3, W_P(wo, 1, 0), W_P(wo, 2, 0));
  PLAIN2_O(4, W_P(wo, 3, 0), W_M0(0, 1); W_P(wo, 4, 0));
  PLAIN2_E(5, NOP_, NOP_);
  PLAIN2_O(6, NOP_, NOP_);
  PLAIN2_E(7, NOP_, NOP_);
  AT_STEP(sB, sA, B, A, 0,
          asm volatile("s_waitcnt vmcnt(0) lgkmcnt(0)\n\ts_barrier" : "+v"(hB[1]), "+v"(hB[2]), "+v"(hB[3]) :: "memory"); STAMP2(2, 7); FLIP(),
          NOP_, NOP_, NOP_, NOP_, NOP_, NOP_, NOP_, NOP_, NOP_, NOP_, NOP_, NOP_, NOP_);
  PLAIN2_E(1, _Pragma("unroll") for (int j = 0; j < 3; ++j) gv[j] = *(const u32x2*)(b2 + cbk * 80 + kh * 48 + (j < 2 ? j : j2) * 16 + 4 * gq), NOP_);
  PLAIN2_O(2, _Pragma("unroll") for (int j = 0; j < 3; ++j) xv[j] = *(const u32x2*)(x + xo + (j < 2 ? j : j2) * 32), NOP_);
  PLAIN2_E(3, NOP_, NOP_);
  PLAIN2_O(4, NOP_, NOP_);
  PLAIN2_E(5, _Pragma("unroll") for (int j = 0; j < 3; ++j) { PIN(gv[j]); PIN(xv[j]); }
           SET_M0(lds0 + (unsigned)(wid * 2560));
           asm volatile("global_load_lds_dwordx4 %0, %3\n\tglobal_load_lds_dwordx4 %1, %3 offset:1024\n\t"
                        "s_mov_b32 exec_hi, 0\n\tglobal_load_lds_dwordx4 %2, %3 offset:2048\n\ts_mov_b32 exec_hi, -1"
                        :: "v"(vo[0]), "v"(vo[1]), "v"(vo[2]), "s"(wo + cbk * 160 + (size_t)(256 + wid * 16) * 1280) : "memory"), NOP_);
  PLAIN2_O(6, NOP_, NOP_);
  PLAIN2_E(7, NOP_, NOP_);
  EX2(sB, 0); PIN(sB); SBAR();
  o = MFMA32(v1A, __builtin_bit_cast(bf16x8, p1), o); SBAR();
  EX2(sB, 2); EX2(sB, 4); EX2(sB, 6);
  p0 = u32x4{cvtpk(sB[0], sB[1]), cvtpk(sB[2], sB[3]), cvtpk(sB[4], sB[5]), cvtpk(sB[6], sB[7])};
  EX2(sB, 8); asm volatile("" : "+v"(p0), "+v"(sB)); SBAR();
  o = MFMA32(v0B, __builtin_bit_cast(bf16x8, p0), o); SBAR();
  EX2(sB, 10); EX2(sB, 12); EX2(sB, 14);
  p1 = u32x4{cvtpk(sB[8], sB[9]), cvtpk(sB[10], sB[11]), cvtpk(sB[12], sB[13]), cvtpk(sB[14], sB[15])};
  o = MFMA32(v1B, __builtin_bit_cast(bf16x8, p1), o);

  STAMP(1, 3);
  STAMPC(1, 15);
  STAMP2(2, 8);
  float l = __uint_as_float(__builtin_amdgcn_permlane32_swap(__float_as_uint(o[8]), __float_as_uint(o[8]), false, false)[1]);
  float big = fmaxf(fmaxf(__builtin_fabsf(o[0]), __builtin_fabsf(o[1])), l);
#pragma unroll
  for (int i = 2; i < 12; i += 2) big = fmaxf(fmaxf(big, __builtin_fabsf(o[i])), __builtin_fabsf(o[i + 1]));
  bool unsafe = !(big < 1.0e38f) || !(l > 0.25f);
#pragma unroll
  for (int i = 0; i < 12; ++i) unsafe |= o[i] != o[i];
  if (__builtin_expect(__any(unsafe), 0)) {
    if (h == 1) { qbv[0] = 0u; }
    const bf16x4 qz = __builtin_bit_cast(bf16x4, qbv);
    float m = -3.0e38f;
#pragma unroll
    for (int i = 0; i < 16; ++i) o[i] = 0.f;
    const unsigned onev = r == 20 ? 0x3f803f80u : 0u;
#pragma unroll 1
    for (int t = 0; t < 64; ++t) {
      const bf16x8 ka = *(const bf16x8*)(Kh + t * 1280 + lane * 16);
      u32x2 kb = *(const u32x2*)(Kh + t * 1280 + 1024 + r * 8);
      u32x4 v0 = {onev, onev, onev, onev}, v1 = v0;
      if (r < 20) {
        v0 = *(const u32x4*)(Vh + t * 1280 + vv);
        v1 = *(const u32x4*)(Vh + t * 1280 + 640 + vv);
      }
      f32x16 s = MFMA32(ka, qa, zero);
      s = MFMA32K8(__builtin_bit_cast(bf16x4, kb), qz, s);
      float tm = s[0];
#pragma unroll
      for (int i = 1; i < 16; ++i) tm = fmaxf(tm, s[i]);
      tm = fmaxf(tm, __shfl_xor(tm, 32));
      const float mn = fmaxf(m, tm);
      const float al = __builtin_amdgcn_exp2f(m - mn);
      m = mn;
      u32x4 q0, q1;
#pragma unroll
      for (int i = 0; i < 16; ++i) { o[i] *= al; s[i] = __builtin_amdgcn_exp2f(s[i] - mn); }
#pragma unroll
      for (int e = 0; e < 4; ++e) { q0[e] = cvtpk(s[2 * e], s[2 * e + 1]); q1[e] = cvtpk(s[8 + 2 * e], s[9 + 2 * e]); }
      o = MFMA32(__builtin_bit_cast(bf16x8, v0), __builtin_bit_cast(bf16x8, q0), o);
      o = MFMA32(__builtin_bit_cast(bf16x8, v1), __builtin_bit_cast(bf16x8, q1), o);
    }
    l = __shfl(o[8], 32 + r);
  }
  {
    const float inv = __builtin_amdgcn_rcpf(l);
    const char* ob = ws + WS_O + (size_t)(qb * 16 + wid * 2) * (20 * 1024);
#pragma unroll
    for (int g = 0; g < 3; ++g)
      if (g < 2 || h == 0)
        st8s(ob, oo[g], u32x2{min(cvtpk(o[4 * g] * inv, o[4 * g + 1] * inv), 0xfffffffeu),
                              min(cvtpk(o[4 * g + 2] * inv, o[4 * g + 3] * inv), 0xfffffffeu)});
  }
  STAMP(1, 4);
  {
    bf16x8 wf[2][5];
#pragma unroll
    for (int t = 0; t < 5; ++t) wf[0][t] = cat8(tr_read(LP(wrb) + t * 32), tr_read(LP(wrb) + t * 32 + 1280));
#pragma unroll
    for (int j = 0; j < 4; ++j) {
      SBAR();
      if (j < 3) {
#pragma unroll
        for (int t = 0; t < 5; ++t)
          wf[(j + 1) & 1][t] = cat8(tr_read(LP(wrb) + (j + 1) * 10240 + t * 32), tr_read(LP(wrb) + (j + 1) * 10240 + t * 32 + 1280));
      }
      SBAR();
#pragma unroll
      for (int t = 0; t < 5; ++t) acc[t] = MFMA16(wf[j & 1][t], hB[j], acc[t]);
      asm volatile("" : "+v"(acc[0]), "+v"(acc[1]), "+v"(acc[2]), "+v"(acc[3]), "+v"(acc[4]));
    }
    asm volatile("s_nop 7" : "+v"(acc[0]), "+v"(acc[1]), "+v"(acc[2]), "+v"(acc[3]), "+v"(acc[4]));
    if (kh == 1) { red[0] = acc[0]; red[64] = acc[1]; red[128] = acc[2]; } else { red[192] = acc[3]; red[256] = acc[4]; }
    asm volatile("s_waitcnt vmcnt(3) lgkmcnt(0)\n\ts_barrier" ::: "memory");
    const char* s_ = wo + cbk * 160 + (size_t)(384 + wid * 32) * 1280;
    W_M0(1, 0);
    asm volatile("global_load_lds_dwordx4 %0, %4\n\tglobal_load_lds_dwordx4 %1, %4 offset:1024\n\t"
                 "global_load_lds_dwordx4 %2, %4 offset:2048\n\tglobal_load_lds_dwordx4 %3, %4 offset:3072"
                 :: "v"(vo[0]), "v"(vo[1]), "v"(vo[2]), "v"(vo[3]), "s"(s_) : "memory");
    W_M0(1, 1);
    asm volatile("global_load_lds_dwordx4 %0, %1" :: "v"(vo[4]), "s"(s_) : "memory");
#define G_FIN(j, i)                                                                                       \
  do {                                                                                                    \
    const f32x4 tt = acc[j] + og[i] + f32x4{bf_lo(gv[i][0]), bf_hi(gv[i][0]), bf_lo(gv[i][1]), bf_hi(gv[i][1])}; \
    float t[4];                                                                                           \
    _Pragma("unroll") for (int e = 0; e < 4; ++e)                                                         \
      t[e] = tt[e] * __builtin_amdgcn_rcpf(1.0f + __builtin_amdgcn_exp2f(tt[e] * -1.4426950408889634f));  \
    gv[i] = u32x2{cvtpk(t[0], t[1]), cvtpk(t[2], t[3])};                                                  \
    PIN(gv[i]);                                                                                           \
  } while (0)
    f32x4 og[3];
    if (kh == 0) { og[0] = red[0]; og[1] = red[64]; og[2] = red[128]; G_FIN(0, 0); G_FIN(1, 1); G_FIN(2, 2); }
    else { og[0] = red[192]; og[1] = red[256]; G_FIN(3, 0); G_FIN(4, 1); }
#undef G_FIN
    SBAR();
  }
  {
    const srd_t ro = make_srd(ws + WS_O, (unsigned)(WS_G - WS_O));
    const unsigned oa = (unsigned)(((rbk * 4 + mi) * 20 + kh) * 1024);
    u32x4 af[10];
#pragma unroll
    for (int c = 0; c < 10; ++c) af[c] = bload16_dev(ro, vlin, oa + c * 2048u);
    SBAR();
#define WO_RD(D, c)                                                                                       \
  do {                                                                                                    \
    const unsigned wa_ = (c) < 4 ? wra + (unsigned)((c) * 10240) : (c) < 6 ? wra - (unsigned)KB_WR + (unsigned)(((c) - 4) * 10240) \
                                                                           : wrb + (unsigned)(((c) - 6) * 10240); \
    _Pragma("unroll") for (int t = 0; t < 5; ++t) D[t] = cat8(tr_read(LP(wa_) + t * 32), tr_read(LP(wa_) + t * 32 + 1280)); \
  } while (0)
    bf16x8 wfa[6][5], wfb[4][5];
#pragma unroll
    for (int c = 0; c < 6; ++c) WO_RD(wfa[c], c);
    SBAR();
#pragma unroll
    for (int j = 0; j < 5; ++j) acc[j] = f32x4{0.f, 0.f, 0.f, 0.f};
    STAMP(1, 6);
    for (unsigned spins = 0; spins < (1u << 15); ++spins) {
      unsigned mx = 0u;
#pragma unroll
      for (int c = 0; c < 10; ++c) mx = max(max(max(mx, af[c][0]), af[c][1]), max(af[c][2], af[c][3]));
      if (!__any(mx == 0xffffffffu)) break;
      __builtin_amdgcn_s_sleep(4);
      asm volatile("" ::: "memory");
#pragma unroll
      for (int c = 0; c < 10; ++c) af[c] = bload16_dev(ro, vlin, oa + c * 2048u);
    }
    STAMP(1, 7);
#pragma unroll
    for (int c = 0; c < 6; ++c)
#pragma unroll
      for (int t = 0; t < 5; ++t) acc[t] = MFMA16(wfa[c][t], __builtin_bit_cast(bf16x8, af[c]), acc[t]);
    asm volatile("" : "+v"(acc[0]), "+v"(acc[1]), "+v"(acc[2]), "+v"(acc[3]), "+v"(acc[4]));
    LBAR(0);
#pragma unroll
    for (int c = 6; c < 10; ++c) WO_RD(wfb[c - 6], c);
#pragma unroll
    for (int c = 6; c < 10; ++c)
#pragma unroll
      for (int t = 0; t < 5; ++t) acc[t] = MFMA16(wfb[c - 6][t], __builtin_bit_cast(bf16x8, af[c]), acc[t]);
#undef WO_RD
  }
  if (kh == 1) { red[0] = acc[0]; red[64] = acc[1]; red[128] = acc[2]; } else { red[192] = acc[3]; red[256] = acc[4]; }
  LBAR(0);
#define O_FIN(j, i)                                                                                       \
  do {                                                                                                    \
    const f32x4 sv = acc[j] + red[(j) * 64];                                                              \
    const float r0 = bf_lo(xv[i][0]) + sv[0] * bf_lo(gv[i][0]), r1 = bf_hi(xv[i][0]) + sv[1] * bf_hi(gv[i][0]); \
    const float r2 = bf_lo(xv[i][1]) + sv[2] * bf_lo(gv[i][1]), r3 = bf_hi(xv[i][1]) + sv[3] * bf_hi(gv[i][1]); \
    *(LDS_AS u32x2*)(stg + (j) * 32) = u32x2{cvtpk(r0, r1), cvtpk(r2, r3)};                               \
  } while (0)
  lds_ptr stg = lds + AT_SLOT + (mi * 16 + (lane & 15)) * 176 + gq * 8;
  if (kh == 0) { O_FIN(0, 0); O_FIN(1, 1); O_FIN(2, 2); } else { O_FIN(3, 0); O_FIN(4, 1); }
#undef O_FIN
  LBAR(0);
  {
    const char* ob = out + (size_t)(rbk * 64) * 1280 + cbk * 160;
#pragma unroll
    for (int i = 0; i < 2; ++i) {
      const int p = i * 512 + tid, row = (p * 6554) >> 16, c16 = p - row * 10;
      if (i == 0 || wid < 2) st16s(ob, (unsigned)(row * 1280 + c16 * 16), *(const LDS_AS u32x4*)(lds + AT_SLOT + row * 176 + c16 * 16));
    }
  }
  STAMP(1, 8);
}

extern "C" void kernel_launch(void* const* d_in, const int* in_sizes, int n_in, void* d_out, int out_size, void* d_ws,
                              size_t ws_size, hipStream_t stream) {
  (void)in_sizes; (void)n_in; (void)out_size; (void)ws_size;
  const char* x = (const char*)d_in[0];
  const char* wqkv = (const char*)d_in[1];
  const char* wo = (const char*)d_in[2];
  const char* w1 = (const char*)d_in[4];
  const unsigned short* b1 = (const unsigned short*)d_in[5];
  const char* w2 = (const char*)d_in[6];
  const unsigned short* b2 = (const unsigned short*)d_in[7];
  char* ws = (char*)d_ws;
  (void)hipFuncSetAttribute((const void*)ka_proj, hipFuncAttributeMaxDynamicSharedMemorySize, KA_LDS);
  (void)hipFuncSetAttribute((const void*)kb_main, hipFuncAttributeMaxDynamicSharedMemorySize, KB_LDS);
  ka_proj<<<256, 512, KA_LDS, stream>>>(x, wqkv, w1, b1, w2, wo, ws);
  kb_main<<<256, 512, KB_LDS, stream>>>(x, w2, b2, wo, ws, (char*)d_out);
}
```
